```python
import torch

def _softmax(x, axis=-1):
    return torch.softmax(x, dim=axis)
def _gelu(x):
    return torch.nn.functional.gelu(x, approximate="tanh")
def _silu(x):
    return torch.nn.functional.silu(x)
def _sigmoid(x):
    return torch.sigmoid(x)
def _layernorm(x, axis=-1, eps=1e-5):
    m = x.mean(dim=axis, keepdim=True)
    v = x.var(dim=axis, keepdim=True, unbiased=False)
    return (x - m) / torch.sqrt(v + eps)
def _scatter_add(tgt, idx, src, axis=0):
    return tgt.clone().index_add_(axis, idx, src)
def _segment_sum(src, seg, n):
    out = torch.zeros((n,) + src.shape[1:], dtype=src.dtype)
    return out.index_add_(0, seg, src)
def _pad(x, pw):
    flat = tuple(v for pair in reversed(pw) for v in pair)
    return torch.nn.functional.pad(x, flat)

kernel_name = 'interleave_layers__linear_attn_block__glu_gated_reglu__prompt'

def setup_inputs():

    _w = (lambda a: (lambda t: t.to(torch.bfloat16) if t.is_floating_point() else t)(torch.as_tensor(a)))
    def _A_setup():
        B, S, D = 1, 2048, 512
        return {
            "x": _w((lambda *s: torch.randn(*s, dtype=torch.bfloat16) * 0.05)(B, S, D)),
            "wqkv": _w((lambda *s: torch.randn(*s, dtype=torch.bfloat16) * 0.05)(D, 3 * D)),
            "wo": _w((lambda *s: torch.randn(*s, dtype=torch.bfloat16) * 0.05)(D, D)),
        }
    _A_in = _A_setup()

    def _B_setup():
        B, S, D, F = 1, 2048, 512, 1376
        return {
            "x": _w((lambda *s: torch.randn(*s, dtype=torch.bfloat16) * 0.05)(B, S, D)),
            "W_gate": _w((lambda *s: torch.randn(*s, dtype=torch.bfloat16) * 0.4)(D, F) * (2.0 / D) ** 0.5),
            "W_up": _w((lambda *s: torch.randn(*s, dtype=torch.bfloat16) * 0.4)(D, F) * (2.0 / D) ** 0.5),
            "W_down": _w((lambda *s: torch.randn(*s, dtype=torch.bfloat16) * 0.4)(F, D) * (1.0 / F) ** 0.5),
        }
    _B_in = _B_setup()

    return {
        "A_x": _A_in["x"],
        "A_wqkv": _A_in["wqkv"],
        "A_wo": _A_in["wo"],
        "B_x": _B_in["x"],
        "B_W_gate": _B_in["W_gate"],
        "B_W_up": _B_in["W_up"],
        "B_W_down": _B_in["W_down"],
    }

def reference(A_x, A_wqkv, A_wo, B_x, B_W_gate, B_W_up, B_W_down):
    def _A_ref(x, wqkv, wo):
        B, S, D = x.shape
        H = 32; Dh = D // H
        qkv = (lambda x, *d: x.permute(*d))((x @ wqkv).reshape(B, S, 3, H, Dh), 2, 0, 3, 1, 4)
        q, k, v = qkv[0], qkv[1], qkv[2]
        phi = lambda t: torch.where(t > 0, t + 1, torch.exp(t)) + 1e-6
        pq, pk = phi(q), phi(k)
        kv = torch.einsum("bhsd,bhse->bhde", pk, v)
        z = torch.einsum("bhsd->bhd", pk)
        num = torch.einsum("bhqd,bhde->bhqe", pq, kv)
        den = torch.einsum("bhqd,bhd->bhq", pq, z)[..., None] + 1e-6
        o = num / den
        return (lambda x, *d: x.permute(*d))(o, 0, 2, 1, 3).reshape(B, S, D) @ wo

    def _B_ref(x, W_gate, W_up, W_down):
        gate = torch.einsum("bsd,df->bsf", x, W_gate)
        up = torch.einsum("bsd,df->bsf", x, W_up)
        gate = gate * (lambda x, dt: x.to(dt))(gate > 0, gate.dtype)
        h = gate * up
        return torch.einsum("bsf,fd->bsd", h, W_down)

    x = A_x
    x = x + _A_ref(_layernorm(x, axis=-1), A_wqkv, A_wo)
    x = x + _B_ref(_layernorm(x, axis=-1), B_W_gate, B_W_up, B_W_down)
    x = x + _A_ref(_layernorm(x, axis=-1), A_wqkv, A_wo)
    x = x + _B_ref(_layernorm(x, axis=-1), B_W_gate, B_W_up, B_W_down)
    return x
```

```cpp
#include <hip/hip_runtime.h>
#include <hip/hip_bf16.h>
typedef unsigned short u16;
__device__ __forceinline__ float bf2f(u16 b){ return __uint_as_float(((unsigned)b)<<16); }
__device__ __forceinline__ u16 f2bf(float f){ __bf16 b=(__bf16)f; return __builtin_bit_cast(u16,b); }
constexpr int S=2048,D=512,F=1376,H=32;
__global__ void ln_k(const u16* x, float* xn){
  int row=blockIdx.x; int t=threadIdx.x;
  float v[8]; float s=0;
  for(int i=0;i<8;i++){ v[i]=bf2f(x[row*D+t+64*i]); s+=v[i]; }
  for(int o=32;o;o>>=1) s+=__shfl_xor(s,o);
  float m=s/D; float q=0;
  for(int i=0;i<8;i++){ float d=v[i]-m; q+=d*d; }
  for(int o=32;o;o>>=1) q+=__shfl_xor(q,o);
  float r=1.0f/sqrtf(q/D+1e-5f);
  for(int i=0;i<8;i++) xn[row*D+t+64*i]=(v[i]-m)*r;
}
__global__ void gemm_k(const float* A,const u16* B,float* C,int M,int N,int K){
  __shared__ float sA[16][17], sB[16][17];
  int tx=threadIdx.x, ty=threadIdx.y; int r=blockIdx.y*16+ty, c=blockIdx.x*16+tx;
  float acc=0;
  for(int k0=0;k0<K;k0+=16){
    sA[ty][tx]=A[r*K+k0+tx]; sB[ty][tx]=bf2f(B[(k0+ty)*N+c]);
    __syncthreads();
    for(int k=0;k<16;k++) acc+=sA[ty][k]*sB[k][tx];
    __syncthreads();
  }
  C[r*N+c]=acc;
}
__device__ __forceinline__ float phi(float t){ return (t>0? t+1.f: __expf(t))+1e-6f; }
__global__ void kv_k(const float* qkv,float* kv){
  int h=blockIdx.x; int t=threadIdx.x; float acc=0;
  if(t<256){ int d=t>>4,e=t&15; for(int s=0;s<S;s++) acc+=phi(qkv[s*1536+512+h*16+d])*qkv[s*1536+1024+h*16+e]; }
  else { int d=t-256; for(int s=0;s<S;s++) acc+=phi(qkv[s*1536+512+h*16+d]); }
  kv[h*272+t]=acc;
}
__global__ void o_k(const float* qkv,const float* kv,float* o){
  int s=blockIdx.x; int t=threadIdx.x; int h=t>>4,e=t&15; float num=0,den=0;
  for(int d=0;d<16;d++){ float p=phi(qkv[s*1536+h*16+d]); num+=p*kv[h*272+d*16+e]; den+=p*kv[h*272+256+d]; }
  o[s*D+t]=num/(den+1e-6f);
}
__global__ void res_k(const u16* x,const float* y,u16* out){ int i=blockIdx.x*256+threadIdx.x; out[i]=f2bf(bf2f(x[i])+y[i]); }
__global__ void h_k(const float* g,const float* u,float* h){ int i=blockIdx.x*256+threadIdx.x; float a=g[i]; h[i]=(a>0?a:0.f)*u[i]; }

extern "C" void kernel_launch(void* const* d_in, const int* in_sizes, int n_in, void* d_out, int out_size, void* d_ws, size_t ws_size, hipStream_t stream){
  const u16* x=(const u16*)d_in[0]; const u16* wqkv=(const u16*)d_in[1]; const u16* wo=(const u16*)d_in[2];
  const u16* wg=(const u16*)d_in[4]; const u16* wu=(const u16*)d_in[5]; const u16* wd=(const u16*)d_in[6];
  float* w=(float*)d_ws;
  float* xn=w; float* qkv=xn+S*D; float* kv=qkv+S*1536; float* o=kv+16384; float* y=o+S*D; float* g=y+S*D; float* u=g+S*F; float* h=u+S*F;
  u16* xa=(u16*)(h+S*F); u16* xb=xa+S*D;
  const u16* cur=x;
  for(int l=0;l<2;l++){
    ln_k<<<S,64,0,stream>>>(cur,xn);
    gemm_k<<<dim3(1536/16,S/16),dim3(16,16),0,stream>>>(xn,wqkv,qkv,S,1536,D);
    kv_k<<<H,272,0,stream>>>(qkv,kv);
    o_k<<<S,512,0,stream>>>(qkv,kv,o);
    gemm_k<<<dim3(D/16,S/16),dim3(16,16),0,stream>>>(o,wo,y,S,D,D);
    res_k<<<S*D/256,256,0,stream>>>(cur,y,xa);
    ln_k<<<S,64,0,stream>>>(xa,xn);
    gemm_k<<<dim3(F/16,S/16),dim3(16,16),0,stream>>>(xn,wg,g,S,F,D);
    gemm_k<<<dim3(F/16,S/16),dim3(16,16),0,stream>>>(xn,wu,u,S,F,D);
    h_k<<<S*F/256,256,0,stream>>>(g,u,h);
    gemm_k<<<dim3(D/16,S/16),dim3(16,16),0,stream>>>(h,wd,y,S,D,F);
    u16* dst = l==1 ? (u16*)d_out : xb;
    res_k<<<S*D/256,256,0,stream>>>(xa,y,dst);
    cur=xb;
  }
}
```

```cpp
#include <hip/hip_runtime.h>
#include <stdint.h>

#define GAS __attribute__((address_space(1)))
#define LAS __attribute__((address_space(3)))
#define DI __device__ __forceinline__
typedef unsigned v4u __attribute__((ext_vector_type(4)));
typedef unsigned v2u __attribute__((ext_vector_type(2)));
typedef float f32x16 __attribute__((ext_vector_type(16)));
typedef float f32x4 __attribute__((ext_vector_type(4)));
typedef float f32x2 __attribute__((ext_vector_type(2)));
typedef __bf16 bf16x8 __attribute__((ext_vector_type(8)));
typedef __bf16 bf16x2 __attribute__((ext_vector_type(2)));
typedef short s16x4 __attribute__((ext_vector_type(4)));

#define MAPMODE 1
#define LDEPTH 8
#define LPRIO 3
#define LSLEEP 2
#define AW 8
#define PKV 4
#define AH 8

#define ROT(m) (((m) + 2 * p) & 7)
namespace {
constexpr int KB = 1024;
constexpr int WS_EPOCH = 0;
constexpr int WS_FKV = 4096;
constexpr int WS_FO = WS_FKV + 32 * KB;
constexpr int WS_FX1 = WS_FO + 32 * KB;
constexpr int WS_FH = WS_FX1 + 32 * KB;
constexpr int WS_FX2 = WS_FH + 32 * KB;
constexpr int WS_ST1 = WS_FX2 + 16 * KB;
constexpr int WS_ST2 = WS_ST1 + 512 * KB;
constexpr int KVW = 640;
constexpr int WS_KVP = WS_ST2 + 256 * KB;
constexpr int WS_O = WS_KVP + 2 * 16 * 16 * 4 * KVW;
constexpr int WS_X1 = WS_O + 4096 * KB;
constexpr int WS_X2 = WS_X1 + 4096 * KB;
constexpr int WS_H = WS_X2 + 2048 * KB;
constexpr int WS_RQ = WS_H + 2 * 64 * 86 * KB;
constexpr int WS_RG = WS_RQ + 1536 * KB;
constexpr int WS_RU = WS_RG + 43 * 32 * KB;
constexpr int WS_DBG = WS_RU + 43 * 32 * KB;
constexpr int WS_END = WS_DBG + 256 * 8 * 64 * 8;
constexpr int NSLOT = 8;
constexpr int L_RING = 0;
constexpr int L_SCR = NSLOT * 16 * KB;
constexpr int L_KVS = L_SCR + 16 * KB;
constexpr int L_CSF = L_KVS + 8 * KB;
constexpr int L_SA = L_CSF + 5120;
constexpr int L_KVR = L_SA + 512;
constexpr int L_ZR = L_KVR + 1024;
constexpr int L_ZS = L_ZR + 64;
constexpr int L_DUMMY = L_ZS + 512;
constexpr int L_RDY = L_DUMMY + 448;
constexpr int L_CSC = L_RDY + 32;
constexpr int L_FCNT = L_CSC + 32;
constexpr int L_F1 = L_FCNT + 16;
constexpr int L_F2 = L_F1 + 16;
constexpr int L_F3 = L_F2 + 16;
constexpr int L_END = L_F3 + 16;
static_assert(L_END <= 160 * KB, "LDS");
constexpr int LDS_BYTES = (L_END + 15) / 16 * 16;
constexpr unsigned BUDGET = 1u << 18;

struct Params {
  const char* x; const char* wqkv; const char* wo; const char* wg; const char* wu; const char* wd;
  char* out; char* ws;
};

extern __shared__ __attribute__((aligned(16))) char smem[];
template <class T> DI LAS T* lp(unsigned off) { return (LAS T*)((LAS char*)smem + off); }
template <class T> DI T vld(unsigned off) { return *(volatile LAS T*)((LAS char*)smem + off); }
template <class T> DI void vst(unsigned off, T v) { *(volatile LAS T*)((LAS char*)smem + off) = v; }
DI unsigned rfl(unsigned v) { return __builtin_amdgcn_readfirstlane(v); }
#define SCHED() __builtin_amdgcn_sched_barrier(0)
#define CFENCE() asm volatile("" ::: "memory")
#define DRAIN() asm volatile("s_waitcnt vmcnt(0)" ::: "memory")
#define PIN(x) asm volatile("" : "+v"(x))

#define STAMP(i) do {} while (0)
#define STAMPV(i, v) do {} while (0)

DI bf16x8 ldw(unsigned a, int second) {
  s16x4 lo = __builtin_amdgcn_ds_read_tr16_b64_v4i16(lp<s16x4>(a));
  s16x4 hi = __builtin_amdgcn_ds_read_tr16_b64_v4i16(lp<s16x4>(a + second));
  return __builtin_bit_cast(bf16x8, __builtin_shufflevector(lo, hi, 0, 1, 2, 3, 4, 5, 6, 7));
}
DI f32x16 mfma(bf16x8 a, bf16x8 b, f32x16 c) { return __builtin_amdgcn_mfma_f32_32x32x16_bf16(a, b, c, 0, 0, 0); }
DI bf16x8 asb(v4u v) { return __builtin_bit_cast(bf16x8, v); }
DI unsigned pk2(float lo, float hi) { bf16x2 r = {(__bf16)lo, (__bf16)hi}; return __builtin_bit_cast(unsigned, r); }
DI unsigned pk2(f32x2 v) { return pk2(v.x, v.y); }
DI float dot2(unsigned a, unsigned b, float c) { return __builtin_amdgcn_fdot2_f32_bf16(__builtin_bit_cast(bf16x2, a), __builtin_bit_cast(bf16x2, b), c, false); }
DI float u2f(unsigned u) { return __builtin_bit_cast(float, u); }
DI unsigned f2u(float f) { return __builtin_bit_cast(unsigned, f); }
DI float sel(bool c, float a, float b) { asm volatile("" : "+v"(a), "+v"(b)); return c ? a : b; }
DI int opaque(int v) { asm volatile("" : "+v"(v)); return v; }
DI int sopaque(int v) { asm volatile("" : "+s"(v)); return v; }
DI float blo(unsigned u) { return u2f(u << 16); }
DI float bhi(unsigned u) { return u2f(u & 0xffff0000u); }
DI float sum_halves(float v) {
  auto r = __builtin_amdgcn_permlane32_swap(f2u(v), f2u(v), false, false);
  float o; asm("v_add_f32 %0, %1, %2" : "=v"(o) : "v"(r[0]), "v"(r[1])); return o;
}
template <int CTRL> DI float dpp_add(float v) { return v + u2f(__builtin_amdgcn_update_dpp(0u, f2u(v), CTRL, 0xf, 0xf, false)); }
DI float row_sum(float v) { v = dpp_add<0x128>(v); v = dpp_add<0x124>(v); v = dpp_add<0x122>(v); return dpp_add<0x121>(v); }
DI f32x2 pair(const f32x16& a, int j) { return f32x2{a[2 * j], a[2 * j + 1]}; }
DI unsigned phi2(f32x2 x, f32x2 a) {
  const f32x2 e2 = x * (a * 1.4426950408889634f);
  const f32x2 t = x * a + 1e-6f;
  const f32x2 e = {__builtin_amdgcn_fmed3f(__builtin_amdgcn_exp2f(e2.x), 0.f, 1.f), __builtin_amdgcn_fmed3f(__builtin_amdgcn_exp2f(e2.y), 0.f, 1.f)};
  const f32x2 mx = {fmaxf(t.x, 1e-6f), fmaxf(t.y, 1e-6f)};
  return pk2(mx + e);
}
constexpr unsigned ONES = 0x3f803f80u;
DI float fadd1(float a, float b) { float r; asm("v_add_f32 %0, %1, %2" : "=v"(r) : "v"(a), "v"(b)); return r; }
DI float fmul1(float a, float b) { float r; asm("v_mul_f32 %0, %1, %2" : "=v"(r) : "v"(a), "v"(b)); return r; }

typedef __amdgpu_buffer_rsrc_t rsrc_t;
DI v4u ld16(rsrc_t rs, int voff, int soff) { return __builtin_amdgcn_raw_buffer_load_b128(rs, voff, soff, 16); }
DI v2u ld8(rsrc_t rs, int voff, int soff) { return __builtin_amdgcn_raw_buffer_load_b64(rs, voff, soff, 16); }
DI void st16(rsrc_t rs, v4u v, int voff, int soff) {
  __builtin_amdgcn_raw_buffer_store_b128(v, rs, voff, soff, 16);
  asm volatile("s_nop 1" ::"v"(v) : "memory");
}
DI void st8(rsrc_t rs, v2u v, int voff, int soff) { __builtin_amdgcn_raw_buffer_store_b64(v, rs, voff, soff, 16); }

struct CW {
  unsigned s, pos, rdy;
  unsigned lane_tr;
  unsigned fa;
  unsigned tag, perm;
  int budget;
  int lane, w;
  char* ws;
};
DI bool spent(CW& c) { c.budget -= 1; return c.budget < 0; }
DI unsigned min4(v4u v) { return rfl(min(min(v.x, v.y), min(v.z, v.w))); }
DI void wait_count(CW& c, unsigned have, unsigned addr, unsigned need) {
  if (__builtin_expect(rfl(have) < need, 0)) {
    for (;;) { if (rfl(vld<unsigned>(addr)) >= need || spent(c)) break; }
  }
}
DI void peek(CW& c, unsigned ahead) { c.rdy = vld<unsigned>(L_RDY + ((c.pos + ahead) & (NSLOT - 1)) * 4); }
DI void wait_full(CW& c) { wait_count(c, c.rdy, L_RDY + c.pos * 4, (c.s / NSLOT + 1) * 4); }
DI void post(CW& c, int word, unsigned v) { vst<unsigned>(c.fa + (word - L_FCNT), v); }
DI void release(CW& c) { c.s += 1; c.pos = (c.pos + 1) & (NSLOT - 1); post(c, L_FCNT, min(c.s + NSLOT, c.perm)); }
DI void permit(CW& c, unsigned v) { c.perm = v; post(c, L_FCNT, min(c.s + NSLOT, v)); }
DI void set_flag(CW& c, int off) {
  const char* base = c.ws + off;
  asm volatile("s_mov_b64 exec, 1\n\ts_nop 4\n\tglobal_atomic_swap %0, %1, %2\n\ts_mov_b64 exec, -1" ::"v"(0u), "v"(c.tag), "s"(base) : "memory");
}
DI void wait_flags(CW& c, int off, unsigned long long mask) {
  unsigned f, bud = rfl(c.budget);
  const unsigned voff = c.lane * 4;
  const char* base = c.ws + off;
  asm volatile(
      "s_mov_b64 exec, %5\n\ts_nop 4\n\t"
      "1:\n\t"
      "global_atomic_or %0, %2, %6, %3 sc0\n\t"
      "s_waitcnt vmcnt(0)\n\t"
      "v_cmp_ne_u32 vcc, %4, %0\n\t"
      "s_cbranch_vccz 2f\n\t"
      "s_sub_i32 %1, %1, 8\n\t"
      "s_cmp_ge_i32 %1, 0\n\t"
      "s_cbranch_scc1 1b\n\t"
      "2:\n\t"
      "s_mov_b64 exec, -1\n\t"
      : "=&v"(f), "+s"(bud) : "v"(voff), "s"(base), "s"(c.tag), "s"(mask), "v"(0u) : "memory", "vcc", "scc");
  c.budget = rfl(bud);
}
DI unsigned poll_issue(CW& c, int off, unsigned long long mask) {
  unsigned f = c.tag;
  const unsigned voff = c.lane * 4;
  const char* base = c.ws + off;
  asm volatile("s_mov_b64 exec, %3\n\tv_not_b32 %0, %0\n\ts_nop 4\n\tglobal_atomic_or %0, %1, %4, %2 sc0\n\ts_mov_b64 exec, -1"
               : "+v"(f) : "v"(voff), "s"(base), "s"(mask), "v"(0u) : "memory");
  return f;
}
template <int N> DI void poll_done(CW& c, unsigned f, int off, unsigned long long mask) {
  asm volatile("s_waitcnt vmcnt(%c1)" : "+v"(f) : "i"(N) : "memory");
  if (__builtin_expect(__builtin_amdgcn_ballot_w64(f != c.tag) != 0ull, 0)) wait_flags(c, off, mask);
}

template <int V> struct IC { static constexpr int value = V; DI constexpr operator int() const { return V; } };
template <int I, int E, class F> DI void static_for(F&& f) { if constexpr (I < E) { f(IC<I>{}); static_for<I + 1, E>(f); } }
struct Pipe { bf16x8 wa[3], wb[3]; unsigned ad, ad2, adn, need, pn; };
DI void slot_first(CW& c, Pipe& q) { peek(c, 0); wait_full(c); q.adn = c.lane_tr + c.pos * 16384u; }
DI void slot_peek(CW& c, Pipe& q) { q.pn = (c.pos + 1) & (NSLOT - 1); c.rdy = vld<unsigned>(L_RDY + q.pn * 4); }
DI void slot_prep(CW& c, Pipe& q) { q.need = ((c.s + 1) / NSLOT + 1) * 4; q.adn = c.lane_tr + q.pn * 16384u; PIN(q.adn); asm volatile("" : "+s"(q.need)); }
DI void slot_check(CW& c, Pipe& q) { wait_count(c, c.rdy, L_RDY + q.pn * 4, q.need); }
template <bool SWAP, int I0, int I1, int NEND, bool MORE, class HOOK>
DI void pair_steps(CW& c, Pipe& q, v4u (&B)[32], f32x16& acc0, f32x16& acc1, HOOK hook) {
  static_for<I0, I1>([&](auto ic) __attribute__((always_inline)) {
    constexpr int i = ic.value, n = i + 2;
    constexpr bool F = n < NEND, NX = F && (MORE || n + 8 - n % 8 < NEND);
    if constexpr (n == 0) slot_first(c, q);
    if constexpr (i >= 0) acc0 = SWAP ? mfma(asb(B[i % 32]), q.wa[i % 3], acc0) : mfma(q.wa[i % 3], asb(B[i % 32]), acc0);
    SCHED();
    if constexpr (F && n % 8 == 0) q.ad = q.adn;
    if constexpr (F) q.wa[n % 3] = ldw(q.ad + (n % 8) * 1024, 512);
    if constexpr (NX && n % 8 == 4) slot_peek(c, q);
    if constexpr (NX && n % 8 == 6) slot_prep(c, q);
    if constexpr (NX && n % 8 == 7) slot_check(c, q);
    hook(IC<(i < 0 ? i : i % 32)>{}, IC<0>{});
    SCHED();
    if constexpr (i >= 0) acc1 = SWAP ? mfma(asb(B[i % 32]), q.wb[i % 3], acc1) : mfma(q.wb[i % 3], asb(B[i % 32]), acc1);
    SCHED();
    if constexpr (F) q.wb[n % 3] = ldw(q.ad + 8192 + (n % 8) * 1024, 512);
    if constexpr (F && n % 8 == 7) release(c);
    hook(IC<(i < 0 ? i : i % 32)>{}, IC<1>{});
    SCHED();
  });
}
template <bool SWAP, class HOOK>
DI void gemm_pair(CW& c, v4u (&B)[32], f32x16& acc0, f32x16& acc1, HOOK hook) {
  Pipe q;
  pair_steps<SWAP, -2, 32, 32, false>(c, q, B, acc0, acc1, hook);
}
template <class HOOK>
DI void gemm_single(CW& c, v4u (&B)[32], f32x16& acc, HOOK hook) {
  bf16x8 wa[5];
  Pipe q;
  static_for<-4, 32>([&](auto ic) __attribute__((always_inline)) {
    constexpr int i = ic.value, n = i + 4;
    if constexpr (n == 0) slot_first(c, q);
    if constexpr (i >= 0) acc = mfma(wa[i % 5], asb(B[i]), acc);
    SCHED();
    if constexpr (n < 32) {
      if constexpr (n % 16 == 0) q.ad = q.adn;
      wa[n % 5] = ldw(q.ad + (n % 16) * 1024, 512);
      if constexpr (n == 8) slot_peek(c, q);
      if constexpr (n == 12) slot_prep(c, q);
      if constexpr (n == 14) slot_check(c, q);
      if constexpr (n % 16 == 15) release(c);
    }
    hook(ic);
    SCHED();
  });
}
constexpr int PW = 12;
struct Wide { bf16x8 wa[PW + 1], wb[PW + 1]; Pipe q; unsigned dO, lt; };
template <int NK, int I0, int I1, class HOOK>
DI void wide_steps(CW& c, Wide& d, v4u (&B)[32], f32x16& accM, f32x16& accO, int kh, HOOK hook) {
  Pipe& q = d.q;
  const unsigned keep = c.lane_tr;
  if constexpr (I0 == -PW) { d.lt = c.lane_tr + kh * (8192u + 512u); d.dO = kh ? -512u : 512u; }
  c.lane_tr = d.lt;
  static_for<I0, I1>([&](auto ic) __attribute__((always_inline)) {
    constexpr int i = ic.value, n = i + PW;
    constexpr bool F = n < NK, NX = F && n + 4 - n % 4 < NK;
    if constexpr (n == 0) slot_first(c, q);
    if constexpr (i >= 0) accO = mfma(d.wb[i % (PW + 1)], asb(B[i % 16]), accO);
    SCHED();
    if constexpr (F && n % 4 == 0) { q.ad = q.adn; if constexpr (n + 3 == NK) q.ad -= kh * 2048u; q.ad2 = q.ad + d.dO; }
    if constexpr (F) d.wb[n % (PW + 1)] = ldw(q.ad2 + (n % 4) * 2048, 1024);
    if constexpr (NX && n % 4 == 0) slot_peek(c, q);
    if constexpr (NX && n % 4 == 2) slot_prep(c, q);
    if constexpr (NX && n % 4 == 3) slot_check(c, q);
    hook(ic, IC<0>{});
    SCHED();
    if constexpr (i >= 0) accM = mfma(d.wa[i % (PW + 1)], asb(B[i % 16]), accM);
    SCHED();
    if constexpr (F) d.wa[n % (PW + 1)] = ldw(q.ad + (n % 4) * 2048, 1024);
    if constexpr (F && (n % 4 == 3 || n == NK - 1)) release(c);
    hook(ic, IC<1>{});
    SCHED();
  });
  c.lane_tr = keep;
}
struct NoHook { template <class A, class B> DI void operator()(A, B) const {} };

DI void ln_factors(float sum, float sq, float& a, unsigned& mf, bool up) {
  float mean = sum * (1.0f / 512.0f);
  PIN(mean);
  const float var = fmaxf(sq * (1.0f / 512.0f) - mean * mean, 0.f);
  a = __builtin_amdgcn_rsqf(var + 1e-5f);
  const unsigned hi = pk2(-mean, -mean);
  const float lo = -mean - blo(hi);
  mf = up ? pk2(lo, lo) : hi;
}
struct Stats { v4u sv[4]; f32x4 t; f32x2 u[4]; float mean, var; };
constexpr int K0 = 34;
DI void stats_hook(CW& c, Stats& t, int k, rsrc_t rs, int soff, float& a, unsigned& mf) {
  if (k == 3 || k == 7 || k == 11 || k == 15) t.sv[k >> 2] = ld16(rs, c.lane * 16 + (k >> 2) * 1024, soff);
  const unsigned sc = L_SCR + (c.w ^ 2) * 4096;
  if (k >= K0 && k < K0 + 4) { const int e = k - K0; t.t[e] = fadd1(fadd1(u2f(t.sv[0][e]), u2f(t.sv[1][e])), fadd1(u2f(t.sv[2][e]), u2f(t.sv[3][e]))); PIN(t.t[e]); }
  if (k == K0 + 4) vst<f32x4>(sc + c.lane * 16, t.t);
  if (k == K0 + 5) { t.u[0] = vld<f32x2>(sc + (c.lane & 31) * 8); t.u[1] = vld<f32x2>(sc + 256 + (c.lane & 31) * 8); }
  if (k == K0 + 6) { t.u[2] = vld<f32x2>(sc + 512 + (c.lane & 31) * 8); t.u[3] = vld<f32x2>(sc + 768 + (c.lane & 31) * 8); }
  if (k == K0 + 10) { t.mean = fadd1(fadd1(t.u[0].x, t.u[1].x), fadd1(t.u[2].x, t.u[3].x)); PIN(t.mean); }
  if (k == K0 + 11) { t.var = fadd1(fadd1(t.u[0].y, t.u[1].y), fadd1(t.u[2].y, t.u[3].y)); PIN(t.var); }
  if (k == K0 + 12) { t.mean *= -1.0f / 512.0f; PIN(t.mean); t.var = t.var * (1.0f / 512.0f) + 1e-5f; PIN(t.var); }
  if (k == K0 + 13) { t.var = fmaxf(t.var - t.mean * t.mean, 1e-5f); PIN(t.var); a = __builtin_amdgcn_rsqf(t.var); PIN(a); }
  if (k == K0 + 14) { mf = pk2(t.mean, t.mean); t.mean -= blo(mf); PIN(mf); PIN(t.mean); }
  if (k == K0 + 15) { mf = c.lane >= 32 ? pk2(t.mean, t.mean) : mf; PIN(mf); }
}
DI void finish_wide(CW& c, const f32x16& accM, const f32x16& accO, unsigned (&R)[8], unsigned seq) {
  const unsigned mine = L_SCR + c.w * 4096 + c.lane * 16, other = L_SCR + (c.w ^ 2) * 4096 + c.lane * 16;
#pragma unroll
  for (int g = 0; g < 4; ++g) vst<f32x4>(mine + g * 1024, f32x4{accO[4 * g], accO[4 * g + 1], accO[4 * g + 2], accO[4 * g + 3]});
  post(c, L_F3, seq);
  f32x2 y[8];
#pragma unroll
  for (int j = 0; j < 8; ++j) { y[j] = pair(accM, j) + f32x2{blo(R[j]), bhi(R[j])}; PIN(y[j]); }
  f32x4 v[4];
  for (;;) {
    const unsigned fl = vld<unsigned>(L_F3 + (c.w ^ 2) * 4);
#pragma unroll
    for (int g = 0; g < 4; ++g) v[g] = vld<f32x4>(other + g * 1024);
    if (rfl(fl) >= seq || spent(c)) break;
  }
#pragma unroll
  for (int g = 0; g < 4; ++g) {
    R[2 * g] = pk2(y[2 * g] + f32x2{v[g].x, v[g].y});
    R[2 * g + 1] = pk2(y[2 * g + 1] + f32x2{v[g].z, v[g].w});
  }
}
DI void publish_x(CW& c, rsrc_t rs, const unsigned (&R)[8], int xoff, int stoff, int floff) {
  st16(rs, v4u{R[0], R[1], R[2], R[3]}, c.lane * 16, xoff);
  st16(rs, v4u{R[4], R[5], R[6], R[7]}, c.lane * 16 + 1024, xoff);
  float s0 = 0.f, q0 = 0.f, s1 = 0.f, q1 = 0.f;
#pragma unroll
  for (int i = 0; i < 4; ++i) { s0 = dot2(R[i], ONES, s0); q0 = dot2(R[i], R[i], q0); s1 = dot2(R[4 + i], ONES, s1); q1 = dot2(R[4 + i], R[4 + i], q1); }
  const float s = sum_halves(s0 + s1), q = sum_halves(q0 + q1);
  st8(rs, v2u{f2u(s), f2u(q)}, (c.lane & 31) * 8, stoff);
  DRAIN();
  set_flag(c, floff);
}

DI void dma4(const char* base, unsigned v0, unsigned v1, unsigned v2, unsigned v3, unsigned dst) {
  asm volatile("s_mov_b32 m0, %5\n\ts_nop 3\n\t"
               "global_load_lds_dwordx4 %0, %4\n\tglobal_load_lds_dwordx4 %1, %4 offset:1024\n\t"
               "global_load_lds_dwordx4 %2, %4 offset:2048\n\tglobal_load_lds_dwordx4 %3, %4 offset:3072"
               ::"v"(v0), "v"(v1), "v"(v2), "v"(v3), "s"(base), "s"(dst) : "memory");
}
DI void dma4_sc1(const char* base, unsigned v, unsigned dst) {
  asm volatile("s_mov_b32 m0, %2\n\ts_nop 3\n\t"
               "global_load_lds_dwordx4 %0, %1 sc1\n\tglobal_load_lds_dwordx4 %0, %1 offset:1024 sc1\n\t"
               "global_load_lds_dwordx4 %0, %1 offset:2048 sc1\n\tglobal_load_lds_dwordx4 %0, %1 offset:3072 sc1"
               ::"v"(v), "s"(base), "s"(dst) : "memory");
}
template <int N> DI void wait_vm() { asm volatile("s_waitcnt vmcnt(%c0)" ::"i"(N) : "memory"); }
DI unsigned tr_lane(int lane) { return (4 * (lane >> 5) + ((lane & 15) >> 2)) * 64 + ((lane >> 4) & 1) * 32 + (lane & 3) * 8; }
DI int fresh_lane() { int l; asm volatile("v_mbcnt_lo_u32_b32 %0, -1, 0\n\tv_mbcnt_hi_u32_b32 %0, -1, %0" : "=v"(l)); return l; }
DI void loader(const Params& P, int p, int m, int lw) {
#define LSTAMP(v, s) do {} while (0)
  const int lane = fresh_lane();
  const unsigned smem_base = (unsigned)(uintptr_t)(LAS char*)smem;
  const unsigned dst0 = smem_base + L_RING + lw * 4096u;
  const unsigned v_raw = (lane >> 2) * 3072 + (lane & 3) * 16;
  {
    const char* b0 = P.wqkv + ((lw >> 1) + 1) * 1024 + m * 64;
#pragma unroll
    for (int j = 0; j < 4; ++j) { LSTAMP(tiss, j); dma4(b0 + ((2 * j + (lw & 1) + ROT(m)) & 7) * (64 * 3072), v_raw, v_raw + 48128, v_raw + 2 * 48128, v_raw + 3 * 48128, dst0 + j * 16384); LSTAMP(tis2, j); }
  }
  const unsigned lane_tr = tr_lane(lane);
  const unsigned nr = m < 11 ? 3 : 2;
  const unsigned NS = 21 + 4 * nr, NT = 2 * NS;
  const unsigned v_lin = lane * 16, v_wide = ((lane & 31) >> 2) * 1024 + (lane >> 5) * 64 + (lane & 3) * 16;
  const unsigned pa = lane == 0 ? L_RDY : L_DUMMY + lane * 4;
  auto arrive = [&](unsigned off, unsigned n) __attribute__((always_inline)) { CFENCE(); (void)__hip_atomic_fetch_add(lp<unsigned>(pa + off), n, __ATOMIC_RELAXED, __HIP_MEMORY_SCOPE_WORKGROUP); CFENCE(); };
  auto colsum4 = [&](unsigned a, float& c0, float& c1) __attribute__((always_inline)) {
#pragma unroll
    for (int n = 0; n < 4; ++n) {
      const v4u f = __builtin_bit_cast(v4u, ldw(a + n * 1024, 512));
      float t0, t1;
      asm("v_lshlrev_b32 %2, 16, %4\n\tv_and_b32 %3, %8, %4\n\tv_add_f32 %0, %0, %2\n\tv_add_f32 %1, %1, %3\n\t"
          "v_lshlrev_b32 %2, 16, %5\n\tv_and_b32 %3, %8, %5\n\tv_add_f32 %0, %0, %2\n\tv_add_f32 %1, %1, %3\n\t"
          "v_lshlrev_b32 %2, 16, %6\n\tv_and_b32 %3, %8, %6\n\tv_add_f32 %0, %0, %2\n\tv_add_f32 %1, %1, %3\n\t"
          "v_lshlrev_b32 %2, 16, %7\n\tv_and_b32 %3, %8, %7\n\tv_add_f32 %0, %0, %2\n\tv_add_f32 %1, %1, %3"
          : "+v"(c0), "+v"(c1), "=&v"(t0), "=&v"(t1) : "v"(f.x), "v"(f.y), "v"(f.z), "v"(f.w), "s"(0xffff0000u));
    }
  };
  auto put_cs = [&](unsigned blk, unsigned part, float v) __attribute__((always_inline)) {
    const float t = sum_halves(v);
    vst<unsigned>(L_CSF + blk * 512 + (lane & 31) * 16 + part * 4, pk2(t, t - blo(pk2(t, t))));
  };
  unsigned long long tab;
  {
    const unsigned s = 4u + (lane & ~3u) + lw, q = lane & 3;
    const bool l2 = s >= NS;
    const unsigned i = l2 ? s - NS : s;
    const unsigned blk = i < 4u ? q >> 1 : 2u;
    const unsigned ks0 = ((i < 4u ? 8u * i + (q & 1) * 4u : 16u * (i - 4u) + q * 4u) + 4u * ROT(m)) & 31u;
    const unsigned g = i - 10u, d = i - 10u - 4u * nr;
    const unsigned row = min((d < 8u ? (d + ROT(m)) & 7u : d) * 128u + q * 32u, 1344u);
    if (i < 6u && l2) tab = (unsigned long long)P.ws + WS_RQ + ((m * 3 + blk) * 32u + ks0) * 1024u + 1u;
    else if (i < 6u) tab = (unsigned long long)P.wqkv + m * 64 + ks0 * (16u * 3072u);
    else if (i < 10u) tab = (unsigned long long)P.wo + ((i - 6u) * 128u + q * 32u) * 1024u + (m & 7) * 128 + 2u;
    else if (i < 10u + 4u * nr) tab = (unsigned long long)P.ws + ((q >> 1) ? WS_RU : WS_RG) + ((16u * (g >> 2) + m) * 32u + ((8u * (g & 3u) + (q & 1) * 4u + 4u * ROT(m)) & 31u)) * 1024u + 1u;
    else tab = (unsigned long long)P.wd + row * 1024u + (m & 7) * 128 + 2u;
  }
  const unsigned tlo = (unsigned)tab, thi = (unsigned)(tab >> 32);
  {
    float c0 = 0.f, c1 = 0.f;
    wait_vm<12>(); arrive(0, 1); LSTAMP(tlan, 0u); colsum4(lane_tr + lw * 4096u, c0, c1);
    wait_vm<8>(); arrive(4, 1); LSTAMP(tlan, 1u); colsum4(lane_tr + 16384u + lw * 4096u, c0, c1);
    wait_vm<4>(); arrive(8, 1); LSTAMP(tlan, 2u); colsum4(lane_tr + 2 * 16384u + lw * 4096u, c0, c1);
    wait_vm<0>(); arrive(12, 1); LSTAMP(tlan, 3u); colsum4(lane_tr + 3 * 16384u + lw * 4096u, c0, c1);
    put_cs(lw >> 1, lw & 1, c0 + c1);
    arrive(L_CSC - L_RDY, 1);
  }
  auto finish = [&](unsigned s) __attribute__((always_inline)) {
    arrive((s & (NSLOT - 1)) * 4, 4);
    LSTAMP(tlan, s);
    if (s < 6u || (s >= 10u && s < 10u + 4u * nr)) {
      const unsigned a = lane_tr + (s & (NSLOT - 1)) * 16384u;
      __builtin_amdgcn_s_setprio(0);
      float c0 = 0.f, c1 = 0.f, c2 = 0.f, c3 = 0.f;
      colsum4(a, c0, c1); colsum4(a + 4096, c0, c1); colsum4(a + 8192, c2, c3); colsum4(a + 12288, c2, c3);
      const unsigned k = s < 6u ? 1u : 2u + ((s - 10u) >> 2);
      if (s < 6u) put_cs(2, lw, (c0 + c1) + (c2 + c3));
      else { put_cs(2 * k, lw, c0 + c1); put_cs(2 * k + 1, lw, c2 + c3); }
      arrive(L_CSC - L_RDY + k * 4, 1);
      __builtin_amdgcn_s_setprio(LPRIO);
    }
  };
  unsigned limit = 0;
  int budget = BUDGET;
  bool pend = false;
  for (unsigned s = 4 + lw, t = 0; s < NT; s += 4, t += 4) {
    while (s >= limit) {
      limit = min4(vld<v4u>(L_FCNT));
      if (s < limit) break;
      if (pend) { wait_vm<0>(); finish(s - 4); pend = false; continue; }
      __builtin_amdgcn_s_sleep(LSLEEP);
      budget -= 1;
      if (budget < 0) limit = NT;
    }
    LSTAMP(tiss, s);
    const unsigned kind = __builtin_amdgcn_readlane(tlo, t) & 3u;
    const unsigned dst = smem_base + L_RING + (s & (NSLOT - 1)) * 16384u;
    if (kind == 1u) {
#pragma unroll
      for (int q = 0; q < 4; ++q) {
        const unsigned lo = __builtin_amdgcn_readlane(tlo, t + q), hi = __builtin_amdgcn_readlane(thi, t + q);
        dma4_sc1((const char*)(((unsigned long long)hi << 32) | (lo & ~3u)), v_lin, dst + q * 4096);
      }
    } else {
      const unsigned v0 = kind == 2u ? v_wide : v_raw, st = kind == 2u ? 7168u : 48128u;
#pragma unroll
      for (int q = 0; q < 4; ++q) {
        const unsigned lo = __builtin_amdgcn_readlane(tlo, t + q), hi = __builtin_amdgcn_readlane(thi, t + q);
        dma4((const char*)(((unsigned long long)hi << 32) | (lo & ~3u)), v0, v0 + st, v0 + 2 * st, v0 + 3 * st, dst + q * 4096);
      }
    }
    LSTAMP(tis2, s);
    if (pend) { wait_vm<16>(); finish(s - 4); }
    pend = true;
    if (s + 4 >= NT) { wait_vm<0>(); finish(s); }
  }
}
template <int AT, bool TWO> DI void cs_hook(CW& c, int i, unsigned k, v4u& a, v4u& b) {
  if (i == AT) a.x = vld<unsigned>(L_CSC + k * 4);
  if (i == AT + 3) {
    wait_count(c, a.x, L_CSC + k * 4, TWO ? 4u : 2u);
    a = vld<v4u>(L_CSF + k * 1024 + (c.lane & 31) * 16);
    if (TWO) b = vld<v4u>(L_CSF + k * 1024 + 512 + (c.lane & 31) * 16);
  }
}

__global__ void __launch_bounds__(512, 2) fused_fwd(Params P) {
  const int tid = threadIdx.x; int lane = tid & 63; const int wv = __builtin_amdgcn_readfirstlane(tid >> 6);
  const int b = blockIdx.x;
  int p, m;
  if (MAPMODE == 0) { p = (b & 7) * 2 + (b >> 7); m = (b >> 3) & 15; }
  else if (MAPMODE == 1) { const int x = b & 7, i = b >> 3; p = (x >> 1) * 4 + (i >> 3); m = (x & 1) * 8 + (i & 7); }
  else { const int x = b & 7, i = b >> 3; p = i >> 1; m = x * 2 + (i & 1); }
  if (tid < (L_END - L_RDY) / 4) vst<unsigned>(L_RDY + tid * 4, 0u);
  if (tid < 128) vst<v4u>(L_CSF + tid * 16, v4u{0u, 0u, 0u, 0u});
  __syncthreads();
  STAMP(0);
  STAMPV(40, __builtin_amdgcn_s_memtime());
  if (wv >= 4) { __builtin_amdgcn_s_setprio(LPRIO); loader(P, p, m, wv - 4); lane = fresh_lane(); STAMP(1); return; }
  __builtin_amdgcn_s_setprio(2);

  int w = wv;
  unsigned epoch;
  asm volatile("s_load_dword %0, %1, 0x0" : "=s"(epoch) : "s"(P.ws) : "memory");
  v4u B[32];
  const unsigned smem_base = (unsigned)(uintptr_t)(LAS char*)smem;
  unsigned xv[4];
#pragma unroll
  for (int k = 0; k < 4; ++k) xv[k] = (8 * k + (lane >> 3)) * 1024 + (((lane & 7) ^ ((4 * k + (lane >> 4)) & 7)) * 16) - k * 1024;
  const char* xbase = P.x + (4 * p + w) * 32768;
  auto xbuf = [&](int q) -> unsigned { const int j = q % 5; return (j == 0 ? L_SCR : j == 1 ? 7 * 16384 : j == 2 ? 6 * 16384 : j == 3 ? 4 * 16384 : 5 * 16384) + w * 4096; };
  auto xdma = [&](int q) __attribute__((always_inline)) { dma4(xbase + ((q + ROT(m)) & 7) * 128, xv[0], xv[1], xv[2], xv[3], smem_base + xbuf(q)); };
  xdma(0); xdma(1);
  v4u rp[5]; int rdst[5];
  {
    const int ks = 2 * p + (w & 1), hi = w >> 1;
    const unsigned vq = (lane >> 2) * 3072 + (lane & 3) * 16, vg = (lane >> 2) * 2752 + (lane & 3) * 16;
    const char* sq = P.wqkv + m * 64 + ks * (16 * 3072);
    const char* sg = (hi ? P.wu : P.wg) + m * 64 + ks * (16 * 2752);
    const int r2 = m < 11 ? 2 : 1;
    const char* s0 = sq + (hi ? 0 : 1024); const char* s1 = sq + (hi ? 0 : 2048); const char* s4 = sg + r2 * 1024;
    asm volatile("s_nop 4\n\tglobal_load_dwordx4 %0, %5, %7\n\tglobal_load_dwordx4 %1, %5, %8\n\tglobal_load_dwordx4 %2, %6, %9\n\t"
                 "global_load_dwordx4 %3, %6, %9 offset:1024\n\tglobal_load_dwordx4 %4, %6, %10"
                 : "=&v"(rp[0]), "=&v"(rp[1]), "=&v"(rp[2]), "=&v"(rp[3]), "=&v"(rp[4])
                 : "v"(vq), "v"(vg), "s"(s0), "s"(s1), "s"(sg), "s"(s4) : "memory");
    rdst[0] = WS_RQ + ((m * 3 + (hi ? 2 : 0)) * 32 + ks) * 1024; rdst[1] = WS_RQ + ((m * 3 + (hi ? 2 : 1)) * 32 + ks) * 1024;
    rdst[2] = (hi ? WS_RU : WS_RG) + (m * 32 + ks) * 1024; rdst[3] = rdst[2] + 16 * 32768; rdst[4] = rdst[2] + r2 * (16 * 32768);
  }
  xdma(2); xdma(3); xdma(4);
  const rsrc_t rs = __builtin_amdgcn_make_buffer_rsrc((void*)P.ws, 0, WS_END, 0x00020000);
  asm volatile("s_waitcnt lgkmcnt(0)" : "+s"(epoch) :: "memory");
  STAMP(19);
  CW c;
  c.s = 0; c.pos = 0; c.rdy = 0; c.perm = 4; c.lane_tr = tr_lane(lane); c.fa = lane == 0 ? L_FCNT + w * 4 : L_DUMMY + lane * 4;
  c.budget = BUDGET; c.lane = lane; c.w = w; c.ws = P.ws; c.tag = epoch + 1u;
  int h, t32, tbq, tbw, kh, cb;
#define FRESH() do { lane = opaque(lane); h = lane >> 5; t32 = lane & 31; c.lane = lane; p = sopaque(p); m = sopaque(m); w = sopaque(w); c.w = w; \
    tbq = layer * 64 + 4 * p + w; kh = w >> 1; tbw = layer * 64 + 4 * p + 2 * (m >> 3) + (w & 1); cb = 2 * (m & 7) + kh; } while (0)
  unsigned R[8];

  int layer = 0;
    f32x16 aK = {}, aV = {};
    float la; unsigned mf;
    f32x4 ar[4];
    v4u ck, cv;
    FRESH();
    auto rows_ab = [&]() __attribute__((always_inline)) {
      vst<float>(L_SA + w * 128 + t32 * 4, la);
#pragma unroll
      for (int g = 0; g < 4; ++g) ar[g] = vld<f32x4>(L_SA + w * 128 + (8 * g + 4 * h) * 4);
    };
    {
      float s1 = 0.f, s2 = 0.f, s3 = 0.f, s4 = 0.f;
      const unsigned xl = t32 * 128 + 8 * h, xf = (t32 >> 1) & 7;
      auto hook = [&](int i, int hf) __attribute__((always_inline)) {
        const int n = i + 2;
        if (n < 32 && hf == 0) {
          const int q = n / 4, j = n % 4;
          if (j == 0) {
            if (q == 0) wait_vm<21>();
            if (q == 1) wait_vm<17>();
            if (q == 2) {
              asm volatile("s_waitcnt vmcnt(12)" : "+v"(rp[0]), "+v"(rp[1]), "+v"(rp[2]), "+v"(rp[3]), "+v"(rp[4]) :: "memory");
#pragma unroll
              for (int u = 0; u < 5; ++u) st16(rs, rp[u], lane * 16, rdst[u]);
            }
            if (q == 3) wait_vm<17>();
            if (q == 4) wait_vm<17>();
            if (q == 5) wait_vm<13>();
            if (q == 6) wait_vm<4>();
            if (q == 7) wait_vm<0>();
          }
          const v2u lo = vld<v2u>(xbuf(q) + xl + (((2 * j) ^ xf) * 16)), hi = vld<v2u>(xbuf(q) + xl + (((2 * j + 1) ^ xf) * 16));
          B[n] = v4u{lo.x, lo.y, hi.x, hi.y};
          if (j == 3 && (q == 3 || q == 4 || q == 7)) permit(c, q == 7 ? 10 : q + 2);
        }
        if (hf == 1) {
          if (i == 3 || i == 7 || i == 11) xdma(5 + i / 4);
          if (i >= 0 && i % 4 == 3) {
#pragma unroll
            for (int u = i - 3; u <= i; ++u) {
              const v4u f = B[u];
              s1 = dot2(f.x, ONES, s1); s2 = dot2(f.x, f.x, s2); s3 = dot2(f.y, ONES, s3); s4 = dot2(f.y, f.y, s4);
              s1 = dot2(f.z, ONES, s1); s2 = dot2(f.z, f.z, s2); s3 = dot2(f.w, ONES, s3); s4 = dot2(f.w, f.w, s4);
            }
            PIN(s1); PIN(s2); PIN(s3); PIN(s4);
          }
          cs_hook<26, true>(c, i, 0, ck, cv);
        }
      };
      gemm_pair<true>(c, B, aK, aV, hook);
      ln_factors(sum_halves(s1 + s3), sum_halves(s2 + s4), la, mf, h != 0);
      rows_ab();
    }
  for (; layer < 2; ++layer) {
    const bf16x8 mfr = asb(v4u{mf, mf, mf, mf});
    aK = mfma(mfr, asb(ck), aK);
    aV = mfma(mfr, asb(cv), aV);
    STAMP(2 + 20 * layer);
    FRESH();
    {
      unsigned pkk[8], pkv[8];
#pragma unroll
      for (int j = 0; j < 8; ++j) {
        const f32x2 a = (j & 1) ? f32x2{ar[j >> 1].z, ar[j >> 1].w} : f32x2{ar[j >> 1].x, ar[j >> 1].y};
        pkk[j] = phi2(pair(aK, j), a);
        pkv[j] = pk2(pair(aV, j) * a);
      }
      const bf16x8 k0 = asb(v4u{pkk[0], pkk[1], pkk[2], pkk[3]}), k1 = asb(v4u{pkk[4], pkk[5], pkk[6], pkk[7]});
      const bf16x8 v0 = asb(v4u{pkv[0], pkv[1], pkv[2], pkv[3]}), v1 = asb(v4u{pkv[4], pkv[5], pkv[6], pkv[7]});
      const bf16x8 one = asb(v4u{ONES, ONES, ONES, ONES});
      f32x16 kv = {}, zz = {};
      kv = mfma(k0, v0, kv); zz = mfma(k0, one, zz); kv = mfma(k1, v1, kv); zz = mfma(k1, one, zz);
      const bool hd = (lane & 16) != 0;
      f32x4 q0, q1, z0, z1;
#pragma unroll
      for (int j = 0; j < 4; ++j) { q0[j] = sel(hd, kv[8 + j], kv[j]); q1[j] = sel(hd, kv[12 + j], kv[4 + j]); }
      vst<f32x4>(L_KVS + w * 2048 + lane * 16, q0);
      vst<f32x4>(L_KVS + w * 2048 + 1024 + lane * 16, q1);
#pragma unroll
      for (int j = 0; j < 4; ++j) { z0[j] = sel(hd, zz[8 + j], zz[j]); z1[j] = sel(hd, zz[12 + j], zz[4 + j]); }
      vst<f32x4>(L_ZS + w * 128 + (lane >> 4) * 32, z0);
      vst<f32x4>(L_ZS + w * 128 + (lane >> 4) * 32 + 16, z1);
      if (layer == 0) DRAIN();
      post(c, L_F1, layer + 1);
      f32x2 tk[4], tz[4];
      for (;;) {
        const v4u fl = vld<v4u>(L_F1);
#pragma unroll
        for (int u = 0; u < 4; ++u) {
          tk[u] = vld<f32x2>(L_KVS + u * 2048 + (w >> 1) * 1024 + lane * 16 + (w & 1) * 8);
          tz[u] = vld<f32x2>(L_ZS + u * 128 + (lane & 3) * 32 + w * 8);
        }
        if (min4(fl) > (unsigned)layer || spent(c)) break;
      }
      const f32x2 sk = (tk[0] + tk[1]) + (tk[2] + tk[3]), sz = (tz[0] + tz[1]) + (tz[2] + tz[3]);
      const int po = WS_KVP + (((layer * 16 + m) * 4 + w) * 16 + p) * KVW;
      st8(rs, v2u{f2u(sk.x), f2u(sk.y)}, lane * 8, po);
      st8(rs, v2u{f2u(sz.x), f2u(sz.y)}, 512 + (lane & 3) * 8, po);
    }
    STAMP(3 + 20 * layer);
    unsigned pq[8], pk;
    {
      f32x16 aQ = {};
      v4u c0;
      auto hook = [&](int i) __attribute__((always_inline)) {
        if (i == 20) {
          DRAIN();
          set_flag(c, WS_FKV + ((layer * 16 + m) * 4 + w) * 256 + p * 4);
          if (layer == 0) {
            const GAS char* xr = (const GAS char*)P.x + (size_t)(tbw * 32 + t32) * 1024 + cb * 64 + h * 8;
#pragma unroll
            for (int g = 0; g < 4; ++g) { const v2u v = *(const GAS v2u*)(xr + g * 16); R[2 * g] = v.x; R[2 * g + 1] = v.y; }
          }
        }
        cs_hook<24, false>(c, i, 1, c0, c0);
        if (i == 31) pk = poll_issue(c, WS_FKV + ((layer * 16 + m) * 4 + w) * 256, 0xffffull);
      };
      gemm_single(c, B, aQ, hook);
      aQ = mfma(asb(c0), mfr, aQ);
#pragma unroll
      for (int j = 0; j < 8; ++j) { pq[j] = phi2(pair(aQ, j), f32x2{la, la}); PIN(pq[j]); }
    }
    STAMP(4 + 20 * layer);
    {
      FRESH();
      poll_done<0>(c, pk, WS_FKV + ((layer * 16 + m) * 4 + w) * 256, 0xffffull);
      STAMP(5 + 20 * layer);
      const int go = WS_KVP + ((layer * 16 + m) * 4 + w) * 16 * KVW;
      const v2u zl = ld8(rs, 512 + (lane & 15) * KVW + (lane >> 4) * 8, go);
#pragma unroll
      for (int i = 0; i < 8; ++i) B[i] = ld16(rs, h * (8 * KVW) + t32 * 16, go + i * KVW);
      SCHED();
      const float zr0 = row_sum(u2f(zl.x)), zr1 = row_sum(u2f(zl.y));
      if ((lane & 15) == 0) vst<unsigned>(L_ZR + (lane >> 4) * 16 + w * 4, pk2(zr0, zr1));
      f32x4 t[8];
#pragma unroll
      for (int i = 0; i < 8; ++i) t[i] = __builtin_bit_cast(f32x4, B[i]);
      const f32x4 r = ((t[0] + t[1]) + (t[2] + t[3])) + ((t[4] + t[5]) + (t[6] + t[7]));
      const unsigned e0 = pk2(sum_halves(r.x), sum_halves(r.y)), e1 = pk2(sum_halves(r.z), sum_halves(r.w));
      vst<unsigned>(L_KVR + (2 * t32 + h) * 16 + w * 4, h ? e1 : e0);
      post(c, L_F2, layer + 1);
      v4u fk, fz;
      for (;;) {
        const v4u fl = vld<v4u>(L_F2);
        fk = vld<v4u>(L_KVR + lane * 16); fz = vld<v4u>(L_ZR + (lane >> 4) * 16);
        if (min4(fl) > (unsigned)layer || spent(c)) break;
      }
      permit(c, c.s + 8);
      const bool hd = (lane & 16) != 0;
      const v4u zero = {0u, 0u, 0u, 0u};
      const bf16x8 p0 = asb(v4u{pq[0], pq[1], pq[2], pq[3]}), p1 = asb(v4u{pq[4], pq[5], pq[6], pq[7]});
      f32x16 num = {}, den = {};
      den = mfma(asb(hd ? zero : fz), p0, den); den = mfma(asb(hd ? fz : zero), p1, den);
      num = mfma(asb(hd ? zero : fk), p0, num); num = mfma(asb(hd ? fk : zero), p1, num);
      const float i0 = __builtin_amdgcn_rcpf(den[0] + 1e-6f), i1 = __builtin_amdgcn_rcpf(den[8] + 1e-6f);
      const int oo = WS_O + (tbq * 32 + 2 * m) * 1024;
      st16(rs, v4u{pk2(pair(num, 0) * i0), pk2(pair(num, 1) * i0), pk2(pair(num, 2) * i0), pk2(pair(num, 3) * i0)}, lane * 16, oo);
      st16(rs, v4u{pk2(pair(num, 4) * i1), pk2(pair(num, 5) * i1), pk2(pair(num, 6) * i1), pk2(pair(num, 7) * i1)}, lane * 16 + 1024, oo);
    }
    {
      f32x16 aM = {}, aO = {};
      Wide d;
      const unsigned s0 = c.s;
      wide_steps<16, -PW, 0>(c, d, B, aM, aO, kh, NoHook());
      DRAIN();
      set_flag(c, WS_FO + tbq * 256 + m * 4);
      STAMP(6 + 20 * layer);
      FRESH();
      wait_flags(c, WS_FO + tbw * 256, kh ? 0xccccull : 0x3333ull);
      STAMP(7 + 20 * layer);
      const int oo = WS_O + (tbw * 32 + 4 * kh) * 1024;
#pragma unroll
      for (int i = 0; i < AW; ++i) B[i] = ld16(rs, lane * 16 + (i & 3) * 1024, oo + (i >> 2) * 8192);
      SCHED();
      auto hook = [&](int i, int hf) __attribute__((always_inline)) {
        const int n = i + AW;
        if (hf == 0) return;
        if (n < 16) B[n] = ld16(rs, lane * 16 + (n & 3) * 1024, oo + (n >> 2) * 8192);
        if (n == 16) permit(c, s0 + 12);
      };
      wide_steps<16, 0, 16>(c, d, B, aM, aO, kh, hook);
      STAMP(8 + 20 * layer);
      FRESH();
      finish_wide(c, aM, aO, R, 2 * layer + 1);
      publish_x(c, rs, R, WS_X1 + (tbw * 32 + 2 * cb) * 1024, WS_ST1 + (tbw * 16 + cb) * 256, WS_FX1 + tbw * 256 + cb * 4);
      vst<v4u>(L_KVS + w * 2048 + lane * 16, v4u{R[0], R[1], R[2], R[3]});
      vst<v4u>(L_KVS + w * 2048 + 1024 + lane * 16, v4u{R[4], R[5], R[6], R[7]});
    }
    STAMP(9 + 20 * layer);
    unsigned pf;
    f32x16 aM = {}, aO = {};
    Wide d;
    unsigned sd;
    {
      FRESH();
      f32x16 G0 = {}, U0 = {}, G1 = {}, U1 = {};
      Pipe q;
      const unsigned s0 = c.s;
      pair_steps<false, -2, 0, 64, false>(c, q, B, G0, U0, NoHook());
      wait_flags(c, WS_FX1 + tbq * 256, 0xffffull);
      STAMP(10 + 20 * layer);
      Stats sv;
      const int xo = WS_X1 + tbq * 32768;
#pragma unroll
      for (int i = 0; i < AH; ++i) B[i] = ld16(rs, lane * 16 + (i & 3) * 1024, xo + (((i >> 2) + ROT(m)) & 7) * 4096);
      SCHED();
      float a2; unsigned gm;
      v4u cg, cu;
      unsigned hp[8];
      const int ho = WS_H + (tbq * 86 + 2 * m) * 1024, fo = WS_FH + tbq * 256 + m * 4;
      float hv;
      auto piece = [&](int e, const f32x16& G, const f32x16& U) __attribute__((always_inline)) {
        float g;
        if (e & 1) asm volatile("v_max_f32 %1, 0, %2\n\tv_mul_f32 %0, %3, %4\n\tv_mul_f32 %1, %1, %0\n\tv_cvt_pk_bf16_f32 %0, %5, %1" : "=&v"(hp[e >> 1]), "=&v"(g) : "v"(G[e]), "v"(U[e]), "v"(a2), "v"(hv));
        else asm volatile("v_max_f32 %1, 0, %2\n\tv_mul_f32 %0, %3, %4\n\tv_mul_f32 %0, %1, %0" : "=&v"(hv), "=&v"(g) : "v"(G[e]), "v"(U[e]), "v"(a2));
      };
      auto tail = [&](const f32x16& G, const f32x16& U) __attribute__((always_inline)) {
#pragma unroll
        for (int j = 0; j < 8; ++j) {
          const f32x2 g = {fmaxf(G[2 * j], 0.f), fmaxf(G[2 * j + 1], 0.f)};
          hp[j] = pk2(g * (pair(U, j) * a2));
        }
      };
      auto store_h = [&](int r, int half) __attribute__((always_inline)) { st16(rs, v4u{hp[4 * half], hp[4 * half + 1], hp[4 * half + 2], hp[4 * half + 3]}, lane * 16 + half * 1024, ho + r * 32768); };
      auto correct = [&](f32x16& G, f32x16& U) __attribute__((always_inline)) {
        const bf16x8 f = asb(v4u{gm, gm, gm, gm});
        G = mfma(asb(cg), f, G); U = mfma(asb(cu), f, U);
      };
      auto shadow = [&](int i, int hf, int r, const f32x16& G, const f32x16& U) __attribute__((always_inline)) {
        constexpr int at[16] = {3, 5, 6, 7, 9, 12, 13, 14, 15, 16, 17, 18, 19, 21, 22, 23};
#pragma unroll
        for (int e = 0; e < 16; ++e) if (2 * i + hf == at[e]) piece(e, G, U);
        if (2 * i + hf == 25) store_h(r, 0);
        if (2 * i + hf == 28) store_h(r, 1);
        if (hf == 0 && i == 24) { DRAIN(); set_flag(c, fo + r * 64); }
        if (hf == 1) cs_hook<25, true>(c, i, 3 + r, cg, cu);
      };
      pair_steps<false, 0, 32, 64, false>(c, q, B, G0, U0, [&](int i, int hf) __attribute__((always_inline)) {
        const int n = i + AH;
        stats_hook(c, sv, 2 * i + hf, rs, WS_ST1 + tbq * 4096, a2, gm);
        if (hf == 0) return;
        if (n < 32) B[n] = ld16(rs, lane * 16 + (n & 3) * 1024, xo + (((n >> 2) + ROT(m)) & 7) * 4096);
        if (n == 32) permit(c, s0 + (m < 11 ? 23 : 19));
        if (i == 26) { a2 = fmul1(a2, a2); PIN(a2); }
        cs_hook<25, true>(c, i, 2, cg, cu);
      });
      correct(G0, U0);
      STAMP(11 + 20 * layer);
      pair_steps<false, 32, 58, 64, false>(c, q, B, G1, U1, [&](int i, int hf) __attribute__((always_inline)) { shadow(i, hf, 0, G0, U0); });
      int rl = 1;
      if (m < 11) {
        pair_steps<false, 58, 64, 96, false>(c, q, B, G1, U1, [&](int i, int hf) __attribute__((always_inline)) { shadow(i, hf, 0, G0, U0); });
        correct(G1, U1);
        STAMP(12 + 20 * layer);
        G0 = f32x16{}; U0 = f32x16{};
        pair_steps<false, 64, 96, 96, false>(c, q, B, G0, U0, [&](int i, int hf) __attribute__((always_inline)) { shadow(i, hf, 1, G1, U1); });
        correct(G0, U0);
        tail(G0, U0);
        rl = 2;
        STAMP(13 + 20 * layer);
      } else {
        pair_steps<false, 58, 64, 64, false>(c, q, B, G1, U1, [&](int i, int hf) __attribute__((always_inline)) { shadow(i, hf, 0, G0, U0); });
        correct(G1, U1);
        tail(G1, U1);
      }
      store_h(rl, 0); store_h(rl, 1);
      pf = poll_issue(c, WS_FH + tbw * 256, kh ? 0xccccccccull : 0x33333333ull);
      sd = c.s;
      wide_steps<43, -PW, 0>(c, d, B, aM, aO, kh, NoHook());
      DRAIN();
      set_flag(c, fo + rl * 64);
    }
    STAMP(14 + 20 * layer);
    {
      FRESH();
      const int fo = WS_FH + tbw * 256;
      poll_done<1>(c, pf, fo, kh ? 0xccccccccull : 0x33333333ull);
      STAMP(15 + 20 * layer);
      const int ho = WS_H + (tbw * 86 + 4 * kh) * 1024;
      auto ldh = [&](int n) __attribute__((always_inline)) {
        if (n < 32) B[n % 16] = ld16(rs, lane * 16 + (n & 3) * 1024, ho + (((n >> 2) + ROT(m)) & 7) * 8192);
        else if (n < 40) B[n % 16] = ld16(rs, lane * 16 + (n & 3) * 1024, ho + (n >> 2) * 8192);
        else B[n % 16] = ld16(rs, lane * 16 + (n & 3) * 1024, ho + 81920 - kh * 1024);
      };
#pragma unroll
      for (int i = 0; i < AW; ++i) ldh(i);
      SCHED();
      unsigned pf2;
      auto hook = [&](int i, int hf) __attribute__((always_inline)) {
        const int n = i + AW;
        if (hf == 0) return;
        if (n == 24) pf2 = poll_issue(c, fo, 0x7ffull << 32);
        if (n == 32) poll_done<8>(c, pf2, fo, 0x7ffull << 32);
        if (n < 43) ldh(n);
        if (n == 43) permit(c, sd + 11 + PKV);
        if (i == 36) {
          const v4u r0 = vld<v4u>(L_KVS + w * 2048 + lane * 16), r1 = vld<v4u>(L_KVS + w * 2048 + 1024 + lane * 16);
          R[0] = r0.x; R[1] = r0.y; R[2] = r0.z; R[3] = r0.w; R[4] = r1.x; R[5] = r1.y; R[6] = r1.z; R[7] = r1.w;
        }
      };
      wide_steps<43, 0, 43>(c, d, B, aM, aO, kh, hook);
      STAMP(17 + 20 * layer);
      FRESH();
      finish_wide(c, aM, aO, R, 2 * layer + 2);
    }
#pragma unroll
    for (int i = 0; i < 32; ++i) asm volatile("" : "=v"(B[i]));
    asm volatile("" : "=v"(aK), "=v"(aV), "=v"(la), "=v"(mf), "=v"(ck), "=v"(cv));
    asm volatile("" : "=v"(ar[0]), "=v"(ar[1]), "=v"(ar[2]), "=v"(ar[3]));
    if (layer == 0) {
      publish_x(c, rs, R, WS_X2 + (tbw * 32 + 2 * cb) * 1024, WS_ST2 + (tbw * 16 + cb) * 256, WS_FX2 + tbw * 256 + cb * 4);
      STAMP(18);
      FRESH();
      aK = f32x16{}; aV = f32x16{};
      Pipe q;
      const unsigned s0 = c.s;
      pair_steps<true, -2, 0, 32, false>(c, q, B, aK, aV, NoHook());
      wait_flags(c, WS_FX2 + tbq * 256, 0xffffull);
      STAMP(20);
      Stats sv;
      const int xo = WS_X2 + tbq * 32768;
#pragma unroll
      for (int i = 0; i < AH; ++i) B[i] = ld16(rs, lane * 16 + (i & 3) * 1024, xo + (((i >> 2) + ROT(m)) & 7) * 4096);
      SCHED();
      auto hook = [&](int i, int hf) __attribute__((always_inline)) {
        const int n = i + AH;
        stats_hook(c, sv, 2 * i + hf, rs, WS_ST2 + tbq * 4096, la, mf);
        if (hf == 0) {
          if (i == 25) vst<float>(L_SA + w * 128 + t32 * 4, la);
          if (i == 26) { ar[0] = vld<f32x4>(L_SA + w * 128 + 4 * h * 4); ar[1] = vld<f32x4>(L_SA + w * 128 + (8 + 4 * h) * 4); }
          if (i == 27) { ar[2] = vld<f32x4>(L_SA + w * 128 + (16 + 4 * h) * 4); ar[3] = vld<f32x4>(L_SA + w * 128 + (24 + 4 * h) * 4); }
          return;
        }
        if (n < 32) B[n] = ld16(rs, lane * 16 + (n & 3) * 1024, xo + (((n >> 2) + ROT(m)) & 7) * 4096);
        if (n == 32) permit(c, s0 + 14);
        cs_hook<26, true>(c, i, 0, ck, cv);
      };
      pair_steps<true, 0, 32, 32, false>(c, q, B, aK, aV, hook);
    }
  }
  {
    layer = 0;
    FRESH();
    GAS char* o = (GAS char*)P.out + (size_t)(tbw * 32 + t32) * 1024 + cb * 64 + h * 8;
#pragma unroll
    for (int g = 0; g < 4; ++g) *(GAS v2u*)(o + g * 16) = v2u{R[2 * g], R[2 * g + 1]};
  }
  STAMP(38);
  STAMPV(41, __builtin_amdgcn_s_memtime());
  STAMPV(42, (unsigned long long)(BUDGET - c.budget));
  if (b == 0 && w == 0 && lane == 0) *(GAS unsigned*)(P.ws + WS_EPOCH) = c.tag;
}
}

extern "C" void kernel_launch(void* const* d_in, const int* in_sizes, int n_in, void* d_out, int out_size, void* d_ws,
                              size_t ws_size, hipStream_t stream) {
  Params P;
  P.x = (const char*)d_in[0]; P.wqkv = (const char*)d_in[1]; P.wo = (const char*)d_in[2];
  P.wg = (const char*)d_in[4]; P.wu = (const char*)d_in[5]; P.wd = (const char*)d_in[6];
  P.out = (char*)d_out; P.ws = (char*)d_ws;
  (void)hipFuncSetAttribute((const void*)fused_fwd, hipFuncAttributeMaxDynamicSharedMemorySize, LDS_BYTES);
  fused_fwd<<<256, 512, LDS_BYTES, stream>>>(P);
}
```

```cpp
#include <hip/hip_runtime.h>
#include <stdint.h>

#define GAS __attribute__((address_space(1)))
#define LAS __attribute__((address_space(3)))
#define DI __device__ __forceinline__
typedef unsigned v4u __attribute__((ext_vector_type(4)));
typedef unsigned v2u __attribute__((ext_vector_type(2)));
typedef float f32x16 __attribute__((ext_vector_type(16)));
typedef float f32x4 __attribute__((ext_vector_type(4)));
typedef float f32x2 __attribute__((ext_vector_type(2)));
typedef __bf16 bf16x8 __attribute__((ext_vector_type(8)));
typedef __bf16 bf16x2 __attribute__((ext_vector_type(2)));
typedef short s16x4 __attribute__((ext_vector_type(4)));

#define MAPMODE 1
#define LDEPTH 8
#define LPRIO 3
#define LSLEEP 2
#define AW 8
#define PKV 4
#define THIN 1
#define PD 2
#define AH 8

#define ROT(m) (((m) + 2 * p) & 7)
namespace {
constexpr int KB = 1024;
constexpr int WS_EPOCH = 0;
constexpr int WS_FKV = 4096;
constexpr int WS_FO = WS_FKV + 32 * KB;
constexpr int WS_FX1 = WS_FO + 32 * KB;
constexpr int WS_FH = WS_FX1 + 32 * KB;
constexpr int WS_FX2 = WS_FH + 32 * KB;
constexpr int WS_ST1 = WS_FX2 + 16 * KB;
constexpr int WS_ST2 = WS_ST1 + 512 * KB;
constexpr int KVW = 640;
constexpr int WS_KVP = WS_ST2 + 256 * KB;
constexpr int WS_O = WS_KVP + 2 * 16 * 16 * 4 * KVW;
constexpr int WS_X1 = WS_O + 4096 * KB;
constexpr int WS_X2 = WS_X1 + 4096 * KB;
constexpr int WS_H = WS_X2 + 2048 * KB;
constexpr int WS_RQ = WS_H + 2 * 64 * 86 * KB;
constexpr int WS_RG = WS_RQ + 1536 * KB;
constexpr int WS_RU = WS_RG + 43 * 32 * KB;
constexpr int WS_DBG = WS_RU + 43 * 32 * KB;
constexpr int WS_END = WS_DBG + 256 * 8 * 64 * 8;
constexpr int NSLOT = 8;
constexpr int L_RING = 0;
constexpr int L_SCR = NSLOT * 16 * KB;
constexpr int L_KVS = L_SCR + 16 * KB;
constexpr int L_CSF = L_KVS + 8 * KB;
constexpr int L_SA = L_CSF + 5120;
constexpr int L_KVR = L_SA + 512;
constexpr int L_ZR = L_KVR + 1024;
constexpr int L_ZS = L_ZR + 64;
constexpr int L_DUMMY = L_ZS + 512;
constexpr int L_RDY = L_DUMMY + 448;
constexpr int L_CSC = L_RDY + 32;
constexpr int L_FCNT = L_CSC + 32;
constexpr int L_F1 = L_FCNT + 16;
constexpr int L_F2 = L_F1 + 16;
constexpr int L_F3 = L_F2 + 16;
constexpr int L_END = L_F3 + 16;
static_assert(L_END <= 160 * KB, "LDS");
constexpr int LDS_BYTES = (L_END + 15) / 16 * 16;
constexpr unsigned CODE_BYTES = 45056;
constexpr unsigned BUDGET = 1u << 18;

struct Params {
  const char* x; const char* wqkv; const char* wo; const char* wg; const char* wu; const char* wd;
  char* out; char* ws;
};

extern __shared__ __attribute__((aligned(16))) char smem[];
template <class T> DI LAS T* lp(unsigned off) { return (LAS T*)((LAS char*)smem + off); }
template <class T> DI T vld(unsigned off) { return *(volatile LAS T*)((LAS char*)smem + off); }
template <class T> DI void vst(unsigned off, T v) { *(volatile LAS T*)((LAS char*)smem + off) = v; }
DI unsigned rfl(unsigned v) { return __builtin_amdgcn_readfirstlane(v); }
#define SCHED() __builtin_amdgcn_sched_barrier(0)
#define CFENCE() asm volatile("" ::: "memory")
#define DRAIN() asm volatile("s_waitcnt vmcnt(0)" ::: "memory")
#define PIN(x) asm volatile("" : "+v"(x))

#define STAMP(i) do {} while (0)
#define STAMPV(i, v) do {} while (0)

DI bf16x8 ldw(unsigned a, int second) {
  s16x4 lo = __builtin_amdgcn_ds_read_tr16_b64_v4i16(lp<s16x4>(a));
  s16x4 hi = __builtin_amdgcn_ds_read_tr16_b64_v4i16(lp<s16x4>(a + second));
  return __builtin_bit_cast(bf16x8, __builtin_shufflevector(lo, hi, 0, 1, 2, 3, 4, 5, 6, 7));
}
DI f32x16 mfma(bf16x8 a, bf16x8 b, f32x16 c) { return __builtin_amdgcn_mfma_f32_32x32x16_bf16(a, b, c, 0, 0, 0); }
DI bf16x8 asb(v4u v) { return __builtin_bit_cast(bf16x8, v); }
DI unsigned pk2(float lo, float hi) { bf16x2 r = {(__bf16)lo, (__bf16)hi}; return __builtin_bit_cast(unsigned, r); }
DI unsigned pk2(f32x2 v) { return pk2(v.x, v.y); }
DI float dot2(unsigned a, unsigned b, float c) { return __builtin_amdgcn_fdot2_f32_bf16(__builtin_bit_cast(bf16x2, a), __builtin_bit_cast(bf16x2, b), c, false); }
DI float u2f(unsigned u) { return __builtin_bit_cast(float, u); }
DI unsigned f2u(float f) { return __builtin_bit_cast(unsigned, f); }
DI float sel(bool c, float a, float b) { asm volatile("" : "+v"(a), "+v"(b)); return c ? a : b; }
DI int opaque(int v) { asm volatile("" : "+v"(v)); return v; }
DI int sopaque(int v) { asm volatile("" : "+s"(v)); return v; }
DI float blo(unsigned u) { return u2f(u << 16); }
DI float bhi(unsigned u) { return u2f(u & 0xffff0000u); }
DI float sum_halves(float v) {
  auto r = __builtin_amdgcn_permlane32_swap(f2u(v), f2u(v), false, false);
  float o; asm("v_add_f32 %0, %1, %2" : "=v"(o) : "v"(r[0]), "v"(r[1])); return o;
}
template <int CTRL> DI float dpp_add(float v) { return v + u2f(__builtin_amdgcn_update_dpp(0u, f2u(v), CTRL, 0xf, 0xf, false)); }
DI float row_sum(float v) { v = dpp_add<0x128>(v); v = dpp_add<0x124>(v); v = dpp_add<0x122>(v); return dpp_add<0x121>(v); }
DI f32x2 pair(const f32x16& a, int j) { return f32x2{a[2 * j], a[2 * j + 1]}; }
DI unsigned phi2(f32x2 x, f32x2 a) {
  const f32x2 e2 = x * (a * 1.4426950408889634f);
  const f32x2 t = x * a + 1e-6f;
  const f32x2 e = {__builtin_amdgcn_fmed3f(__builtin_amdgcn_exp2f(e2.x), 0.f, 1.f), __builtin_amdgcn_fmed3f(__builtin_amdgcn_exp2f(e2.y), 0.f, 1.f)};
  const f32x2 mx = {fmaxf(t.x, 1e-6f), fmaxf(t.y, 1e-6f)};
  return pk2(mx + e);
}
constexpr unsigned ONES = 0x3f803f80u;
DI float fadd1(float a, float b) { float r; asm("v_add_f32 %0, %1, %2" : "=v"(r) : "v"(a), "v"(b)); return r; }
DI float fmul1(float a, float b) { float r; asm("v_mul_f32 %0, %1, %2" : "=v"(r) : "v"(a), "v"(b)); return r; }

typedef __amdgpu_buffer_rsrc_t rsrc_t;
DI v4u ld16(rsrc_t rs, int voff, int soff) { return __builtin_amdgcn_raw_buffer_load_b128(rs, voff, soff, 16); }
DI v2u ld8(rsrc_t rs, int voff, int soff) { return __builtin_amdgcn_raw_buffer_load_b64(rs, voff, soff, 16); }
DI void st16(rsrc_t rs, v4u v, int voff, int soff) {
  __builtin_amdgcn_raw_buffer_store_b128(v, rs, voff, soff, 16);
  asm volatile("s_nop 1" ::"v"(v) : "memory");
}
DI void st8(rsrc_t rs, v2u v, int voff, int soff) { __builtin_amdgcn_raw_buffer_store_b64(v, rs, voff, soff, 16); }

struct CW {
  unsigned s, pos, rdy;
  unsigned lane_tr;
  unsigned fa;
  unsigned tag, perm;
  int budget;
  int lane, w;
  char* ws;
};
DI bool spent(CW& c) { c.budget -= 1; return c.budget < 0; }
DI unsigned min4(v4u v) { return rfl(min(min(v.x, v.y), min(v.z, v.w))); }
DI void wait_count(CW& c, unsigned have, unsigned addr, unsigned need) {
  if (__builtin_expect(rfl(have) < need, 0)) {
    for (;;) { if (rfl(vld<unsigned>(addr)) >= need || spent(c)) break; }
  }
}
DI void peek(CW& c, unsigned ahead) { c.rdy = vld<unsigned>(L_RDY + ((c.pos + ahead) & (NSLOT - 1)) * 4); }
DI void wait_full(CW& c) { wait_count(c, c.rdy, L_RDY + c.pos * 4, (c.s / NSLOT + 1) * 4); }
DI void post(CW& c, int word, unsigned v) { vst<unsigned>(c.fa + (word - L_FCNT), v); }
DI void release(CW& c) { c.s += 1; c.pos = (c.pos + 1) & (NSLOT - 1); post(c, L_FCNT, min(c.s + NSLOT, c.perm)); }
DI void permit(CW& c, unsigned v) { c.perm = v; post(c, L_FCNT, min(c.s + NSLOT, v)); }
DI void set_flag(CW& c, int off) {
  const char* base = c.ws + off;
  asm volatile("s_mov_b64 exec, 1\n\ts_nop 4\n\tglobal_atomic_swap %0, %1, %2\n\ts_mov_b64 exec, -1" ::"v"(0u), "v"(c.tag), "s"(base) : "memory");
}
DI void wait_flags(CW& c, int off, unsigned long long mask) {
  unsigned f, bud = rfl(c.budget);
  const unsigned voff = c.lane * 4;
  const char* base = c.ws + off;
  asm volatile(
      "s_mov_b64 exec, %5\n\ts_nop 4\n\t"
      "1:\n\t"
      "global_atomic_or %0, %2, %6, %3 sc0\n\t"
      "s_waitcnt vmcnt(0)\n\t"
      "v_cmp_ne_u32 vcc, %4, %0\n\t"
      "s_cbranch_vccz 2f\n\t"
      "s_sub_i32 %1, %1, 8\n\t"
      "s_cmp_ge_i32 %1, 0\n\t"
      "s_cbranch_scc1 1b\n\t"
      "2:\n\t"
      "s_mov_b64 exec, -1\n\t"
      : "=&v"(f), "+s"(bud) : "v"(voff), "s"(base), "s"(c.tag), "s"(mask), "v"(0u) : "memory", "vcc", "scc");
  c.budget = rfl(bud);
}
DI unsigned poll_issue(CW& c, int off, unsigned long long mask) {
  unsigned f = c.tag;
  const unsigned voff = c.lane * 4;
  const char* base = c.ws + off;
  asm volatile("s_mov_b64 exec, %3\n\tv_not_b32 %0, %0\n\ts_nop 4\n\tglobal_atomic_or %0, %1, %4, %2 sc0\n\ts_mov_b64 exec, -1"
               : "+v"(f) : "v"(voff), "s"(base), "s"(mask), "v"(0u) : "memory");
  return f;
}
template <int N> DI void poll_done(CW& c, unsigned f, int off, unsigned long long mask) {
  asm volatile("s_waitcnt vmcnt(%c1)" : "+v"(f) : "i"(N) : "memory");
  if (__builtin_expect(__builtin_amdgcn_ballot_w64(f != c.tag) != 0ull, 0)) wait_flags(c, off, mask);
}

template <int V> struct IC { static constexpr int value = V; DI constexpr operator int() const { return V; } };
template <int I, int E, class F> DI void static_for(F&& f) { if constexpr (I < E) { f(IC<I>{}); static_for<I + 1, E>(f); } }
struct Pipe { bf16x8 wa[PD + 1], wb[PD + 1]; unsigned ad, ad2, adn, need, pn; };
DI void slot_first(CW& c, Pipe& q) { peek(c, 0); wait_full(c); q.adn = c.lane_tr + c.pos * 16384u; }
DI void slot_peek(CW& c, Pipe& q) { q.pn = (c.pos + 1) & (NSLOT - 1); c.rdy = vld<unsigned>(L_RDY + q.pn * 4); }
DI void slot_prep(CW& c, Pipe& q) { q.need = ((c.s + 1) / NSLOT + 1) * 4; q.adn = c.lane_tr + q.pn * 16384u; PIN(q.adn); asm volatile("" : "+s"(q.need)); }
DI void slot_check(CW& c, Pipe& q) { wait_count(c, c.rdy, L_RDY + q.pn * 4, q.need); }
template <bool SWAP, int I0, int I1, int NEND, bool MORE, class HOOK>
DI void pair_steps(CW& c, Pipe& q, v4u (&B)[32], f32x16& acc0, f32x16& acc1, HOOK hook) {
  static_for<I0, I1>([&](auto ic) __attribute__((always_inline)) {
    constexpr int i = ic.value, n = i + PD;
    constexpr bool F = n < NEND, NX = F && (MORE || n + 8 - n % 8 < NEND);
    if constexpr (n == 0) slot_first(c, q);
    if constexpr (i >= 0) acc0 = SWAP ? mfma(asb(B[i % 32]), q.wa[i % (PD + 1)], acc0) : mfma(q.wa[i % (PD + 1)], asb(B[i % 32]), acc0);
    SCHED();
    if constexpr (F && n % 8 == 0) q.ad = q.adn;
    if constexpr (F) q.wa[n % (PD + 1)] = ldw(q.ad + (n % 8) * 1024, 512);
    if constexpr (NX && n % 8 == 4) slot_peek(c, q);
    if constexpr (NX && n % 8 == 6) slot_prep(c, q);
    if constexpr (NX && n % 8 == 7) slot_check(c, q);
    hook(IC<(i < 0 ? i : i % 32)>{}, IC<0>{});
    SCHED();
    if constexpr (i >= 0) acc1 = SWAP ? mfma(asb(B[i % 32]), q.wb[i % (PD + 1)], acc1) : mfma(q.wb[i % (PD + 1)], asb(B[i % 32]), acc1);
    SCHED();
    if constexpr (F) q.wb[n % (PD + 1)] = ldw(q.ad + 8192 + (n % 8) * 1024, 512);
    if constexpr (F && n % 8 == 7) release(c);
    hook(IC<(i < 0 ? i : i % 32)>{}, IC<1>{});
    SCHED();
  });
}
template <bool SWAP, class HOOK>
DI void gemm_pair(CW& c, v4u (&B)[32], f32x16& acc0, f32x16& acc1, HOOK hook) {
  Pipe q;
  pair_steps<SWAP, -PD, 32, 32, false>(c, q, B, acc0, acc1, hook);
}
template <class HOOK>
DI void gemm_single(CW& c, v4u (&B)[32], f32x16& acc, HOOK hook) {
  bf16x8 wa[5];
  Pipe q;
  static_for<-4, 32>([&](auto ic) __attribute__((always_inline)) {
    constexpr int i = ic.value, n = i + 4;
    if constexpr (n == 0) slot_first(c, q);
    if constexpr (i >= 0) acc = mfma(wa[i % 5], asb(B[i]), acc);
    SCHED();
    if constexpr (n < 32) {
      if constexpr (n % 16 == 0) q.ad = q.adn;
      wa[n % 5] = ldw(q.ad + (n % 16) * 1024, 512);
      if constexpr (n == 8) slot_peek(c, q);
      if constexpr (n == 12) slot_prep(c, q);
      if constexpr (n == 14) slot_check(c, q);
      if constexpr (n % 16 == 15) release(c);
    }
    hook(ic);
    SCHED();
  });
}
constexpr int PW = 12;
struct Wide { bf16x8 wa[PW + 1], wb[PW + 1]; Pipe q; unsigned dO, lt; };
template <int NK, int I0, int I1, class HOOK>
DI void wide_steps(CW& c, Wide& d, v4u (&B)[32], f32x16& accM, f32x16& accO, int kh, HOOK hook) {
  Pipe& q = d.q;
  const unsigned keep = c.lane_tr;
  if constexpr (I0 == -PW) { d.lt = c.lane_tr + kh * (8192u + 512u); d.dO = kh ? -512u : 512u; }
  c.lane_tr = d.lt;
  static_for<I0, I1>([&](auto ic) __attribute__((always_inline)) {
    constexpr int i = ic.value, n = i + PW;
    constexpr bool F = n < NK, NX = F && n + 4 - n % 4 < NK;
    if constexpr (n == 0) slot_first(c, q);
    if constexpr (i >= 0) accO = mfma(d.wb[i % (PW + 1)], asb(B[i % 16]), accO);
    SCHED();
    if constexpr (F && n % 4 == 0) { q.ad = q.adn; if constexpr (n + 3 == NK) q.ad -= kh * 2048u; q.ad2 = q.ad + d.dO; }
    if constexpr (F) d.wb[n % (PW + 1)] = ldw(q.ad2 + (n % 4) * 2048, 1024);
    if constexpr (NX && n % 4 == 0) slot_peek(c, q);
    if constexpr (NX && n % 4 == 2) slot_prep(c, q);
    if constexpr (NX && n % 4 == 3) slot_check(c, q);
    hook(ic, IC<0>{});
    SCHED();
    if constexpr (i >= 0) accM = mfma(d.wa[i % (PW + 1)], asb(B[i % 16]), accM);
    SCHED();
    if constexpr (F) d.wa[n % (PW + 1)] = ldw(q.ad + (n % 4) * 2048, 1024);
    if constexpr (F && (n % 4 == 3 || n == NK - 1)) release(c);
    hook(ic, IC<1>{});
    SCHED();
  });
  c.lane_tr = keep;
}
struct NoHook { template <class A, class B> DI void operator()(A, B) const {} };

DI void ln_factors(float sum, float sq, float& a, unsigned& mf, bool up) {
  float mean = sum * (1.0f / 512.0f);
  PIN(mean);
  const float var = fmaxf(sq * (1.0f / 512.0f) - mean * mean, 0.f);
  a = __builtin_amdgcn_rsqf(var + 1e-5f);
  const unsigned hi = pk2(-mean, -mean);
  const float lo = -mean - blo(hi);
  mf = up ? pk2(lo, lo) : hi;
}
struct Stats { v4u sv[4]; f32x4 t; f32x2 u[4]; float mean, var; };
constexpr int K0 = 34;
DI void stats_hook(CW& c, Stats& t, int k, rsrc_t rs, int soff, float& a, unsigned& mf) {
  if (k == 3 || k == 7 || k == 11 || k == 15) t.sv[k >> 2] = ld16(rs, c.lane * 16 + (k >> 2) * 1024, soff);
  const unsigned sc = L_SCR + (c.w ^ 2) * 4096;
  if (k >= K0 && k < K0 + 4) { const int e = k - K0; t.t[e] = fadd1(fadd1(u2f(t.sv[0][e]), u2f(t.sv[1][e])), fadd1(u2f(t.sv[2][e]), u2f(t.sv[3][e]))); PIN(t.t[e]); }
  if (k == K0 + 4) vst<f32x4>(sc + c.lane * 16, t.t);
  if (k == K0 + 5) { t.u[0] = vld<f32x2>(sc + (c.lane & 31) * 8); t.u[1] = vld<f32x2>(sc + 256 + (c.lane & 31) * 8); }
  if (k == K0 + 6) { t.u[2] = vld<f32x2>(sc + 512 + (c.lane & 31) * 8); t.u[3] = vld<f32x2>(sc + 768 + (c.lane & 31) * 8); }
  if (k == K0 + 10) { t.mean = fadd1(fadd1(t.u[0].x, t.u[1].x), fadd1(t.u[2].x, t.u[3].x)); PIN(t.mean); }
  if (k == K0 + 11) { t.var = fadd1(fadd1(t.u[0].y, t.u[1].y), fadd1(t.u[2].y, t.u[3].y)); PIN(t.var); }
  if (k == K0 + 12) { t.mean *= -1.0f / 512.0f; PIN(t.mean); t.var = t.var * (1.0f / 512.0f) + 1e-5f; PIN(t.var); }
  if (k == K0 + 13) { t.var = fmaxf(t.var - t.mean * t.mean, 1e-5f); PIN(t.var); a = __builtin_amdgcn_rsqf(t.var); PIN(a); }
  if (k == K0 + 14) { mf = pk2(t.mean, t.mean); t.mean -= blo(mf); PIN(mf); PIN(t.mean); }
  if (k == K0 + 15) { mf = c.lane >= 32 ? pk2(t.mean, t.mean) : mf; PIN(mf); }
}
DI void finish_wide(CW& c, const f32x16& accM, const f32x16& accO, unsigned (&R)[8], unsigned seq) {
  const unsigned mine = L_SCR + c.w * 4096 + c.lane * 16, other = L_SCR + (c.w ^ 2) * 4096 + c.lane * 16;
#pragma unroll
  for (int g = 0; g < 4; ++g) vst<f32x4>(mine + g * 1024, f32x4{accO[4 * g], accO[4 * g + 1], accO[4 * g + 2], accO[4 * g + 3]});
  post(c, L_F3, seq);
  f32x2 y[8];
#pragma unroll
  for (int j = 0; j < 8; ++j) { y[j] = pair(accM, j) + f32x2{blo(R[j]), bhi(R[j])}; PIN(y[j]); }
  f32x4 v[4];
  for (;;) {
    const unsigned fl = vld<unsigned>(L_F3 + (c.w ^ 2) * 4);
#pragma unroll
    for (int g = 0; g < 4; ++g) v[g] = vld<f32x4>(other + g * 1024);
    if (rfl(fl) >= seq || spent(c)) break;
  }
#pragma unroll
  for (int g = 0; g < 4; ++g) {
    R[2 * g] = pk2(y[2 * g] + f32x2{v[g].x, v[g].y});
    R[2 * g + 1] = pk2(y[2 * g + 1] + f32x2{v[g].z, v[g].w});
  }
}
DI void publish_x(CW& c, rsrc_t rs, const unsigned (&R)[8], int xoff, int stoff, int floff) {
  st16(rs, v4u{R[0], R[1], R[2], R[3]}, c.lane * 16, xoff);
  st16(rs, v4u{R[4], R[5], R[6], R[7]}, c.lane * 16 + 1024, xoff);
  float s0 = 0.f, q0 = 0.f, s1 = 0.f, q1 = 0.f;
#pragma unroll
  for (int i = 0; i < 4; ++i) { s0 = dot2(R[i], ONES, s0); q0 = dot2(R[i], R[i], q0); s1 = dot2(R[4 + i], ONES, s1); q1 = dot2(R[4 + i], R[4 + i], q1); }
  const float s = sum_halves(s0 + s1), q = sum_halves(q0 + q1);
  st8(rs, v2u{f2u(s), f2u(q)}, (c.lane & 31) * 8, stoff);
  DRAIN();
  set_flag(c, floff);
}

DI void dma4(const char* base, unsigned v0, unsigned v1, unsigned v2, unsigned v3, unsigned dst) {
  asm volatile("s_mov_b32 m0, %5\n\ts_nop 3\n\t"
               "global_load_lds_dwordx4 %0, %4\n\tglobal_load_lds_dwordx4 %1, %4 offset:1024\n\t"
               "global_load_lds_dwordx4 %2, %4 offset:2048\n\tglobal_load_lds_dwordx4 %3, %4 offset:3072"
               ::"v"(v0), "v"(v1), "v"(v2), "v"(v3), "s"(base), "s"(dst) : "memory");
}
DI void dma4_sc1(const char* base, unsigned v, unsigned dst) {
  asm volatile("s_mov_b32 m0, %2\n\ts_nop 3\n\t"
               "global_load_lds_dwordx4 %0, %1 sc1\n\tglobal_load_lds_dwordx4 %0, %1 offset:1024 sc1\n\t"
               "global_load_lds_dwordx4 %0, %1 offset:2048 sc1\n\tglobal_load_lds_dwordx4 %0, %1 offset:3072 sc1"
               ::"v"(v), "s"(base), "s"(dst) : "memory");
}
template <int N> DI void wait_vm() { asm volatile("s_waitcnt vmcnt(%c0)" ::"i"(N) : "memory"); }
DI unsigned tr_lane(int lane) { return (4 * (lane >> 5) + ((lane & 15) >> 2)) * 64 + ((lane >> 4) & 1) * 32 + (lane & 3) * 8; }
DI int fresh_lane() { int l; asm volatile("v_mbcnt_lo_u32_b32 %0, -1, 0\n\tv_mbcnt_hi_u32_b32 %0, -1, %0" : "=v"(l)); return l; }
DI void loader(const Params& P, int p, int m, int lw) {
#define LSTAMP(v, s) do {} while (0)
  const int lane = fresh_lane();
  const unsigned smem_base = (unsigned)(uintptr_t)(LAS char*)smem;
  const unsigned v_raw = (lane >> 2) * 3072 + (lane & 3) * 16;
  const unsigned lane_tr = tr_lane(lane);
  const unsigned nr = m < 11 ? 3 : 2;
  const unsigned NS = 21 + 4 * nr, NT = 2 * NS;
  const unsigned v_lin = lane * 16, v_wide = ((lane & 31) >> 2) * 1024 + (lane >> 5) * 64 + (lane & 3) * 16;
  const unsigned pa = lane == 0 ? L_RDY : L_DUMMY + lane * 4;
  auto arrive = [&](unsigned off, unsigned n) __attribute__((always_inline)) { CFENCE(); (void)__hip_atomic_fetch_add(lp<unsigned>(pa + off), n, __ATOMIC_RELAXED, __HIP_MEMORY_SCOPE_WORKGROUP); CFENCE(); };
  auto colsum4 = [&](unsigned a, float& c0, float& c1) __attribute__((always_inline)) {
#pragma unroll
    for (int n = 0; n < 4; ++n) {
      const v4u f = __builtin_bit_cast(v4u, ldw(a + n * 1024, 512));
      float t0, t1;
      asm("v_lshlrev_b32 %2, 16, %4\n\tv_and_b32 %3, %8, %4\n\tv_add_f32 %0, %0, %2\n\tv_add_f32 %1, %1, %3\n\t"
          "v_lshlrev_b32 %2, 16, %5\n\tv_and_b32 %3, %8, %5\n\tv_add_f32 %0, %0, %2\n\tv_add_f32 %1, %1, %3\n\t"
          "v_lshlrev_b32 %2, 16, %6\n\tv_and_b32 %3, %8, %6\n\tv_add_f32 %0, %0, %2\n\tv_add_f32 %1, %1, %3\n\t"
          "v_lshlrev_b32 %2, 16, %7\n\tv_and_b32 %3, %8, %7\n\tv_add_f32 %0, %0, %2\n\tv_add_f32 %1, %1, %3"
          : "+v"(c0), "+v"(c1), "=&v"(t0), "=&v"(t1) : "v"(f.x), "v"(f.y), "v"(f.z), "v"(f.w), "s"(0xffff0000u));
    }
  };
  auto put_cs = [&](unsigned blk, unsigned part, float v) __attribute__((always_inline)) {
    const float t = sum_halves(v);
    vst<unsigned>(L_CSF + blk * 512 + (lane & 31) * 16 + part * 4, pk2(t, t - blo(pk2(t, t))));
  };
  {
    LSTAMP(tiss, 0u); LSTAMP(tlan, 0u);
    {
      const unsigned ly = lane & 1, k = lane >> 1, tq = ly * 64 + 4 * p + lw, tw = ly * 64 + 4 * p + 2 * (m >> 3) + (lw & 1);
      const unsigned off = k == 0 ? WS_FKV + ((ly * 16 + m) * 4 + lw) * 256 : k == 1 ? WS_FO + tw * 256 : k == 2 ? WS_FX1 + tq * 256 : k == 3 ? WS_FH + tw * 256 : k == 4 ? WS_FH + tw * 256 + 128 : WS_FX2 + (4 * p + lw) * 256;
      asm volatile("s_mov_b64 exec, 0x7ff\n\ts_nop 4\n\tglobal_atomic_or %0, %1, %2 offset:124\n\ts_mov_b64 exec, -1" ::"v"(off), "v"(0u), "s"(P.ws) : "memory");
    }
    LSTAMP(tiss, 1u); LSTAMP(tlan, 1u);
    __builtin_amdgcn_s_setprio(LPRIO);
  }
  unsigned long long tab;
  {
    const unsigned s = 4u + (lane & ~3u) + lw, q = lane & 3;
    const bool l2 = s >= NS;
    const unsigned i = l2 ? s - NS : s;
    const unsigned blk = i < 4u ? q >> 1 : 2u;
    const unsigned ks0 = ((i < 4u ? 8u * i + (q & 1) * 4u : 16u * (i - 4u) + q * 4u) + 4u * ROT(m)) & 31u;
    const unsigned g = i - 10u, d = i - 10u - 4u * nr;
    const unsigned row = min((d < 8u ? (d + ROT(m)) & 7u : d) * 128u + q * 32u, 1344u);
    if (i < 6u && l2) tab = (unsigned long long)P.ws + WS_RQ + ((m * 3 + blk) * 32u + ks0) * 1024u + 1u;
    else if (i < 6u) tab = (unsigned long long)P.wqkv + m * 64 + ks0 * (16u * 3072u);
    else if (i < 10u) tab = (unsigned long long)P.wo + ((i - 6u) * 128u + q * 32u) * 1024u + (m & 7) * 128 + 2u;
    else if (i < 10u + 4u * nr) tab = (unsigned long long)P.ws + ((q >> 1) ? WS_RU : WS_RG) + ((16u * (g >> 2) + m) * 32u + ((8u * (g & 3u) + (q & 1) * 4u + 4u * ROT(m)) & 31u)) * 1024u + 1u;
    else tab = (unsigned long long)P.wd + row * 1024u + (m & 7) * 128 + 2u;
  }
  const unsigned tlo = (unsigned)tab, thi = (unsigned)(tab >> 32);
  auto finish = [&](unsigned s) __attribute__((always_inline)) {
    arrive((s & (NSLOT - 1)) * 4, 4);
    LSTAMP(tlan, s);
    if (s < 6u || (s >= 10u && s < 10u + 4u * nr)) {
      const unsigned a = lane_tr + (s & (NSLOT - 1)) * 16384u;
      __builtin_amdgcn_s_setprio(0);
      float c0 = 0.f, c1 = 0.f, c2 = 0.f, c3 = 0.f;
      colsum4(a, c0, c1); colsum4(a + 4096, c0, c1); colsum4(a + 8192, c2, c3); colsum4(a + 12288, c2, c3);
      const unsigned k = s < 6u ? 1u : 2u + ((s - 10u) >> 2);
      if (s < 6u) put_cs(2, lw, (c0 + c1) + (c2 + c3));
      else { put_cs(2 * k, lw, c0 + c1); put_cs(2 * k + 1, lw, c2 + c3); }
      arrive(L_CSC - L_RDY + k * 4, 1);
      __builtin_amdgcn_s_setprio(LPRIO);
    }
  };
  unsigned limit = 0;
  int budget = BUDGET;
  bool pend = false;
  for (unsigned s = 4 + lw, t = 0; s < NT; s += 4, t += 4) {
    while (s >= limit) {
      limit = min4(vld<v4u>(L_FCNT));
      if (s < limit) break;
      if (pend) { wait_vm<0>(); finish(s - 4); pend = false; continue; }
      __builtin_amdgcn_s_sleep(LSLEEP);
      budget -= 1;
      if (budget < 0) limit = NT;
    }
    {
      const unsigned i = s >= NS ? s - NS : s;
      if (i < 18u) {
        if (pend) { wait_vm<0>(); finish(s - 4); pend = false; }
        const unsigned sp = s - THIN;
        while (rfl(vld<unsigned>(L_RDY + (sp & (NSLOT - 1)) * 4)) < (sp / NSLOT + 1) * 4 && --budget >= 0) __builtin_amdgcn_s_sleep(1);
      }
    }
    LSTAMP(tiss, s);
    const unsigned kind = __builtin_amdgcn_readlane(tlo, t) & 3u;
    const unsigned dst = smem_base + L_RING + (s & (NSLOT - 1)) * 16384u;
    if (kind == 1u) {
#pragma unroll
      for (int q = 0; q < 4; ++q) {
        const unsigned lo = __builtin_amdgcn_readlane(tlo, t + q), hi = __builtin_amdgcn_readlane(thi, t + q);
        dma4_sc1((const char*)(((unsigned long long)hi << 32) | (lo & ~3u)), v_lin, dst + q * 4096);
      }
    } else {
      const unsigned v0 = kind == 2u ? v_wide : v_raw, st = kind == 2u ? 7168u : 48128u;
#pragma unroll
      for (int q = 0; q < 4; ++q) {
        const unsigned lo = __builtin_amdgcn_readlane(tlo, t + q), hi = __builtin_amdgcn_readlane(thi, t + q);
        dma4((const char*)(((unsigned long long)hi << 32) | (lo & ~3u)), v0, v0 + st, v0 + 2 * st, v0 + 3 * st, dst + q * 4096);
      }
    }
    LSTAMP(tis2, s);
    if (pend) { wait_vm<16>(); finish(s - 4); }
    pend = true;
    if (s + 4 >= NT) { wait_vm<0>(); finish(s); }
  }
}
template <int AT, bool TWO> DI void cs_hook(CW& c, int i, unsigned k, v4u& a, v4u& b) {
  if (i == AT) a.x = vld<unsigned>(L_CSC + k * 4);
  if (i == AT + 3) {
    wait_count(c, a.x, L_CSC + k * 4, TWO ? 4u : 2u);
    a = vld<v4u>(L_CSF + k * 1024 + (c.lane & 31) * 16);
    if (TWO) b = vld<v4u>(L_CSF + k * 1024 + 512 + (c.lane & 31) * 16);
  }
}

__global__ void __launch_bounds__(512, 2) fused_fwd(Params P) {
  const int tid = threadIdx.x; int lane = tid & 63; const int wv = __builtin_amdgcn_readfirstlane(tid >> 6);
  const int b = blockIdx.x;
  {
    const unsigned co = CODE_BYTES - 256u - min(((b >> 3) * 8 + wv) * 256u, CODE_BYTES - 256u) + lane * 4;
    unsigned long long pc;
    asm volatile("s_mov_b32 m0, %2\n\ts_getpc_b64 %0\n\tglobal_load_lds_dword %1, %0" : "=&s"(pc) : "v"(co), "s"((unsigned)(uintptr_t)(LAS char*)smem + L_DUMMY) : "memory");
  }
  int p, m;
  if (MAPMODE == 0) { p = (b & 7) * 2 + (b >> 7); m = (b >> 3) & 15; }
  else if (MAPMODE == 1) { const int x = b & 7, i = b >> 3; p = (x >> 1) * 4 + (i >> 3); m = (x & 1) * 8 + (i & 7); }
  else { const int x = b & 7, i = b >> 3; p = i >> 1; m = x * 2 + (i & 1); }
  if (tid < (L_END - L_RDY) / 4) vst<unsigned>(L_RDY + tid * 4, 0u);
  if (tid < 128) vst<v4u>(L_CSF + tid * 16, v4u{0u, 0u, 0u, 0u});
  asm volatile("s_waitcnt lgkmcnt(0)\n\ts_barrier" ::: "memory");
  const unsigned smem_base = (unsigned)(uintptr_t)(LAS char*)smem;
  int w = wv & 3;
  unsigned xv[4];
  const char* xbase = (wv < 4 ? P.x : P.wqkv) + (unsigned)sopaque(wv < 4 ? (4 * p + w) * 32768 : ((w >> 1) + 1) * 1024 + m * 64);
  auto xbuf = [&](int q) -> unsigned { const int j = q % 5; return (j == 0 ? L_SCR : j == 1 ? 7 * 16384 : j == 2 ? 6 * 16384 : j == 3 ? 4 * 16384 : 5 * 16384) + w * 4096; };
  {
#pragma unroll
    for (int k = 0; k < 4; ++k) {
      const unsigned a = (8 * k + (lane >> 3)) * 1024 + (((lane & 7) ^ ((4 * k + (lane >> 4)) & 7)) * 16) - k * 1024;
      const unsigned c = (lane >> 2) * 3072 + (lane & 3) * 16 + k * 48128;
      xv[k] = wv < 4 ? a : c;
    }
    const unsigned cw = wv < 4, stride = sopaque(cw ? 128 : 64 * 3072), r0 = ROT(m) + (cw ? 0 : (w & 1)), sh = cw ? 0 : 1;
#pragma unroll
    for (int j = 0; j < 2; ++j)
      dma4(xbase + (((j << sh) + r0) & 7) * stride, xv[0], xv[1], xv[2], xv[3], smem_base + (cw ? xbuf(j) : j * 16384 + w * 4096));
    if (!cw) {
      const unsigned pa = smem_base + (lane == 0 ? L_RDY : L_DUMMY + lane * 4), a = tr_lane(lane) + w * 4096u;
      float c0 = 0.f, c1 = 0.f;
      auto cs = [&](int j) __attribute__((always_inline)) {
#pragma unroll
        for (int n = 0; n < 4; ++n) {
          const v4u f = __builtin_bit_cast(v4u, ldw(a + j * 16384 + n * 1024, 512));
          c0 = dot2(f.x, ONES, c0); c1 = dot2(f.y, ONES, c1); c0 = dot2(f.z, ONES, c0); c1 = dot2(f.w, ONES, c1);
        }
        PIN(c0); PIN(c1);
      };
      asm volatile("s_waitcnt vmcnt(4)\n\tds_add_u32 %0, %1" ::"v"(pa), "v"(1u) : "memory");
      dma4(xbase + ((4 + r0) & 7) * stride, xv[0], xv[1], xv[2], xv[3], smem_base + 2 * 16384 + w * 4096);
      asm volatile("s_waitcnt vmcnt(4)\n\tds_add_u32 %0, %1 offset:4" ::"v"(pa), "v"(1u) : "memory");
      dma4(xbase + ((6 + r0) & 7) * stride, xv[0], xv[1], xv[2], xv[3], smem_base + 3 * 16384 + w * 4096);
      cs(0);
      asm volatile("s_waitcnt vmcnt(4)\n\tds_add_u32 %0, %1 offset:8" ::"v"(pa), "v"(1u) : "memory");
      cs(1);
      asm volatile("s_waitcnt vmcnt(0)\n\tds_add_u32 %0, %1 offset:12" ::"v"(pa), "v"(1u) : "memory");
      cs(2); cs(3);
      const float t = sum_halves(c0 + c1);
      vst<unsigned>(L_CSF + (w >> 1) * 512 + (lane & 31) * 16 + (w & 1) * 4, pk2(t, t - blo(pk2(t, t))));
      asm volatile("ds_add_u32 %0, %1 offset:%c2" ::"v"(pa), "v"(1u), "i"(L_CSC - L_RDY) : "memory");
    }
  }
  STAMPV(0, t00);
  STAMP(61);
  if (sopaque(wv) >= 4) { loader(P, p, m, wv - 4); lane = fresh_lane(); STAMP(1); return; }
  __builtin_amdgcn_s_setprio(2);

  unsigned epoch;
  asm volatile("s_load_dword %0, %1, 0x0" : "=s"(epoch) : "s"(P.ws) : "memory");
  v4u B[32];
  auto xdma = [&](int q) __attribute__((always_inline)) { dma4(xbase + ((q + ROT(m)) & 7) * 128, xv[0], xv[1], xv[2], xv[3], smem_base + xbuf(q)); };
  xdma(2);
  v4u rp[5];
  auto rp_load = [&]() __attribute__((always_inline)) {
    const int ks = 2 * p + (w & 1), hi = w >> 1;
    const unsigned vq = (lane >> 2) * 3072 + (lane & 3) * 16, vg = (lane >> 2) * 2752 + (lane & 3) * 16;
    const char* sq = P.wqkv + m * 64 + ks * (16 * 3072);
    const char* sg = (hi ? P.wu : P.wg) + m * 64 + ks * (16 * 2752);
    const int r2 = m < 11 ? 2 : 1;
    const char* s0 = sq + (hi ? 0 : 1024); const char* s1 = sq + (hi ? 0 : 2048); const char* s4 = sg + r2 * 1024;
    asm volatile("s_nop 4\n\tglobal_load_dwordx4 %0, %5, %7\n\tglobal_load_dwordx4 %1, %5, %8\n\tglobal_load_dwordx4 %2, %6, %9\n\t"
                 "global_load_dwordx4 %3, %6, %9 offset:1024\n\tglobal_load_dwordx4 %4, %6, %10"
                 : "=&v"(rp[0]), "=&v"(rp[1]), "=&v"(rp[2]), "=&v"(rp[3]), "=&v"(rp[4])
                 : "v"(vq), "v"(vg), "s"(s0), "s"(s1), "s"(sg), "s"(s4) : "memory");
  };
  const rsrc_t rs = __builtin_amdgcn_make_buffer_rsrc((void*)P.ws, 0, WS_END, 0x00020000);
  asm volatile("s_waitcnt lgkmcnt(0)" : "+s"(epoch) :: "memory");
  STAMP(19);
  CW c;
  c.s = 0; c.pos = 0; c.rdy = 0; c.perm = 4; c.lane_tr = tr_lane(lane); c.fa = lane == 0 ? L_FCNT + w * 4 : L_DUMMY + lane * 4;
  c.budget = BUDGET; c.lane = lane; c.w = w; c.ws = P.ws; c.tag = epoch + 1u;
  int h, t32, tbq, tbw, kh, cb;
#define FRESH() do { lane = opaque(lane); h = lane >> 5; t32 = lane & 31; c.lane = lane; p = sopaque(p); m = sopaque(m); w = sopaque(w); c.w = w; \
    tbq = layer * 64 + 4 * p + w; kh = w >> 1; tbw = layer * 64 + 4 * p + 2 * (m >> 3) + (w & 1); cb = 2 * (m & 7) + kh; } while (0)
  unsigned R[8];

  int layer = 0;
    f32x16 aK = {}, aV = {};
    float la; unsigned mf;
    f32x4 ar[4];
    v4u ck, cv;
    FRESH();
    auto rows_ab = [&]() __attribute__((always_inline)) {
      vst<float>(L_SA + w * 128 + t32 * 4, la);
#pragma unroll
      for (int g = 0; g < 4; ++g) ar[g] = vld<f32x4>(L_SA + w * 128 + (8 * g + 4 * h) * 4);
    };
    {
      float s1 = 0.f, s2 = 0.f, s3 = 0.f, s4 = 0.f;
      const unsigned xl = t32 * 128 + 8 * h, xf = (t32 >> 1) & 7;
      auto hook = [&](int i, int hf) __attribute__((always_inline)) {
        const int n = i + 2;
        if (n >= 0 && n < 32 && hf == 0) {
          const int q = n / 4, j = n % 4;
          if (j == 0) {
            if (q == 0) wait_vm<8>();
            if (q == 1 || q == 2 || q == 3) wait_vm<12>();
            if (q == 4) wait_vm<17>();
            if (q == 5) wait_vm<13>();
            if (q == 6) wait_vm<9>();
            if (q == 7) wait_vm<5>();
          }
          const v2u lo = vld<v2u>(xbuf(q) + xl + (((2 * j) ^ xf) * 16)), hi = vld<v2u>(xbuf(q) + xl + (((2 * j + 1) ^ xf) * 16));
          B[n] = v4u{lo.x, lo.y, hi.x, hi.y};
          if (j == 3 && (q == 3 || q == 4 || q == 7)) permit(c, q == 7 ? 10 : q + 2);
        }
        if (hf == 1) {
          if (i == -1) xdma(3);
          if (i == 1) xdma(4);
          if (i == 3 || i == 7 || i == 11) xdma(5 + i / 4);
          if (i == 13) rp_load();
          if (i == 28) {
            asm volatile("s_waitcnt vmcnt(0)" : "+v"(rp[0]), "+v"(rp[1]), "+v"(rp[2]), "+v"(rp[3]), "+v"(rp[4]) :: "memory");
            const int ks = 2 * p + (w & 1), hi = w >> 1, dg = (hi ? WS_RU : WS_RG) + (m * 32 + ks) * 1024;
            st16(rs, rp[0], lane * 16, WS_RQ + ((m * 3 + (hi ? 2 : 0)) * 32 + ks) * 1024);
            st16(rs, rp[1], lane * 16, WS_RQ + ((m * 3 + (hi ? 2 : 1)) * 32 + ks) * 1024);
            st16(rs, rp[2], lane * 16, dg); st16(rs, rp[3], lane * 16, dg + 16 * 32768); st16(rs, rp[4], lane * 16, dg + (m < 11 ? 2 : 1) * (16 * 32768));
          }
          if (i >= 0 && i % 4 == 3) {
#pragma unroll
            for (int u = i - 3; u <= i; ++u) {
              const v4u f = B[u];
              s1 = dot2(f.x, ONES, s1); s2 = dot2(f.x, f.x, s2); s3 = dot2(f.y, ONES, s3); s4 = dot2(f.y, f.y, s4);
              s1 = dot2(f.z, ONES, s1); s2 = dot2(f.z, f.z, s2); s3 = dot2(f.w, ONES, s3); s4 = dot2(f.w, f.w, s4);
            }
            PIN(s1); PIN(s2); PIN(s3); PIN(s4);
          }
          cs_hook<26, true>(c, i, 0, ck, cv);
        }
      };
      gemm_pair<true>(c, B, aK, aV, hook);
      ln_factors(sum_halves(s1 + s3), sum_halves(s2 + s4), la, mf, h != 0);
      rows_ab();
    }
  for (; layer < 2; ++layer) {
    const bf16x8 mfr = asb(v4u{mf, mf, mf, mf});
    aK = mfma(mfr, asb(ck), aK);
    aV = mfma(mfr, asb(cv), aV);
    STAMP(2 + 20 * layer);
    FRESH();
    {
      unsigned pkk[8], pkv[8];
#pragma unroll
      for (int j = 0; j < 8; ++j) {
        const f32x2 a = (j & 1) ? f32x2{ar[j >> 1].z, ar[j >> 1].w} : f32x2{ar[j >> 1].x, ar[j >> 1].y};
        pkk[j] = phi2(pair(aK, j), a);
        pkv[j] = pk2(pair(aV, j) * a);
      }
      const bf16x8 k0 = asb(v4u{pkk[0], pkk[1], pkk[2], pkk[3]}), k1 = asb(v4u{pkk[4], pkk[5], pkk[6], pkk[7]});
      const bf16x8 v0 = asb(v4u{pkv[0], pkv[1], pkv[2], pkv[3]}), v1 = asb(v4u{pkv[4], pkv[5], pkv[6], pkv[7]});
      const bf16x8 one = asb(v4u{ONES, ONES, ONES, ONES});
      f32x16 kv = {}, zz = {};
      kv = mfma(k0, v0, kv); zz = mfma(k0, one, zz); kv = mfma(k1, v1, kv); zz = mfma(k1, one, zz);
      const bool hd = (lane & 16) != 0;
      f32x4 q0, q1, z0, z1;
#pragma unroll
      for (int j = 0; j < 4; ++j) { q0[j] = sel(hd, kv[8 + j], kv[j]); q1[j] = sel(hd, kv[12 + j], kv[4 + j]); }
      vst<f32x4>(L_KVS + w * 2048 + lane * 16, q0);
      vst<f32x4>(L_KVS + w * 2048 + 1024 + lane * 16, q1);
#pragma unroll
      for (int j = 0; j < 4; ++j) { z0[j] = sel(hd, zz[8 + j], zz[j]); z1[j] = sel(hd, zz[12 + j], zz[4 + j]); }
      vst<f32x4>(L_ZS + w * 128 + (lane >> 4) * 32, z0);
      vst<f32x4>(L_ZS + w * 128 + (lane >> 4) * 32 + 16, z1);
      if (layer == 0) DRAIN();
      post(c, L_F1, layer + 1);
      f32x2 tk[4], tz[4];
      for (;;) {
        const v4u fl = vld<v4u>(L_F1);
#pragma unroll
        for (int u = 0; u < 4; ++u) {
          tk[u] = vld<f32x2>(L_KVS + u * 2048 + (w >> 1) * 1024 + lane * 16 + (w & 1) * 8);
          tz[u] = vld<f32x2>(L_ZS + u * 128 + (lane & 3) * 32 + w * 8);
        }
        if (min4(fl) > (unsigned)layer || spent(c)) break;
      }
      const f32x2 sk = (tk[0] + tk[1]) + (tk[2] + tk[3]), sz = (tz[0] + tz[1]) + (tz[2] + tz[3]);
      const int po = WS_KVP + (((layer * 16 + m) * 4 + w) * 16 + p) * KVW;
      st8(rs, v2u{f2u(sk.x), f2u(sk.y)}, lane * 8, po);
      st8(rs, v2u{f2u(sz.x), f2u(sz.y)}, 512 + (lane & 15) * 8, po);
    }
    STAMP(3 + 20 * layer);
    unsigned pq[8], pk;
    {
      f32x16 aQ = {};
      v4u c0;
      auto hook = [&](int i) __attribute__((always_inline)) {
        if (i == 20) {
          DRAIN();
          set_flag(c, WS_FKV + ((layer * 16 + m) * 4 + w) * 256 + p * 4);
          if (layer == 0) {
            const GAS char* xr = (const GAS char*)P.x + (size_t)(tbw * 32 + t32) * 1024 + cb * 64 + h * 8;
#pragma unroll
            for (int g = 0; g < 4; ++g) { const v2u v = *(const GAS v2u*)(xr + g * 16); R[2 * g] = v.x; R[2 * g + 1] = v.y; }
          }
        }
        cs_hook<24, false>(c, i, 1, c0, c0);
        if (i == 31) pk = poll_issue(c, WS_FKV + ((layer * 16 + m) * 4 + w) * 256, 0xffffull);
      };
      gemm_single(c, B, aQ, hook);
      aQ = mfma(asb(c0), mfr, aQ);
#pragma unroll
      for (int j = 0; j < 8; ++j) { pq[j] = phi2(pair(aQ, j), f32x2{la, la}); PIN(pq[j]); }
    }
    STAMP(4 + 20 * layer);
    {
      FRESH();
      poll_done<0>(c, pk, WS_FKV + ((layer * 16 + m) * 4 + w) * 256, 0xffffull);
      STAMP(5 + 20 * layer);
      const int go = WS_KVP + ((layer * 16 + m) * 4 + w) * 16 * KVW;
      const v2u zl = ld8(rs, 512 + (lane & 15) * KVW + (lane >> 4) * 8, go);
#pragma unroll
      for (int i = 0; i < 8; ++i) B[i] = ld16(rs, h * (8 * KVW) + t32 * 16, go + i * KVW);
      SCHED();
      const float zr0 = row_sum(u2f(zl.x)), zr1 = row_sum(u2f(zl.y));
      if ((lane & 15) == 0) vst<unsigned>(L_ZR + (lane >> 4) * 16 + w * 4, pk2(zr0, zr1));
      f32x4 t[8];
#pragma unroll
      for (int i = 0; i < 8; ++i) t[i] = __builtin_bit_cast(f32x4, B[i]);
      const f32x4 r = ((t[0] + t[1]) + (t[2] + t[3])) + ((t[4] + t[5]) + (t[6] + t[7]));
      const unsigned e0 = pk2(sum_halves(r.x), sum_halves(r.y)), e1 = pk2(sum_halves(r.z), sum_halves(r.w));
      vst<unsigned>(L_KVR + (2 * t32 + h) * 16 + w * 4, h ? e1 : e0);
      post(c, L_F2, layer + 1);
      v4u fk, fz;
      for (;;) {
        const v4u fl = vld<v4u>(L_F2);
        fk = vld<v4u>(L_KVR + lane * 16); fz = vld<v4u>(L_ZR + (lane >> 4) * 16);
        if (min4(fl) > (unsigned)layer || spent(c)) break;
      }
      permit(c, c.s + 8);
      const bool hd = (lane & 16) != 0;
      const v4u zero = {0u, 0u, 0u, 0u};
      const bf16x8 p0 = asb(v4u{pq[0], pq[1], pq[2], pq[3]}), p1 = asb(v4u{pq[4], pq[5], pq[6], pq[7]});
      f32x16 num = {}, den = {};
      den = mfma(asb(hd ? zero : fz), p0, den); den = mfma(asb(hd ? fz : zero), p1, den);
      num = mfma(asb(hd ? zero : fk), p0, num); num = mfma(asb(hd ? fk : zero), p1, num);
      const float i0 = __builtin_amdgcn_rcpf(den[0] + 1e-6f), i1 = __builtin_amdgcn_rcpf(den[8] + 1e-6f);
      const int oo = WS_O + (tbq * 32 + 2 * m) * 1024;
      st16(rs, v4u{pk2(pair(num, 0) * i0), pk2(pair(num, 1) * i0), pk2(pair(num, 2) * i0), pk2(pair(num, 3) * i0)}, lane * 16, oo);
      st16(rs, v4u{pk2(pair(num, 4) * i1), pk2(pair(num, 5) * i1), pk2(pair(num, 6) * i1), pk2(pair(num, 7) * i1)}, lane * 16 + 1024, oo);
    }
    {
      f32x16 aM = {}, aO = {};
      Wide d;
      const unsigned s0 = c.s;
      wide_steps<16, -PW, 0>(c, d, B, aM, aO, kh, NoHook());
      DRAIN();
      set_flag(c, WS_FO + tbq * 256 + m * 4);
      STAMP(6 + 20 * layer);
      FRESH();
      wait_flags(c, WS_FO + tbw * 256, kh ? 0xccccull : 0x3333ull);
      STAMP(7 + 20 * layer);
      const int oo = WS_O + (tbw * 32 + 4 * kh) * 1024;
#pragma unroll
      for (int i = 0; i < AW; ++i) B[i] = ld16(rs, lane * 16 + (i & 3) * 1024, oo + (i >> 2) * 8192);
      SCHED();
      auto hook = [&](int i, int hf) __attribute__((always_inline)) {
        const int n = i + AW;
        if (hf == 0) return;
        if (n < 16) B[n] = ld16(rs, lane * 16 + (n & 3) * 1024, oo + (n >> 2) * 8192);
        if (n == 16) permit(c, s0 + 12);
      };
      wide_steps<16, 0, 16>(c, d, B, aM, aO, kh, hook);
      STAMP(8 + 20 * layer);
      FRESH();
      finish_wide(c, aM, aO, R, 2 * layer + 1);
      publish_x(c, rs, R, WS_X1 + (tbw * 32 + 2 * cb) * 1024, WS_ST1 + (tbw * 16 + cb) * 256, WS_FX1 + tbw * 256 + cb * 4);
      vst<v4u>(L_KVS + w * 2048 + lane * 16, v4u{R[0], R[1], R[2], R[3]});
      vst<v4u>(L_KVS + w * 2048 + 1024 + lane * 16, v4u{R[4], R[5], R[6], R[7]});
    }
    STAMP(9 + 20 * layer);
    unsigned pf;
    f32x16 aM = {}, aO = {};
    Wide d;
    unsigned sd;
    {
      FRESH();
      f32x16 G0 = {}, U0 = {}, G1 = {}, U1 = {};
      Pipe q;
      const unsigned s0 = c.s;
      pair_steps<false, -PD, 0, 64, false>(c, q, B, G0, U0, NoHook());
      wait_flags(c, WS_FX1 + tbq * 256, 0xffffull);
      STAMP(10 + 20 * layer);
      Stats sv;
      const int xo = WS_X1 + tbq * 32768;
#pragma unroll
      for (int i = 0; i < AH; ++i) B[i] = ld16(rs, lane * 16 + (i & 3) * 1024, xo + (((i >> 2) + ROT(m)) & 7) * 4096);
      SCHED();
      float a2; unsigned gm;
      v4u cg, cu;
      unsigned hp[8];
      const int ho = WS_H + (tbq * 86 + 2 * m) * 1024, fo = WS_FH + tbq * 256 + m * 4;
      float hv;
      auto piece = [&](int e, const f32x16& G, const f32x16& U) __attribute__((always_inline)) {
        float g;
        if (e & 1) asm volatile("v_max_f32 %1, 0, %2\n\tv_mul_f32 %0, %3, %4\n\tv_mul_f32 %1, %1, %0\n\tv_cvt_pk_bf16_f32 %0, %5, %1" : "=&v"(hp[e >> 1]), "=&v"(g) : "v"(G[e]), "v"(U[e]), "v"(a2), "v"(hv));
        else asm volatile("v_max_f32 %1, 0, %2\n\tv_mul_f32 %0, %3, %4\n\tv_mul_f32 %0, %1, %0" : "=&v"(hv), "=&v"(g) : "v"(G[e]), "v"(U[e]), "v"(a2));
      };
      auto tail = [&](const f32x16& G, const f32x16& U) __attribute__((always_inline)) {
#pragma unroll
        for (int j = 0; j < 8; ++j) {
          const f32x2 g = {fmaxf(G[2 * j], 0.f), fmaxf(G[2 * j + 1], 0.f)};
          hp[j] = pk2(g * (pair(U, j) * a2));
        }
      };
      auto store_h = [&](int r, int half) __attribute__((always_inline)) { st16(rs, v4u{hp[4 * half], hp[4 * half + 1], hp[4 * half + 2], hp[4 * half + 3]}, lane * 16 + half * 1024, ho + r * 32768); };
      auto correct = [&](f32x16& G, f32x16& U) __attribute__((always_inline)) {
        const bf16x8 f = asb(v4u{gm, gm, gm, gm});
        G = mfma(asb(cg), f, G); U = mfma(asb(cu), f, U);
      };
      auto shadow = [&](int i, int hf, int r, const f32x16& G, const f32x16& U) __attribute__((always_inline)) {
        constexpr int at[16] = {3, 5, 6, 7, 9, 12, 13, 14, 15, 16, 17, 18, 19, 21, 22, 23};
#pragma unroll
        for (int e = 0; e < 16; ++e) if (2 * i + hf == at[e]) piece(e, G, U);
        if (2 * i + hf == 25) store_h(r, 0);
        if (2 * i + hf == 28) store_h(r, 1);
        if (hf == 0 && i == 24) { DRAIN(); set_flag(c, fo + r * 64); }
        if (hf == 1) cs_hook<25, true>(c, i, 3 + r, cg, cu);
      };
      pair_steps<false, 0, 32, 64, false>(c, q, B, G0, U0, [&](int i, int hf) __attribute__((always_inline)) {
        const int n = i + AH;
        stats_hook(c, sv, 2 * i + hf, rs, WS_ST1 + tbq * 4096, a2, gm);
        if (hf == 0) return;
        if (n < 32) B[n] = ld16(rs, lane * 16 + (n & 3) * 1024, xo + (((n >> 2) + ROT(m)) & 7) * 4096);
        if (n == 32) permit(c, s0 + (m < 11 ? 23 : 19));
        if (i == 26) { a2 = fmul1(a2, a2); PIN(a2); }
        cs_hook<25, true>(c, i, 2, cg, cu);
      });
      correct(G0, U0);
      STAMP(11 + 20 * layer);
      pair_steps<false, 32, 60 - PD, 64, false>(c, q, B, G1, U1, [&](int i, int hf) __attribute__((always_inline)) { shadow(i, hf, 0, G0, U0); });
      int rl = 1;
      if (m < 11) {
        pair_steps<false, 60 - PD, 64, 96, false>(c, q, B, G1, U1, [&](int i, int hf) __attribute__((always_inline)) { shadow(i, hf, 0, G0, U0); });
        correct(G1, U1);
        STAMP(12 + 20 * layer);
        G0 = f32x16{}; U0 = f32x16{};
        pair_steps<false, 64, 96, 96, false>(c, q, B, G0, U0, [&](int i, int hf) __attribute__((always_inline)) { shadow(i, hf, 1, G1, U1); });
        correct(G0, U0);
        tail(G0, U0);
        rl = 2;
        STAMP(13 + 20 * layer);
      } else {
        pair_steps<false, 60 - PD, 64, 64, false>(c, q, B, G1, U1, [&](int i, int hf) __attribute__((always_inline)) { shadow(i, hf, 0, G0, U0); });
        correct(G1, U1);
        tail(G1, U1);
      }
      store_h(rl, 0); store_h(rl, 1);
      pf = poll_issue(c, WS_FH + tbw * 256, kh ? 0xccccccccull : 0x33333333ull);
      sd = c.s;
      wide_steps<43, -PW, 0>(c, d, B, aM, aO, kh, NoHook());
      DRAIN();
      set_flag(c, fo + rl * 64);
    }
    STAMP(14 + 20 * layer);
    {
      FRESH();
      const int fo = WS_FH + tbw * 256;
      poll_done<1>(c, pf, fo, kh ? 0xccccccccull : 0x33333333ull);
      STAMP(15 + 20 * layer);
      const int ho = WS_H + (tbw * 86 + 4 * kh) * 1024;
      auto ldh = [&](int n) __attribute__((always_inline)) {
        if (n < 32) B[n % 16] = ld16(rs, lane * 16 + (n & 3) * 1024, ho + (((n >> 2) + ROT(m)) & 7) * 8192);
        else if (n < 40) B[n % 16] = ld16(rs, lane * 16 + (n & 3) * 1024, ho + (n >> 2) * 8192);
        else B[n % 16] = ld16(rs, lane * 16 + (n & 3) * 1024, ho + 81920 - kh * 1024);
      };
#pragma unroll
      for (int i = 0; i < AW; ++i) ldh(i);
      SCHED();
      unsigned pf2;
      auto hook = [&](int i, int hf) __attribute__((always_inline)) {
        const int n = i + AW;
        if (hf == 0) return;
        if (n == 24) pf2 = poll_issue(c, fo, 0x7ffull << 32);
        if (n == 32) poll_done<8>(c, pf2, fo, 0x7ffull << 32);
        if (n < 43) ldh(n);
        if (n == 43) permit(c, sd + 11 + PKV);
        if (i == 36) {
          const v4u r0 = vld<v4u>(L_KVS + w * 2048 + lane * 16), r1 = vld<v4u>(L_KVS + w * 2048 + 1024 + lane * 16);
          R[0] = r0.x; R[1] = r0.y; R[2] = r0.z; R[3] = r0.w; R[4] = r1.x; R[5] = r1.y; R[6] = r1.z; R[7] = r1.w;
        }
      };
      wide_steps<43, 0, 43>(c, d, B, aM, aO, kh, hook);
      STAMP(17 + 20 * layer);
      FRESH();
      finish_wide(c, aM, aO, R, 2 * layer + 2);
    }
#pragma unroll
    for (int i = 0; i < 32; ++i) asm volatile("" : "=v"(B[i]));
    asm volatile("" : "=v"(aK), "=v"(aV), "=v"(la), "=v"(mf), "=v"(ck), "=v"(cv));
    asm volatile("" : "=v"(ar[0]), "=v"(ar[1]), "=v"(ar[2]), "=v"(ar[3]));
    if (layer == 0) {
      publish_x(c, rs, R, WS_X2 + (tbw * 32 + 2 * cb) * 1024, WS_ST2 + (tbw * 16 + cb) * 256, WS_FX2 + tbw * 256 + cb * 4);
      STAMP(18);
      FRESH();
      aK = f32x16{}; aV = f32x16{};
      Pipe q;
      const unsigned s0 = c.s;
      pair_steps<true, -PD, 0, 32, false>(c, q, B, aK, aV, NoHook());
      wait_flags(c, WS_FX2 + tbq * 256, 0xffffull);
      STAMP(20);
      Stats sv;
      const int xo = WS_X2 + tbq * 32768;
#pragma unroll
      for (int i = 0; i < AH; ++i) B[i] = ld16(rs, lane * 16 + (i & 3) * 1024, xo + (((i >> 2) + ROT(m)) & 7) * 4096);
      SCHED();
      auto hook = [&](int i, int hf) __attribute__((always_inline)) {
        const int n = i + AH;
        stats_hook(c, sv, 2 * i + hf, rs, WS_ST2 + tbq * 4096, la, mf);
        if (hf == 0) {
          if (i == 25) vst<float>(L_SA + w * 128 + t32 * 4, la);
          if (i == 26) { ar[0] = vld<f32x4>(L_SA + w * 128 + 4 * h * 4); ar[1] = vld<f32x4>(L_SA + w * 128 + (8 + 4 * h) * 4); }
          if (i == 27) { ar[2] = vld<f32x4>(L_SA + w * 128 + (16 + 4 * h) * 4); ar[3] = vld<f32x4>(L_SA + w * 128 + (24 + 4 * h) * 4); }
          return;
        }
        if (n < 32) B[n] = ld16(rs, lane * 16 + (n & 3) * 1024, xo + (((n >> 2) + ROT(m)) & 7) * 4096);
        if (n == 32) permit(c, s0 + 14);
        cs_hook<26, true>(c, i, 0, ck, cv);
      };
      pair_steps<true, 0, 32, 32, false>(c, q, B, aK, aV, hook);
    }
  }
  {
    layer = 0;
    FRESH();
    GAS char* o = (GAS char*)P.out + (size_t)(tbw * 32 + t32) * 1024 + cb * 64 + h * 8;
#pragma unroll
    for (int g = 0; g < 4; ++g) *(GAS v2u*)(o + g * 16) = v2u{R[2 * g], R[2 * g + 1]};
  }
  STAMP(38);
  STAMPV(41, __builtin_amdgcn_s_memtime());
  STAMPV(42, (unsigned long long)(BUDGET - c.budget));
  if (b == 0 && w == 0 && lane == 0) *(GAS unsigned*)(P.ws + WS_EPOCH) = c.tag;
}
}

extern "C" void kernel_launch(void* const* d_in, const int* in_sizes, int n_in, void* d_out, int out_size, void* d_ws,
                              size_t ws_size, hipStream_t stream) {
  Params P;
  P.x = (const char*)d_in[0]; P.wqkv = (const char*)d_in[1]; P.wo = (const char*)d_in[2];
  P.wg = (const char*)d_in[4]; P.wu = (const char*)d_in[5]; P.wd = (const char*)d_in[6];
  P.out = (char*)d_out; P.ws = (char*)d_ws;
  (void)hipFuncSetAttribute((const void*)fused_fwd, hipFuncAttributeMaxDynamicSharedMemorySize, LDS_BYTES);
  fused_fwd<<<256, 512, LDS_BYTES, stream>>>(P);
}
```

```cpp
#include <hip/hip_runtime.h>
#include <stdint.h>

#define GAS __attribute__((address_space(1)))
#define LAS __attribute__((address_space(3)))
#define DI __device__ __forceinline__
typedef unsigned v4u __attribute__((ext_vector_type(4)));
typedef unsigned v2u __attribute__((ext_vector_type(2)));
typedef float f32x16 __attribute__((ext_vector_type(16)));
typedef float f32x4 __attribute__((ext_vector_type(4)));
typedef float f32x2 __attribute__((ext_vector_type(2)));
typedef __bf16 bf16x8 __attribute__((ext_vector_type(8)));
typedef __bf16 bf16x2 __attribute__((ext_vector_type(2)));
typedef short s16x4 __attribute__((ext_vector_type(4)));

#define MAPMODE 1
#define LDEPTH 8
#define LPRIO 3
#define LSLEEP 2
#define AW 8
#define PKV 4
#define THIN 1
#define PD 2
#define PKVAT 16
#define AH 8

#define ROT(m) (((m) + 2 * p) & 7)
namespace {
constexpr int KB = 1024;
constexpr int WS_EPOCH = 0;
constexpr int WS_FKV = 4096;
constexpr int WS_FO = WS_FKV + 32 * KB;
constexpr int WS_FX1 = WS_FO + 32 * KB;
constexpr int WS_FH = WS_FX1 + 32 * KB;
constexpr int WS_FX2 = WS_FH + 32 * KB;
constexpr int WS_ST1 = WS_FX2 + 16 * KB;
constexpr int WS_ST2 = WS_ST1 + 512 * KB;
constexpr int KVW = 640;
constexpr int WS_KVP = WS_ST2 + 256 * KB;
constexpr int WS_O = WS_KVP + 2 * 16 * 16 * 4 * KVW;
constexpr int WS_X1 = WS_O + 4096 * KB;
constexpr int WS_X2 = WS_X1 + 4096 * KB;
constexpr int WS_H = WS_X2 + 2048 * KB;
constexpr int WS_RQ = WS_H + 2 * 64 * 86 * KB;
constexpr int WS_RG = WS_RQ + 1536 * KB;
constexpr int WS_RU = WS_RG + 43 * 32 * KB;
constexpr int WS_DBG = WS_RU + 43 * 32 * KB;
constexpr int WS_END = WS_DBG + 256 * 8 * 64 * 8;
constexpr int NSLOT = 8;
constexpr int L_RING = 0;
constexpr int L_SCR = NSLOT * 16 * KB;
constexpr int L_KVS = L_SCR + 16 * KB;
constexpr int L_CSF = L_KVS + 8 * KB;
constexpr int L_SA = L_CSF + 5120;
constexpr int L_KVR = L_SA + 512;
constexpr int L_ZR = L_KVR + 1024;
constexpr int L_ZS = L_ZR + 64;
constexpr int L_DUMMY = L_ZS + 512;
constexpr int L_RDY = L_DUMMY + 448;
constexpr int L_CSC = L_RDY + 32;
constexpr int L_FCNT = L_CSC + 32;
constexpr int L_F1 = L_FCNT + 16;
constexpr int L_F2 = L_F1 + 16;
constexpr int L_F3 = L_F2 + 16;
constexpr int L_END = L_F3 + 16;
static_assert(L_END <= 160 * KB, "LDS");
constexpr int LDS_BYTES = (L_END + 15) / 16 * 16;
constexpr unsigned CODE_BYTES = 45056;
constexpr unsigned BUDGET = 1u << 18;

struct Params {
  const char* x; const char* wqkv; const char* wo; const char* wg; const char* wu; const char* wd;
  char* out; char* ws;
};

extern __shared__ __attribute__((aligned(16))) char smem[];
template <class T> DI LAS T* lp(unsigned off) { return (LAS T*)((LAS char*)smem + off); }
template <class T> DI T vld(unsigned off) { return *(volatile LAS T*)((LAS char*)smem + off); }
template <class T> DI void vst(unsigned off, T v) { *(volatile LAS T*)((LAS char*)smem + off) = v; }
DI unsigned rfl(unsigned v) { return __builtin_amdgcn_readfirstlane(v); }
#define SCHED() __builtin_amdgcn_sched_barrier(0)
#define CFENCE() asm volatile("" ::: "memory")
#define DRAIN() asm volatile("s_waitcnt vmcnt(0)" ::: "memory")
#define PIN(x) asm volatile("" : "+v"(x))

#define STAMP(i) do {} while (0)
#define STAMPV(i, v) do {} while (0)
#define STAMPB(i) do {} while (0)
#define STAMP_END() do {} while (0)

DI bf16x8 ldw(unsigned a, int second) {
  s16x4 lo = __builtin_amdgcn_ds_read_tr16_b64_v4i16(lp<s16x4>(a));
  s16x4 hi = __builtin_amdgcn_ds_read_tr16_b64_v4i16(lp<s16x4>(a + second));
  return __builtin_bit_cast(bf16x8, __builtin_shufflevector(lo, hi, 0, 1, 2, 3, 4, 5, 6, 7));
}
DI f32x16 mfma(bf16x8 a, bf16x8 b, f32x16 c) { return __builtin_amdgcn_mfma_f32_32x32x16_bf16(a, b, c, 0, 0, 0); }
DI bf16x8 asb(v4u v) { return __builtin_bit_cast(bf16x8, v); }
DI unsigned pk2(float lo, float hi) { bf16x2 r = {(__bf16)lo, (__bf16)hi}; return __builtin_bit_cast(unsigned, r); }
DI unsigned pk2(f32x2 v) { return pk2(v.x, v.y); }
DI float dot2(unsigned a, unsigned b, float c) { return __builtin_amdgcn_fdot2_f32_bf16(__builtin_bit_cast(bf16x2, a), __builtin_bit_cast(bf16x2, b), c, false); }
DI float u2f(unsigned u) { return __builtin_bit_cast(float, u); }
DI unsigned f2u(float f) { return __builtin_bit_cast(unsigned, f); }
DI float sel(bool c, float a, float b) { asm volatile("" : "+v"(a), "+v"(b)); return c ? a : b; }
DI int opaque(int v) { asm volatile("" : "+v"(v)); return v; }
DI int sopaque(int v) { asm volatile("" : "+s"(v)); return v; }
DI float blo(unsigned u) { return u2f(u << 16); }
DI float bhi(unsigned u) { return u2f(u & 0xffff0000u); }
DI float sum_halves(float v) {
  auto r = __builtin_amdgcn_permlane32_swap(f2u(v), f2u(v), false, false);
  float o; asm("v_add_f32 %0, %1, %2" : "=v"(o) : "v"(r[0]), "v"(r[1])); return o;
}
template <int CTRL> DI float dpp_add(float v) { return v + u2f(__builtin_amdgcn_update_dpp(0u, f2u(v), CTRL, 0xf, 0xf, false)); }
DI float row_sum(float v) { v = dpp_add<0x128>(v); v = dpp_add<0x124>(v); v = dpp_add<0x122>(v); return dpp_add<0x121>(v); }
DI f32x2 pair(const f32x16& a, int j) { return f32x2{a[2 * j], a[2 * j + 1]}; }
DI unsigned phi2(f32x2 x, f32x2 a) {
  const f32x2 e2 = x * (a * 1.4426950408889634f);
  const f32x2 t = x * a + 1e-6f;
  const f32x2 e = {__builtin_amdgcn_fmed3f(__builtin_amdgcn_exp2f(e2.x), 0.f, 1.f), __builtin_amdgcn_fmed3f(__builtin_amdgcn_exp2f(e2.y), 0.f, 1.f)};
  const f32x2 mx = {fmaxf(t.x, 1e-6f), fmaxf(t.y, 1e-6f)};
  return pk2(mx + e);
}
constexpr unsigned ONES = 0x3f803f80u;
DI float fadd1(float a, float b) { float r; asm("v_add_f32 %0, %1, %2" : "=v"(r) : "v"(a), "v"(b)); return r; }
DI float fmul1(float a, float b) { float r; asm("v_mul_f32 %0, %1, %2" : "=v"(r) : "v"(a), "v"(b)); return r; }

typedef __amdgpu_buffer_rsrc_t rsrc_t;
DI v4u ld16(rsrc_t rs, int voff, int soff) { return __builtin_amdgcn_raw_buffer_load_b128(rs, voff, soff, 16); }
DI v2u ld8(rsrc_t rs, int voff, int soff) { return __builtin_amdgcn_raw_buffer_load_b64(rs, voff, soff, 16); }
DI void st16(rsrc_t rs, v4u v, int voff, int soff) {
  __builtin_amdgcn_raw_buffer_store_b128(v, rs, voff, soff, 16);
  asm volatile("s_nop 1" ::"v"(v) : "memory");
}
DI void st8(rsrc_t rs, v2u v, int voff, int soff) { __builtin_amdgcn_raw_buffer_store_b64(v, rs, voff, soff, 16); }

struct CW {
  unsigned s, pos, rdy;
  unsigned lane_tr;
  unsigned fa;
  unsigned tag, perm;
  int budget;
  int lane, w;
  char* ws;
};
DI bool spent(CW& c) { c.budget -= 1; return c.budget < 0; }
DI unsigned min4(v4u v) { return rfl(min(min(v.x, v.y), min(v.z, v.w))); }
DI void wait_count(CW& c, unsigned have, unsigned addr, unsigned need) {
  if (__builtin_expect(rfl(have) < need, 0)) {
    for (;;) { if (rfl(vld<unsigned>(addr)) >= need || spent(c)) break; }
  }
}
DI void peek(CW& c, unsigned ahead) { c.rdy = vld<unsigned>(L_RDY + ((c.pos + ahead) & (NSLOT - 1)) * 4); }
DI void wait_full(CW& c) { wait_count(c, c.rdy, L_RDY + c.pos * 4, (c.s / NSLOT + 1) * 4); }
DI void post(CW& c, int word, unsigned v) { vst<unsigned>(c.fa + (word - L_FCNT), v); }
DI void release(CW& c) { c.s += 1; c.pos = (c.pos + 1) & (NSLOT - 1); post(c, L_FCNT, min(c.s + NSLOT, c.perm)); }
DI void permit(CW& c, unsigned v) { c.perm = v; post(c, L_FCNT, min(c.s + NSLOT, v)); }
DI void set_flag(CW& c, int off) {
  const char* base = c.ws + off;
  asm volatile("s_mov_b64 exec, 1\n\ts_nop 4\n\tglobal_atomic_swap %0, %1, %2\n\ts_mov_b64 exec, -1" ::"v"(0u), "v"(c.tag), "s"(base) : "memory");
}
DI void wait_flags(CW& c, int off, unsigned long long mask) {
  unsigned f, bud = rfl(c.budget);
  const unsigned voff = c.lane * 4;
  const char* base = c.ws + off;
  asm volatile(
      "s_mov_b64 exec, %5\n\ts_nop 4\n\t"
      "1:\n\t"
      "global_atomic_or %0, %2, %6, %3 sc0\n\t"
      "s_waitcnt vmcnt(0)\n\t"
      "v_cmp_ne_u32 vcc, %4, %0\n\t"
      "s_cbranch_vccz 2f\n\t"
      "s_sub_i32 %1, %1, 8\n\t"
      "s_cmp_ge_i32 %1, 0\n\t"
      "s_cbranch_scc1 1b\n\t"
      "2:\n\t"
      "s_mov_b64 exec, -1\n\t"
      : "=&v"(f), "+s"(bud) : "v"(voff), "s"(base), "s"(c.tag), "s"(mask), "v"(0u) : "memory", "vcc", "scc");
  c.budget = rfl(bud);
}
DI unsigned poll_issue(CW& c, int off, unsigned long long mask) {
  unsigned f = c.tag;
  const unsigned voff = c.lane * 4;
  const char* base = c.ws + off;
  asm volatile("s_mov_b64 exec, %3\n\tv_not_b32 %0, %0\n\ts_nop 4\n\tglobal_atomic_or %0, %1, %4, %2 sc0\n\ts_mov_b64 exec, -1"
               : "+v"(f) : "v"(voff), "s"(base), "s"(mask), "v"(0u) : "memory");
  return f;
}
template <int N> DI void poll_done(CW& c, unsigned f, int off, unsigned long long mask) {
  asm volatile("s_waitcnt vmcnt(%c1)" : "+v"(f) : "i"(N) : "memory");
  if (__builtin_expect(__builtin_amdgcn_ballot_w64(f != c.tag) != 0ull, 0)) wait_flags(c, off, mask);
}

template <int V> struct IC { static constexpr int value = V; DI constexpr operator int() const { return V; } };
template <int I, int E, class F> DI void static_for(F&& f) { if constexpr (I < E) { f(IC<I>{}); static_for<I + 1, E>(f); } }
struct Pipe { bf16x8 wa[PD + 1], wb[PD + 1]; unsigned ad, ad2, adn, need, pn; };
DI void slot_first(CW& c, Pipe& q) { peek(c, 0); wait_full(c); q.adn = c.lane_tr + c.pos * 16384u; }
DI void slot_peek(CW& c, Pipe& q) { q.pn = (c.pos + 1) & (NSLOT - 1); c.rdy = vld<unsigned>(L_RDY + q.pn * 4); }
DI void slot_prep(CW& c, Pipe& q) { q.need = ((c.s + 1) / NSLOT + 1) * 4; q.adn = c.lane_tr + q.pn * 16384u; PIN(q.adn); asm volatile("" : "+s"(q.need)); }
DI void slot_check(CW& c, Pipe& q) { wait_count(c, c.rdy, L_RDY + q.pn * 4, q.need); }
template <bool SWAP, int I0, int I1, int NEND, bool MORE, class HOOK>
DI void pair_steps(CW& c, Pipe& q, v4u (&B)[32], f32x16& acc0, f32x16& acc1, HOOK hook) {
  static_for<I0, I1>([&](auto ic) __attribute__((always_inline)) {
    constexpr int i = ic.value, n = i + PD;
    constexpr bool F = n < NEND, NX = F && (MORE || n + 8 - n % 8 < NEND);
    if constexpr (n == 0) slot_first(c, q);
    if constexpr (i >= 0) acc0 = SWAP ? mfma(asb(B[i % 32]), q.wa[i % (PD + 1)], acc0) : mfma(q.wa[i % (PD + 1)], asb(B[i % 32]), acc0);
    SCHED();
    if constexpr (F && n % 8 == 0) q.ad = q.adn;
    if constexpr (F) q.wa[n % (PD + 1)] = ldw(q.ad + (n % 8) * 1024, 512);
    if constexpr (NX && n % 8 == 4) slot_peek(c, q);
    if constexpr (NX && n % 8 == 6) slot_prep(c, q);
    if constexpr (NX && n % 8 == 7) slot_check(c, q);
    hook(IC<(i < 0 ? i : i % 32)>{}, IC<0>{});
    SCHED();
    if constexpr (i >= 0) acc1 = SWAP ? mfma(asb(B[i % 32]), q.wb[i % (PD + 1)], acc1) : mfma(q.wb[i % (PD + 1)], asb(B[i % 32]), acc1);
    SCHED();
    if constexpr (F) q.wb[n % (PD + 1)] = ldw(q.ad + 8192 + (n % 8) * 1024, 512);
    if constexpr (F && n % 8 == 7) release(c);
    hook(IC<(i < 0 ? i : i % 32)>{}, IC<1>{});
    SCHED();
  });
}
template <bool SWAP, class HOOK>
DI void gemm_pair(CW& c, v4u (&B)[32], f32x16& acc0, f32x16& acc1, HOOK hook) {
  Pipe q;
  pair_steps<SWAP, -PD, 32, 32, false>(c, q, B, acc0, acc1, hook);
}
template <class HOOK>
DI void gemm_single(CW& c, v4u (&B)[32], f32x16& acc, HOOK hook) {
  bf16x8 wa[5];
  Pipe q;
  static_for<-4, 32>([&](auto ic) __attribute__((always_inline)) {
    constexpr int i = ic.value, n = i + 4;
    if constexpr (n == 0) slot_first(c, q);
    if constexpr (i >= 0) acc = mfma(wa[i % 5], asb(B[i]), acc);
    SCHED();
    if constexpr (n < 32) {
      if constexpr (n % 16 == 0) q.ad = q.adn;
      wa[n % 5] = ldw(q.ad + (n % 16) * 1024, 512);
      if constexpr (n == 8) slot_peek(c, q);
      if constexpr (n == 12) slot_prep(c, q);
      if constexpr (n == 14) slot_check(c, q);
      if constexpr (n % 16 == 15) release(c);
    }
    hook(ic);
    SCHED();
  });
}
template <int PW> struct Wide { bf16x8 wa[PW + 1], wb[PW + 1]; Pipe q; unsigned dO, lt; };
template <int NK, int I0, int I1, int PW, class HOOK>
DI void wide_steps(CW& c, Wide<PW>& d, v4u (&B)[32], f32x16& accM, f32x16& accO, int kh, HOOK hook) {
  Pipe& q = d.q;
  const unsigned keep = c.lane_tr;
  if constexpr (I0 == -PW) { d.lt = c.lane_tr + kh * (8192u + 512u); d.dO = kh ? -512u : 512u; }
  c.lane_tr = d.lt;
  static_for<I0, I1>([&](auto ic) __attribute__((always_inline)) {
    constexpr int i = ic.value, n = i + PW;
    constexpr bool F = n < NK, NX = F && n + 4 - n % 4 < NK;
    if constexpr (n == 0) slot_first(c, q);
    if constexpr (i >= 0) accO = mfma(d.wb[i % (PW + 1)], asb(B[i % 16]), accO);
    SCHED();
    if constexpr (F && n % 4 == 0) { q.ad = q.adn; if constexpr (n + 3 == NK) q.ad -= kh * 2048u; q.ad2 = q.ad + d.dO; }
    if constexpr (F) d.wb[n % (PW + 1)] = ldw(q.ad2 + (n % 4) * 2048, 1024);
    if constexpr (NX && n % 4 == 0) slot_peek(c, q);
    if constexpr (NX && n % 4 == 2) slot_prep(c, q);
    if constexpr (NX && n % 4 == 3) slot_check(c, q);
    hook(ic, IC<0>{});
    SCHED();
    if constexpr (i >= 0) accM = mfma(d.wa[i % (PW + 1)], asb(B[i % 16]), accM);
    SCHED();
    if constexpr (F) d.wa[n % (PW + 1)] = ldw(q.ad + (n % 4) * 2048, 1024);
    if constexpr (F && (n % 4 == 3 || n == NK - 1)) release(c);
    hook(ic, IC<1>{});
    SCHED();
  });
  c.lane_tr = keep;
}
struct NoHook { template <class A, class B> DI void operator()(A, B) const {} };

DI void ln_factors(float sum, float sq, float& a, unsigned& mf, bool up) {
  float mean = sum * (1.0f / 512.0f);
  PIN(mean);
  const float var = fmaxf(sq * (1.0f / 512.0f) - mean * mean, 0.f);
  a = __builtin_amdgcn_rsqf(var + 1e-5f);
  const unsigned hi = pk2(-mean, -mean);
  const float lo = -mean - blo(hi);
  mf = up ? pk2(lo, lo) : hi;
}
struct Stats { v4u sv[4]; f32x4 t; f32x2 u[4]; float mean, var; };
constexpr int K0 = 34;
DI void stats_hook(CW& c, Stats& t, int k, rsrc_t rs, int soff, float& a, unsigned& mf) {
  if (k == 3 || k == 7 || k == 11 || k == 15) t.sv[k >> 2] = ld16(rs, c.lane * 16 + (k >> 2) * 1024, soff);
  const unsigned sc = L_SCR + (c.w ^ 2) * 4096;
  if (k >= K0 && k < K0 + 4) { const int e = k - K0; t.t[e] = fadd1(fadd1(u2f(t.sv[0][e]), u2f(t.sv[1][e])), fadd1(u2f(t.sv[2][e]), u2f(t.sv[3][e]))); PIN(t.t[e]); }
  if (k == K0 + 4) vst<f32x4>(sc + c.lane * 16, t.t);
  if (k == K0 + 5) { t.u[0] = vld<f32x2>(sc + (c.lane & 31) * 8); t.u[1] = vld<f32x2>(sc + 256 + (c.lane & 31) * 8); }
  if (k == K0 + 6) { t.u[2] = vld<f32x2>(sc + 512 + (c.lane & 31) * 8); t.u[3] = vld<f32x2>(sc + 768 + (c.lane & 31) * 8); }
  if (k == K0 + 10) { t.mean = fadd1(fadd1(t.u[0].x, t.u[1].x), fadd1(t.u[2].x, t.u[3].x)); PIN(t.mean); }
  if (k == K0 + 11) { t.var = fadd1(fadd1(t.u[0].y, t.u[1].y), fadd1(t.u[2].y, t.u[3].y)); PIN(t.var); }
  if (k == K0 + 12) { t.mean *= -1.0f / 512.0f; PIN(t.mean); t.var = t.var * (1.0f / 512.0f) + 1e-5f; PIN(t.var); }
  if (k == K0 + 13) { t.var = fmaxf(t.var - t.mean * t.mean, 1e-5f); PIN(t.var); a = __builtin_amdgcn_rsqf(t.var); PIN(a); }
  if (k == K0 + 14) { mf = pk2(t.mean, t.mean); t.mean -= blo(mf); PIN(mf); PIN(t.mean); }
  if (k == K0 + 15) { mf = c.lane >= 32 ? pk2(t.mean, t.mean) : mf; PIN(mf); }
}
DI void finish_wide(CW& c, const f32x16& accM, const f32x16& accO, unsigned (&R)[8], unsigned seq) {
  const unsigned mine = L_SCR + c.w * 4096 + c.lane * 16, other = L_SCR + (c.w ^ 2) * 4096 + c.lane * 16;
#pragma unroll
  for (int g = 0; g < 4; ++g) vst<f32x4>(mine + g * 1024, f32x4{accO[4 * g], accO[4 * g + 1], accO[4 * g + 2], accO[4 * g + 3]});
  post(c, L_F3, seq);
  f32x2 y[8];
#pragma unroll
  for (int j = 0; j < 8; ++j) { y[j] = pair(accM, j) + f32x2{blo(R[j]), bhi(R[j])}; PIN(y[j]); }
  f32x4 v[4];
  for (;;) {
    const unsigned fl = vld<unsigned>(L_F3 + (c.w ^ 2) * 4);
#pragma unroll
    for (int g = 0; g < 4; ++g) v[g] = vld<f32x4>(other + g * 1024);
    if (rfl(fl) >= seq || spent(c)) break;
  }
#pragma unroll
  for (int g = 0; g < 4; ++g) {
    R[2 * g] = pk2(y[2 * g] + f32x2{v[g].x, v[g].y});
    R[2 * g + 1] = pk2(y[2 * g + 1] + f32x2{v[g].z, v[g].w});
  }
}
DI void publish_x(CW& c, rsrc_t rs, const unsigned (&R)[8], int xoff, int stoff, int floff) {
  st16(rs, v4u{R[0], R[1], R[2], R[3]}, c.lane * 16, xoff);
  st16(rs, v4u{R[4], R[5], R[6], R[7]}, c.lane * 16 + 1024, xoff);
  float s0 = 0.f, q0 = 0.f, s1 = 0.f, q1 = 0.f;
#pragma unroll
  for (int i = 0; i < 4; ++i) { s0 = dot2(R[i], ONES, s0); q0 = dot2(R[i], R[i], q0); s1 = dot2(R[4 + i], ONES, s1); q1 = dot2(R[4 + i], R[4 + i], q1); }
  const float s = sum_halves(s0 + s1), q = sum_halves(q0 + q1);
  st8(rs, v2u{f2u(s), f2u(q)}, (c.lane & 31) * 8, stoff);
  DRAIN();
  set_flag(c, floff);
}

DI void dma4(const char* base, unsigned v0, unsigned v1, unsigned v2, unsigned v3, unsigned dst) {
  asm volatile("s_mov_b32 m0, %5\n\ts_nop 3\n\t"
               "global_load_lds_dwordx4 %0, %4\n\tglobal_load_lds_dwordx4 %1, %4 offset:1024\n\t"
               "global_load_lds_dwordx4 %2, %4 offset:2048\n\tglobal_load_lds_dwordx4 %3, %4 offset:3072"
               ::"v"(v0), "v"(v1), "v"(v2), "v"(v3), "s"(base), "s"(dst) : "memory");
}
DI void dma4_sc1(const char* base, unsigned v, unsigned dst) {
  asm volatile("s_mov_b32 m0, %2\n\ts_nop 3\n\t"
               "global_load_lds_dwordx4 %0, %1 sc1\n\tglobal_load_lds_dwordx4 %0, %1 offset:1024 sc1\n\t"
               "global_load_lds_dwordx4 %0, %1 offset:2048 sc1\n\tglobal_load_lds_dwordx4 %0, %1 offset:3072 sc1"
               ::"v"(v), "s"(base), "s"(dst) : "memory");
}
template <int N> DI void wait_vm() { asm volatile("s_waitcnt vmcnt(%c0)" ::"i"(N) : "memory"); }
DI unsigned tr_lane(int lane) { return (4 * (lane >> 5) + ((lane & 15) >> 2)) * 64 + ((lane >> 4) & 1) * 32 + (lane & 3) * 8; }
DI int fresh_lane() { int l; asm volatile("v_mbcnt_lo_u32_b32 %0, -1, 0\n\tv_mbcnt_hi_u32_b32 %0, -1, %0" : "=v"(l)); return l; }
DI void loader(const Params& P, int p, int m, int lw) {
#define LSTAMP(v, s) do {} while (0)
  const int lane = fresh_lane();
  const unsigned smem_base = (unsigned)(uintptr_t)(LAS char*)smem;
  const unsigned v_raw = (lane >> 2) * 3072 + (lane & 3) * 16;
  const unsigned lane_tr = tr_lane(lane);
  const unsigned nr = m < 11 ? 3 : 2;
  const unsigned NS = 21 + 4 * nr, NT = 2 * NS;
  const unsigned v_lin = lane * 16, v_wide = ((lane & 31) >> 2) * 1024 + (lane >> 5) * 64 + (lane & 3) * 16;
  const unsigned pa = lane == 0 ? L_RDY : L_DUMMY + lane * 4;
  auto arrive = [&](unsigned off, unsigned n) __attribute__((always_inline)) { CFENCE(); (void)__hip_atomic_fetch_add(lp<unsigned>(pa + off), n, __ATOMIC_RELAXED, __HIP_MEMORY_SCOPE_WORKGROUP); CFENCE(); };
  auto colsum4 = [&](unsigned a, float& c0, float& c1) __attribute__((always_inline)) {
#pragma unroll
    for (int n = 0; n < 4; ++n) {
      const v4u f = __builtin_bit_cast(v4u, ldw(a + n * 1024, 512));
      float t0, t1;
      asm("v_lshlrev_b32 %2, 16, %4\n\tv_and_b32 %3, %8, %4\n\tv_add_f32 %0, %0, %2\n\tv_add_f32 %1, %1, %3\n\t"
          "v_lshlrev_b32 %2, 16, %5\n\tv_and_b32 %3, %8, %5\n\tv_add_f32 %0, %0, %2\n\tv_add_f32 %1, %1, %3\n\t"
          "v_lshlrev_b32 %2, 16, %6\n\tv_and_b32 %3, %8, %6\n\tv_add_f32 %0, %0, %2\n\tv_add_f32 %1, %1, %3\n\t"
          "v_lshlrev_b32 %2, 16, %7\n\tv_and_b32 %3, %8, %7\n\tv_add_f32 %0, %0, %2\n\tv_add_f32 %1, %1, %3"
          : "+v"(c0), "+v"(c1), "=&v"(t0), "=&v"(t1) : "v"(f.x), "v"(f.y), "v"(f.z), "v"(f.w), "s"(0xffff0000u));
    }
  };
  auto put_cs = [&](unsigned blk, unsigned part, float v) __attribute__((always_inline)) {
    const float t = sum_halves(v);
    vst<unsigned>(L_CSF + blk * 512 + (lane & 31) * 16 + part * 4, pk2(t, t - blo(pk2(t, t))));
  };
  {
    LSTAMP(tiss, 0u); LSTAMP(tlan, 0u);
    {
      const unsigned ly = lane & 1, k = lane >> 1, tq = ly * 64 + 4 * p + lw, tw = ly * 64 + 4 * p + 2 * (m >> 3) + (lw & 1);
      const unsigned off = k == 0 ? WS_FKV + ((ly * 16 + m) * 4 + lw) * 256 : k == 1 ? WS_FO + tw * 256 : k == 2 ? WS_FX1 + tq * 256 : k == 3 ? WS_FH + tw * 256 : k == 4 ? WS_FH + tw * 256 + 128 : WS_FX2 + (4 * p + lw) * 256;
      asm volatile("s_mov_b64 exec, 0x7ff\n\ts_nop 4\n\tglobal_atomic_or %0, %1, %2 offset:124\n\ts_mov_b64 exec, -1" ::"v"(off), "v"(0u), "s"(P.ws) : "memory");
    }
    LSTAMP(tiss, 1u); LSTAMP(tlan, 1u);
    __builtin_amdgcn_s_setprio(LPRIO);
  }
  unsigned long long tab;
  {
    const unsigned s = 4u + (lane & ~3u) + lw, q = lane & 3;
    const bool l2 = s >= NS;
    const unsigned i = l2 ? s - NS : s;
    const unsigned blk = i < 4u ? q >> 1 : 2u;
    const unsigned ks0 = ((i < 4u ? 8u * i + (q & 1) * 4u : 16u * (i - 4u) + q * 4u) + 4u * ROT(m)) & 31u;
    const unsigned g = i - 10u, d = i - 10u - 4u * nr;
    const unsigned row = min((d < 8u ? (d & 4u) | ((d + ROT(m)) & 3u) : d) * 128u + q * 32u, 1344u);
    if (i < 6u && l2) tab = (unsigned long long)P.ws + WS_RQ + ((m * 3 + blk) * 32u + ks0) * 1024u + 1u;
    else if (i < 6u) tab = (unsigned long long)P.wqkv + m * 64 + ks0 * (16u * 3072u);
    else if (i < 10u) tab = (unsigned long long)P.wo + ((i - 6u) * 128u + q * 32u) * 1024u + (m & 7) * 128 + 2u;
    else if (i < 10u + 4u * nr) tab = (unsigned long long)P.ws + ((q >> 1) ? WS_RU : WS_RG) + ((16u * (g >> 2) + m) * 32u + ((8u * (g & 3u) + (q & 1) * 4u + 4u * ROT(m)) & 31u)) * 1024u + 1u;
    else tab = (unsigned long long)P.wd + row * 1024u + (m & 7) * 128 + 2u;
  }
  const unsigned tlo = (unsigned)tab, thi = (unsigned)(tab >> 32);
  auto finish = [&](unsigned s) __attribute__((always_inline)) {
    arrive((s & (NSLOT - 1)) * 4, 4);
    LSTAMP(tlan, s);
    if (s < 6u || (s >= 10u && s < 10u + 4u * nr)) {
      const unsigned a = lane_tr + (s & (NSLOT - 1)) * 16384u;
      __builtin_amdgcn_s_setprio(0);
      float c0 = 0.f, c1 = 0.f, c2 = 0.f, c3 = 0.f;
      colsum4(a, c0, c1); colsum4(a + 4096, c0, c1); colsum4(a + 8192, c2, c3); colsum4(a + 12288, c2, c3);
      const unsigned k = s < 6u ? 1u : 2u + ((s - 10u) >> 2);
      if (s < 6u) put_cs(2, lw, (c0 + c1) + (c2 + c3));
      else { put_cs(2 * k, lw, c0 + c1); put_cs(2 * k + 1, lw, c2 + c3); }
      arrive(L_CSC - L_RDY + k * 4, 1);
      __builtin_amdgcn_s_setprio(LPRIO);
    }
  };
  unsigned limit = 0;
  int budget = BUDGET;
  bool pend = false;
  for (unsigned s = 4 + lw, t = 0; s < NT; s += 4, t += 4) {
    while (s >= limit) {
      limit = min4(vld<v4u>(L_FCNT));
      if (s < limit) break;
      if (pend) { wait_vm<0>(); finish(s - 4); pend = false; continue; }
      __builtin_amdgcn_s_sleep(LSLEEP);
      budget -= 1;
      if (budget < 0) limit = NT;
    }
    {
      const unsigned i = s >= NS ? s - NS : s;
      if (i < 18u) {
        if (pend) { wait_vm<0>(); finish(s - 4); pend = false; }
        const unsigned sp = s - THIN;
        while (rfl(vld<unsigned>(L_RDY + (sp & (NSLOT - 1)) * 4)) < (sp / NSLOT + 1) * 4 && --budget >= 0) __builtin_amdgcn_s_sleep(1);
      }
    }
    LSTAMP(tiss, s);
    const unsigned kind = __builtin_amdgcn_readlane(tlo, t) & 3u;
    const unsigned dst = smem_base + L_RING + (s & (NSLOT - 1)) * 16384u;
    if (kind == 1u) {
#pragma unroll
      for (int q = 0; q < 4; ++q) {
        const unsigned lo = __builtin_amdgcn_readlane(tlo, t + q), hi = __builtin_amdgcn_readlane(thi, t + q);
        dma4_sc1((const char*)(((unsigned long long)hi << 32) | (lo & ~3u)), v_lin, dst + q * 4096);
      }
    } else {
      const unsigned v0 = kind == 2u ? v_wide : v_raw, st = kind == 2u ? 7168u : 48128u;
#pragma unroll
      for (int q = 0; q < 4; ++q) {
        const unsigned lo = __builtin_amdgcn_readlane(tlo, t + q), hi = __builtin_amdgcn_readlane(thi, t + q);
        dma4((const char*)(((unsigned long long)hi << 32) | (lo & ~3u)), v0, v0 + st, v0 + 2 * st, v0 + 3 * st, dst + q * 4096);
      }
    }
    LSTAMP(tis2, s);
    if (pend) { wait_vm<16>(); finish(s - 4); }
    pend = true;
    if (s + 4 >= NT) { wait_vm<0>(); finish(s); }
  }
}
template <int AT, bool TWO> DI void cs_hook(CW& c, int i, unsigned k, v4u& a, v4u& b) {
  if (i == AT) a.x = vld<unsigned>(L_CSC + k * 4);
  if (i == AT + 3) {
    wait_count(c, a.x, L_CSC + k * 4, TWO ? 4u : 2u);
    a = vld<v4u>(L_CSF + k * 1024 + (c.lane & 31) * 16);
    if (TWO) b = vld<v4u>(L_CSF + k * 1024 + 512 + (c.lane & 31) * 16);
  }
}

__global__ void __launch_bounds__(512, 2) fused_fwd(Params P) {
  const int tid = threadIdx.x; int lane = tid & 63; const int wv = __builtin_amdgcn_readfirstlane(tid >> 6);
  const int b = blockIdx.x;
  {
    const unsigned co = CODE_BYTES - 256u - min(((b >> 3) * 8 + wv) * 256u, CODE_BYTES - 256u) + lane * 4;
    unsigned long long pc;
    asm volatile("s_mov_b32 m0, %2\n\ts_getpc_b64 %0\n\tglobal_load_lds_dword %1, %0" : "=&s"(pc) : "v"(co), "s"((unsigned)(uintptr_t)(LAS char*)smem + L_DUMMY) : "memory");
  }
  int p, m;
  if (MAPMODE == 0) { p = (b & 7) * 2 + (b >> 7); m = (b >> 3) & 15; }
  else if (MAPMODE == 1) { const int x = b & 7, i = b >> 3; p = (x >> 1) * 4 + (i >> 3); m = (x & 1) * 8 + (i & 7); }
  else { const int x = b & 7, i = b >> 3; p = i >> 1; m = x * 2 + (i & 1); }
  if (tid < (L_END - L_RDY) / 4) vst<unsigned>(L_RDY + tid * 4, 0u);
  if (tid < 128) vst<v4u>(L_CSF + tid * 16, v4u{0u, 0u, 0u, 0u});
  asm volatile("s_waitcnt lgkmcnt(0)\n\ts_barrier" ::: "memory");
  const unsigned smem_base = (unsigned)(uintptr_t)(LAS char*)smem;
  int w = wv & 3;
  unsigned xv[4];
  const char* xbase = (wv < 4 ? P.x : P.wqkv) + (unsigned)sopaque(wv < 4 ? (4 * p + w) * 32768 : ((w >> 1) + 1) * 1024 + m * 64);
  auto xbuf = [&](int q) -> unsigned { const int j = q % 5; return (j == 0 ? L_SCR : j == 1 ? 7 * 16384 : j == 2 ? 6 * 16384 : j == 3 ? 4 * 16384 : 5 * 16384) + w * 4096; };
  {
#pragma unroll
    for (int k = 0; k < 4; ++k) {
      const unsigned a = (8 * k + (lane >> 3)) * 1024 + (((lane & 7) ^ ((4 * k + (lane >> 4)) & 7)) * 16) - k * 1024;
      const unsigned c = (lane >> 2) * 3072 + (lane & 3) * 16 + k * 48128;
      xv[k] = wv < 4 ? a : c;
    }
    const unsigned cw = wv < 4, stride = sopaque(cw ? 128 : 64 * 3072), r0 = ROT(m) + (cw ? 0 : (w & 1)), sh = cw ? 0 : 1;
#pragma unroll
    for (int j = 0; j < 2; ++j)
      dma4(xbase + (((j << sh) + r0) & 7) * stride, xv[0], xv[1], xv[2], xv[3], smem_base + (cw ? xbuf(j) : j * 16384 + w * 4096));
    if (!cw) {
      const unsigned pa = smem_base + (lane == 0 ? L_RDY : L_DUMMY + lane * 4), a = tr_lane(lane) + w * 4096u;
      float c0 = 0.f, c1 = 0.f;
      auto cs = [&](int j) __attribute__((always_inline)) {
#pragma unroll
        for (int n = 0; n < 4; ++n) {
          const v4u f = __builtin_bit_cast(v4u, ldw(a + j * 16384 + n * 1024, 512));
          c0 = dot2(f.x, ONES, c0); c1 = dot2(f.y, ONES, c1); c0 = dot2(f.z, ONES, c0); c1 = dot2(f.w, ONES, c1);
        }
        PIN(c0); PIN(c1);
      };
      asm volatile("s_waitcnt vmcnt(4)\n\tds_add_u32 %0, %1" ::"v"(pa), "v"(1u) : "memory");
      dma4(xbase + ((4 + r0) & 7) * stride, xv[0], xv[1], xv[2], xv[3], smem_base + 2 * 16384 + w * 4096);
      asm volatile("s_waitcnt vmcnt(4)\n\tds_add_u32 %0, %1 offset:4" ::"v"(pa), "v"(1u) : "memory");
      dma4(xbase + ((6 + r0) & 7) * stride, xv[0], xv[1], xv[2], xv[3], smem_base + 3 * 16384 + w * 4096);
      cs(0);
      asm volatile("s_waitcnt vmcnt(4)\n\tds_add_u32 %0, %1 offset:8" ::"v"(pa), "v"(1u) : "memory");
      cs(1);
      asm volatile("s_waitcnt vmcnt(0)\n\tds_add_u32 %0, %1 offset:12" ::"v"(pa), "v"(1u) : "memory");
      cs(2); cs(3);
      const float t = sum_halves(c0 + c1);
      vst<unsigned>(L_CSF + (w >> 1) * 512 + (lane & 31) * 16 + (w & 1) * 4, pk2(t, t - blo(pk2(t, t))));
      asm volatile("ds_add_u32 %0, %1 offset:%c2" ::"v"(pa), "v"(1u), "i"(L_CSC - L_RDY) : "memory");
    }
  }
  STAMPV(0, t00);
  STAMP(61);
  if (sopaque(wv) >= 4) { loader(P, p, m, wv - 4); STAMP(1); STAMP_END(); return; }
  __builtin_amdgcn_s_setprio(2);

  unsigned epoch;
  asm volatile("s_load_dword %0, %1, 0x0" : "=s"(epoch) : "s"(P.ws) : "memory");
  v4u B[32];
  auto xdma = [&](int q) __attribute__((always_inline)) { dma4(xbase + ((q + ROT(m)) & 7) * 128, xv[0], xv[1], xv[2], xv[3], smem_base + xbuf(q)); };
  xdma(2);
  v4u rp[5];
  auto rp_load = [&]() __attribute__((always_inline)) {
    const int ks = 2 * p + (w & 1), hi = w >> 1;
    const unsigned vq = (lane >> 2) * 3072 + (lane & 3) * 16, vg = (lane >> 2) * 2752 + (lane & 3) * 16;
    const char* sq = P.wqkv + m * 64 + ks * (16 * 3072);
    const char* sg = (hi ? P.wu : P.wg) + m * 64 + ks * (16 * 2752);
    const int r2 = m < 11 ? 2 : 1;
    const char* s0 = sq + (hi ? 0 : 1024); const char* s1 = sq + (hi ? 0 : 2048); const char* s4 = sg + r2 * 1024;
    asm volatile("s_nop 4\n\tglobal_load_dwordx4 %0, %5, %7\n\tglobal_load_dwordx4 %1, %5, %8\n\tglobal_load_dwordx4 %2, %6, %9\n\t"
                 "global_load_dwordx4 %3, %6, %9 offset:1024\n\tglobal_load_dwordx4 %4, %6, %10"
                 : "=&v"(rp[0]), "=&v"(rp[1]), "=&v"(rp[2]), "=&v"(rp[3]), "=&v"(rp[4])
                 : "v"(vq), "v"(vg), "s"(s0), "s"(s1), "s"(sg), "s"(s4) : "memory");
  };
  const rsrc_t rs = __builtin_amdgcn_make_buffer_rsrc((void*)P.ws, 0, WS_END, 0x00020000);
  asm volatile("s_waitcnt lgkmcnt(0)" : "+s"(epoch) :: "memory");
  STAMP(19);
  CW c;
  c.s = 0; c.pos = 0; c.rdy = 0; c.perm = 4; c.lane_tr = tr_lane(lane); c.fa = lane == 0 ? L_FCNT + w * 4 : L_DUMMY + lane * 4;
  c.budget = BUDGET; c.lane = lane; c.w = w; c.ws = P.ws; c.tag = epoch + 1u;
  int h, t32, tbq, tbw, kh, cb;
#define FRESH() do { lane = opaque(lane); h = lane >> 5; t32 = lane & 31; c.lane = lane; p = sopaque(p); m = sopaque(m); w = sopaque(w); c.w = w; \
    tbq = layer * 64 + 4 * p + w; kh = w >> 1; tbw = layer * 64 + 4 * p + 2 * (m >> 3) + (w & 1); cb = 2 * (m & 7) + kh; } while (0)
  unsigned R[8];

  int layer = 0;
    f32x16 aK = {}, aV = {};
    float la; unsigned mf;
    f32x4 ar[4];
    v4u ck, cv;
    FRESH();
    auto rows_ab = [&]() __attribute__((always_inline)) {
      vst<float>(L_SA + w * 128 + t32 * 4, la);
#pragma unroll
      for (int g = 0; g < 4; ++g) ar[g] = vld<f32x4>(L_SA + w * 128 + (8 * g + 4 * h) * 4);
    };
    {
      float s1 = 0.f, s2 = 0.f, s3 = 0.f, s4 = 0.f;
      const unsigned xl = t32 * 128 + 8 * h, xf = (t32 >> 1) & 7;
      auto hook = [&](int i, int hf) __attribute__((always_inline)) {
        const int n = i + 2;
        if (n >= 0 && n < 32 && hf == 0) {
          const int q = n / 4, j = n % 4;
          if (j == 0) {
            if (q == 0) wait_vm<8>();
            if (q == 1 || q == 2 || q == 3) wait_vm<12>();
            if (q == 4) wait_vm<17>();
            if (q == 5) wait_vm<13>();
            if (q == 6) wait_vm<9>();
            if (q == 7) wait_vm<5>();
          }
          const v2u lo = vld<v2u>(xbuf(q) + xl + (((2 * j) ^ xf) * 16)), hi = vld<v2u>(xbuf(q) + xl + (((2 * j + 1) ^ xf) * 16));
          B[n] = v4u{lo.x, lo.y, hi.x, hi.y};
          if (j == 3 && (q == 3 || q == 4 || q == 7)) permit(c, q == 7 ? 10 : q + 2);
        }
        if (hf == 1) {
          if (i == -1) xdma(3);
          if (i == 1) xdma(4);
          if (i == 3 || i == 7 || i == 11) xdma(5 + i / 4);
          if (i == 13) rp_load();
          if (i == 28) {
            asm volatile("s_waitcnt vmcnt(0)" : "+v"(rp[0]), "+v"(rp[1]), "+v"(rp[2]), "+v"(rp[3]), "+v"(rp[4]) :: "memory");
            const int ks = 2 * p + (w & 1), hi = w >> 1, dg = (hi ? WS_RU : WS_RG) + (m * 32 + ks) * 1024;
            st16(rs, rp[0], lane * 16, WS_RQ + ((m * 3 + (hi ? 2 : 0)) * 32 + ks) * 1024);
            st16(rs, rp[1], lane * 16, WS_RQ + ((m * 3 + (hi ? 2 : 1)) * 32 + ks) * 1024);
            st16(rs, rp[2], lane * 16, dg); st16(rs, rp[3], lane * 16, dg + 16 * 32768); st16(rs, rp[4], lane * 16, dg + (m < 11 ? 2 : 1) * (16 * 32768));
          }
          if (i >= 0 && i % 4 == 3) {
#pragma unroll
            for (int u = i - 3; u <= i; ++u) {
              const v4u f = B[u];
              s1 = dot2(f.x, ONES, s1); s2 = dot2(f.x, f.x, s2); s3 = dot2(f.y, ONES, s3); s4 = dot2(f.y, f.y, s4);
              s1 = dot2(f.z, ONES, s1); s2 = dot2(f.z, f.z, s2); s3 = dot2(f.w, ONES, s3); s4 = dot2(f.w, f.w, s4);
            }
            PIN(s1); PIN(s2); PIN(s3); PIN(s4);
          }
          cs_hook<26, true>(c, i, 0, ck, cv);
        }
      };
      gemm_pair<true>(c, B, aK, aV, hook);
      ln_factors(sum_halves(s1 + s3), sum_halves(s2 + s4), la, mf, h != 0);
      rows_ab();
    }
  for (; layer < 2; ++layer) {
    const bf16x8 mfr = asb(v4u{mf, mf, mf, mf});
    aK = mfma(mfr, asb(ck), aK);
    aV = mfma(mfr, asb(cv), aV);
    STAMPB(2 + 20 * layer);
    FRESH();
    {
      unsigned pkk[8], pkv[8];
#pragma unroll
      for (int j = 0; j < 8; ++j) {
        const f32x2 a = (j & 1) ? f32x2{ar[j >> 1].z, ar[j >> 1].w} : f32x2{ar[j >> 1].x, ar[j >> 1].y};
        pkk[j] = phi2(pair(aK, j), a);
        pkv[j] = pk2(pair(aV, j) * a);
      }
      const bf16x8 k0 = asb(v4u{pkk[0], pkk[1], pkk[2], pkk[3]}), k1 = asb(v4u{pkk[4], pkk[5], pkk[6], pkk[7]});
      const bf16x8 v0 = asb(v4u{pkv[0], pkv[1], pkv[2], pkv[3]}), v1 = asb(v4u{pkv[4], pkv[5], pkv[6], pkv[7]});
      const bf16x8 one = asb(v4u{ONES, ONES, ONES, ONES});
      f32x16 kv = {}, zz = {};
      kv = mfma(k0, v0, kv); zz = mfma(k0, one, zz); kv = mfma(k1, v1, kv); zz = mfma(k1, one, zz);
      const bool hd = (lane & 16) != 0;
      f32x4 q0, q1, z0, z1;
#pragma unroll
      for (int j = 0; j < 4; ++j) { q0[j] = sel(hd, kv[8 + j], kv[j]); q1[j] = sel(hd, kv[12 + j], kv[4 + j]); }
      vst<f32x4>(L_KVS + w * 2048 + lane * 16, q0);
      vst<f32x4>(L_KVS + w * 2048 + 1024 + lane * 16, q1);
#pragma unroll
      for (int j = 0; j < 4; ++j) { z0[j] = sel(hd, zz[8 + j], zz[j]); z1[j] = sel(hd, zz[12 + j], zz[4 + j]); }
      vst<f32x4>(L_ZS + w * 128 + (lane >> 4) * 32, z0);
      vst<f32x4>(L_ZS + w * 128 + (lane >> 4) * 32 + 16, z1);
      if (layer == 0) DRAIN();
      post(c, L_F1, layer + 1);
      f32x2 tk[4], tz[4];
      for (;;) {
        const v4u fl = vld<v4u>(L_F1);
#pragma unroll
        for (int u = 0; u < 4; ++u) {
          tk[u] = vld<f32x2>(L_KVS + u * 2048 + (w >> 1) * 1024 + lane * 16 + (w & 1) * 8);
          tz[u] = vld<f32x2>(L_ZS + u * 128 + (lane & 3) * 32 + w * 8);
        }
        if (min4(fl) > (unsigned)layer || spent(c)) break;
      }
      const f32x2 sk = (tk[0] + tk[1]) + (tk[2] + tk[3]), sz = (tz[0] + tz[1]) + (tz[2] + tz[3]);
      const int po = WS_KVP + (((layer * 16 + m) * 4 + w) * 16 + p) * KVW;
      st8(rs, v2u{f2u(sk.x), f2u(sk.y)}, lane * 8, po);
      st8(rs, v2u{f2u(sz.x), f2u(sz.y)}, 512 + (lane & 15) * 8, po);
    }
    STAMPB(3 + 20 * layer);
    unsigned pq[8], pk;
    {
      f32x16 aQ = {};
      v4u c0;
      auto hook = [&](int i) __attribute__((always_inline)) {
        if (i == 20) {
          DRAIN();
          set_flag(c, WS_FKV + ((layer * 16 + m) * 4 + w) * 256 + p * 4);
          if (layer == 0) {
            const GAS char* xr = (const GAS char*)P.x + (size_t)(tbw * 32 + t32) * 1024 + cb * 64 + h * 8;
#pragma unroll
            for (int g = 0; g < 4; ++g) { const v2u v = *(const GAS v2u*)(xr + g * 16); R[2 * g] = v.x; R[2 * g + 1] = v.y; }
          }
        }
        cs_hook<24, false>(c, i, 1, c0, c0);
        if (i == 31) pk = poll_issue(c, WS_FKV + ((layer * 16 + m) * 4 + w) * 256, 0xffffull);
      };
      gemm_single(c, B, aQ, hook);
      aQ = mfma(asb(c0), mfr, aQ);
#pragma unroll
      for (int j = 0; j < 8; ++j) { pq[j] = phi2(pair(aQ, j), f32x2{la, la}); PIN(pq[j]); }
    }
    STAMPB(4 + 20 * layer);
    {
      FRESH();
      poll_done<0>(c, pk, WS_FKV + ((layer * 16 + m) * 4 + w) * 256, 0xffffull);
      STAMPB(5 + 20 * layer);
      const int go = WS_KVP + ((layer * 16 + m) * 4 + w) * 16 * KVW;
      const v2u zl = ld8(rs, 512 + (lane & 15) * KVW + (lane >> 4) * 8, go);
#pragma unroll
      for (int i = 0; i < 8; ++i) B[i] = ld16(rs, h * (8 * KVW) + t32 * 16, go + i * KVW);
      SCHED();
      const float zr0 = row_sum(u2f(zl.x)), zr1 = row_sum(u2f(zl.y));
      if ((lane & 15) == 0) vst<unsigned>(L_ZR + (lane >> 4) * 16 + w * 4, pk2(zr0, zr1));
      f32x4 t[8];
#pragma unroll
      for (int i = 0; i < 8; ++i) t[i] = __builtin_bit_cast(f32x4, B[i]);
      const f32x4 r = ((t[0] + t[1]) + (t[2] + t[3])) + ((t[4] + t[5]) + (t[6] + t[7]));
      const unsigned e0 = pk2(sum_halves(r.x), sum_halves(r.y)), e1 = pk2(sum_halves(r.z), sum_halves(r.w));
      vst<unsigned>(L_KVR + (2 * t32 + h) * 16 + w * 4, h ? e1 : e0);
      post(c, L_F2, layer + 1);
      v4u fk, fz;
      for (;;) {
        const v4u fl = vld<v4u>(L_F2);
        fk = vld<v4u>(L_KVR + lane * 16); fz = vld<v4u>(L_ZR + (lane >> 4) * 16);
        if (min4(fl) > (unsigned)layer || spent(c)) break;
      }
      permit(c, c.s + 8);
      const bool hd = (lane & 16) != 0;
      const v4u zero = {0u, 0u, 0u, 0u};
      const bf16x8 p0 = asb(v4u{pq[0], pq[1], pq[2], pq[3]}), p1 = asb(v4u{pq[4], pq[5], pq[6], pq[7]});
      f32x16 num = {}, den = {};
      den = mfma(asb(hd ? zero : fz), p0, den); den = mfma(asb(hd ? fz : zero), p1, den);
      num = mfma(asb(hd ? zero : fk), p0, num); num = mfma(asb(hd ? fk : zero), p1, num);
      const float i0 = __builtin_amdgcn_rcpf(den[0] + 1e-6f), i1 = __builtin_amdgcn_rcpf(den[8] + 1e-6f);
      const int oo = WS_O + (tbq * 32 + 2 * m) * 1024;
      st16(rs, v4u{pk2(pair(num, 0) * i0), pk2(pair(num, 1) * i0), pk2(pair(num, 2) * i0), pk2(pair(num, 3) * i0)}, lane * 16, oo);
      st16(rs, v4u{pk2(pair(num, 4) * i1), pk2(pair(num, 5) * i1), pk2(pair(num, 6) * i1), pk2(pair(num, 7) * i1)}, lane * 16 + 1024, oo);
    }
    {
      f32x16 aM = {}, aO = {};
      Wide<12> d;
      const unsigned s0 = c.s;
      wide_steps<16, -12, 0>(c, d, B, aM, aO, kh, NoHook());
      DRAIN();
      set_flag(c, WS_FO + tbq * 256 + m * 4);
      STAMPB(6 + 20 * layer);
      FRESH();
      wait_flags(c, WS_FO + tbw * 256, kh ? 0xccccull : 0x3333ull);
      STAMPB(7 + 20 * layer);
      const int oo = WS_O + (tbw * 32 + 4 * kh) * 1024;
#pragma unroll
      for (int i = 0; i < AW; ++i) B[i] = ld16(rs, lane * 16 + (i & 3) * 1024, oo + (i >> 2) * 8192);
      SCHED();
      auto hook = [&](int i, int hf) __attribute__((always_inline)) {
        const int n = i + AW;
        if (hf == 0) return;
        if (n < 16) B[n] = ld16(rs, lane * 16 + (n & 3) * 1024, oo + (n >> 2) * 8192);
        if (n == 16) permit(c, s0 + 12);
      };
      wide_steps<16, 0, 16>(c, d, B, aM, aO, kh, hook);
      STAMPB(8 + 20 * layer);
      FRESH();
      finish_wide(c, aM, aO, R, 2 * layer + 1);
      publish_x(c, rs, R, WS_X1 + (tbw * 32 + 2 * cb) * 1024, WS_ST1 + (tbw * 16 + cb) * 256, WS_FX1 + tbw * 256 + cb * 4);
      vst<v4u>(L_KVS + w * 2048 + lane * 16, v4u{R[0], R[1], R[2], R[3]});
      vst<v4u>(L_KVS + w * 2048 + 1024 + lane * 16, v4u{R[4], R[5], R[6], R[7]});
    }
    STAMPB(9 + 20 * layer);
    unsigned pf;
    f32x16 aM = {}, aO = {};
    Wide<3> d;
    unsigned sd;
    int fd, hd0, fl2; unsigned long long mk;
    auto ldh = [&](int n) __attribute__((always_inline)) {
      if (n < 32) B[n % 16] = ld16(rs, lane * 16 + (n & 3) * 1024, hd0 + (((n >> 2) & 4) | (((n >> 2) + ROT(m)) & 3)) * 8192);
      else if (n < 40) B[n % 16] = ld16(rs, lane * 16 + (n & 3) * 1024, hd0 + (n >> 2) * 8192);
      else B[n % 16] = ld16(rs, lane * 16 + (n & 3) * 1024, hd0 + 81920 - kh * 1024);
    };
    {
      FRESH();
      f32x16 G0 = {}, U0 = {}, G1 = {}, U1 = {};
      Pipe q;
      const unsigned s0 = c.s;
      pair_steps<false, -PD, 0, 64, false>(c, q, B, G0, U0, NoHook());
      wait_flags(c, WS_FX1 + tbq * 256, 0xffffull);
      STAMPB(10 + 20 * layer);
      Stats sv;
      const int xo = WS_X1 + tbq * 32768;
#pragma unroll
      for (int i = 0; i < AH; ++i) B[i] = ld16(rs, lane * 16 + (i & 3) * 1024, xo + (((i >> 2) + ROT(m)) & 7) * 4096);
      SCHED();
      float a2; unsigned gm;
      v4u cg, cu;
      unsigned hp[8];
      const int ho = WS_H + (tbq * 86 + 2 * m) * 1024, fo = WS_FH + tbq * 256 + m * 4;
      float hv;
      fd = WS_FH + tbw * 256; mk = kh ? 0xccccull : 0x3333ull; hd0 = WS_H + (tbw * 86 + 4 * kh) * 1024;
      fl2 = fo + 128;
      auto piece = [&](int e, const f32x16& G, const f32x16& U) __attribute__((always_inline)) {
        float g;
        if (e & 1) asm volatile("v_max_f32 %1, 0, %2\n\tv_mul_f32 %0, %3, %4\n\tv_mul_f32 %1, %1, %0\n\tv_cvt_pk_bf16_f32 %0, %5, %1" : "=&v"(hp[e >> 1]), "=&v"(g) : "v"(G[e]), "v"(U[e]), "v"(a2), "v"(hv));
        else asm volatile("v_max_f32 %1, 0, %2\n\tv_mul_f32 %0, %3, %4\n\tv_mul_f32 %0, %1, %0" : "=&v"(hv), "=&v"(g) : "v"(G[e]), "v"(U[e]), "v"(a2));
      };
      auto tail = [&](const f32x16& G, const f32x16& U) __attribute__((always_inline)) {
#pragma unroll
        for (int j = 0; j < 8; ++j) {
          const f32x2 g = {fmaxf(G[2 * j], 0.f), fmaxf(G[2 * j + 1], 0.f)};
          hp[j] = pk2(g * (pair(U, j) * a2));
        }
      };
      auto store_h = [&](int r, int half) __attribute__((always_inline)) { st16(rs, v4u{hp[4 * half], hp[4 * half + 1], hp[4 * half + 2], hp[4 * half + 3]}, lane * 16 + half * 1024, ho + r * 32768); };
      auto correct = [&](f32x16& G, f32x16& U) __attribute__((always_inline)) {
        const bf16x8 f = asb(v4u{gm, gm, gm, gm});
        G = mfma(asb(cg), f, G); U = mfma(asb(cu), f, U);
      };
      auto shadow = [&](int i, int hf, int r, const f32x16& G, const f32x16& U) __attribute__((always_inline)) {
        constexpr int at[16] = {3, 5, 6, 7, 9, 12, 13, 14, 15, 16, 17, 18, 19, 21, 22, 23};
#pragma unroll
        for (int e = 0; e < 16; ++e) if (2 * i + hf == at[e]) piece(e, G, U);
        if (2 * i + hf == 25) store_h(r, 0);
        if (2 * i + hf == 28) store_h(r, 1);
        if (hf == 0 && i == 23) { if (r == 0) DRAIN(); else wait_vm<6>(); set_flag(c, fo + r * 64); }
        if (hf == 1) cs_hook<25, true>(c, i, 3 + r, cg, cu);
        if (r == 1) {
          if (hf == 0 && i == 8) pf = poll_issue(c, fd, mk);
          if (hf == 0 && i == 16) { poll_done<2>(c, pf, fd, mk); if (layer) STAMPB(51); }
          if (hf == 1 && i >= 17) ldh(i - 17);
          if (hf == 0 && i == 30) pf = poll_issue(c, fd, mk << 16);
        }
      };
      pair_steps<false, 0, 32, 64, false>(c, q, B, G0, U0, [&](int i, int hf) __attribute__((always_inline)) {
        const int n = i + AH;
        stats_hook(c, sv, 2 * i + hf, rs, WS_ST1 + tbq * 4096, a2, gm);
        if (hf == 0) return;
        if (n < 32) B[n] = ld16(rs, lane * 16 + (n & 3) * 1024, xo + (((n >> 2) + ROT(m)) & 7) * 4096);
        if (n == 32) permit(c, s0 + (m < 11 ? 23 : 19));
        if (i == 26) { a2 = fmul1(a2, a2); PIN(a2); }
        cs_hook<25, true>(c, i, 2, cg, cu);
      });
      correct(G0, U0);
      STAMPB(11 + 20 * layer);
      pair_steps<false, 32, 64, 96, false>(c, q, B, G1, U1, [&](int i, int hf) __attribute__((always_inline)) { shadow(i, hf, 0, G0, U0); });
      correct(G1, U1);
      STAMPB(12 + 20 * layer);
      pf = ~c.tag;
      if (sopaque(m) >= 11) {
        tail(G1, U1);
        store_h(1, 0); store_h(1, 1);
        DRAIN();
        set_flag(c, fo + 64);
        wait_flags(c, fd, mk);
      }
      if (sopaque(m) < 11) {
        G0 = f32x16{}; U0 = f32x16{};
        pair_steps<false, 64, 96, 96, false>(c, q, B, G0, U0, [&](int i, int hf) __attribute__((always_inline)) { shadow(i, hf, 1, G1, U1); });
        correct(G0, U0);
        tail(G0, U0);
        store_h(2, 0); store_h(2, 1);
        STAMPB(13 + 20 * layer);
      }
      if (sopaque(m) >= 11) {
#pragma unroll
        for (int i = 0; i < 15; ++i) ldh(i);
      }
      sd = c.s;
      wide_steps<43, -3, 0>(c, d, B, aM, aO, kh, NoHook());
    }
    STAMPB(14 + 20 * layer);
    {
      unsigned pf2;
      auto hook = [&](int i, int hf) __attribute__((always_inline)) {
        if (hf == 0) {
          if (i == 7) { wait_vm<7>(); set_flag(c, fl2); }
          if (i == 13) pf2 = poll_issue(c, fd, 0x7ffull << 32);
          return;
        }
        if (i == 1) { poll_done<5>(c, pf, fd, mk << 16); if (layer) STAMPB(47); }
        if (i == 19 && layer) STAMPB(48);
        if (i == 31 && layer) STAMPB(50);
        if (i + 15 < 32) ldh(i + 15);
        if (i == 20) { poll_done<4>(c, pf2, fd, 0x7ffull << 32); if (layer) STAMPB(49); }
        if (i >= 20 && i <= 23) { ldh(2 * i - 8); ldh(2 * i - 7); }
        if (i >= 24 && i <= 26) ldh(i + 16);
        if (i == PKVAT) permit(c, sd + 11 + PKV);
        if (i == 36) {
          const v4u r0 = vld<v4u>(L_KVS + w * 2048 + lane * 16), r1 = vld<v4u>(L_KVS + w * 2048 + 1024 + lane * 16);
          R[0] = r0.x; R[1] = r0.y; R[2] = r0.z; R[3] = r0.w; R[4] = r1.x; R[5] = r1.y; R[6] = r1.z; R[7] = r1.w;
        }
      };
      wide_steps<43, 0, 43>(c, d, B, aM, aO, kh, hook);
      STAMPB(17 + 20 * layer);
      FRESH();
      finish_wide(c, aM, aO, R, 2 * layer + 2);
    }
#pragma unroll
    for (int i = 0; i < 32; ++i) asm volatile("" : "=v"(B[i]));
    asm volatile("" : "=v"(aK), "=v"(aV), "=v"(la), "=v"(mf), "=v"(ck), "=v"(cv));
    asm volatile("" : "=v"(ar[0]), "=v"(ar[1]), "=v"(ar[2]), "=v"(ar[3]));
    if (layer == 0) {
      publish_x(c, rs, R, WS_X2 + (tbw * 32 + 2 * cb) * 1024, WS_ST2 + (tbw * 16 + cb) * 256, WS_FX2 + tbw * 256 + cb * 4);
      STAMPB(18);
      FRESH();
      aK = f32x16{}; aV = f32x16{};
      Pipe q;
      const unsigned s0 = c.s;
      pair_steps<true, -PD, 0, 32, false>(c, q, B, aK, aV, NoHook());
      wait_flags(c, WS_FX2 + tbq * 256, 0xffffull);
      STAMPB(20);
      Stats sv;
      const int xo = WS_X2 + tbq * 32768;
#pragma unroll
      for (int i = 0; i < AH; ++i) B[i] = ld16(rs, lane * 16 + (i & 3) * 1024, xo + (((i >> 2) + ROT(m)) & 7) * 4096);
      SCHED();
      auto hook = [&](int i, int hf) __attribute__((always_inline)) {
        const int n = i + AH;
        stats_hook(c, sv, 2 * i + hf, rs, WS_ST2 + tbq * 4096, la, mf);
        if (hf == 0) {
          if (i == 25) vst<float>(L_SA + w * 128 + t32 * 4, la);
          if (i == 26) { ar[0] = vld<f32x4>(L_SA + w * 128 + 4 * h * 4); ar[1] = vld<f32x4>(L_SA + w * 128 + (8 + 4 * h) * 4); }
          if (i == 27) { ar[2] = vld<f32x4>(L_SA + w * 128 + (16 + 4 * h) * 4); ar[3] = vld<f32x4>(L_SA + w * 128 + (24 + 4 * h) * 4); }
          return;
        }
        if (n < 32) B[n] = ld16(rs, lane * 16 + (n & 3) * 1024, xo + (((n >> 2) + ROT(m)) & 7) * 4096);
        if (n == 32) permit(c, s0 + 14);
        cs_hook<26, true>(c, i, 0, ck, cv);
      };
      pair_steps<true, 0, 32, 32, false>(c, q, B, aK, aV, hook);
    }
  }
  {
    layer = 0;
    FRESH();
    GAS char* o = (GAS char*)P.out + (size_t)(tbw * 32 + t32) * 1024 + cb * 64 + h * 8;
#pragma unroll
    for (int g = 0; g < 4; ++g) *(GAS v2u*)(o + g * 16) = v2u{R[2 * g], R[2 * g + 1]};
  }
  STAMPB(38);
  STAMP_END();
  STAMPV(42, (unsigned long long)(BUDGET - c.budget));
  if (b == 0 && w == 0 && lane == 0) *(GAS unsigned*)(P.ws + WS_EPOCH) = c.tag;
}
}

extern "C" void kernel_launch(void* const* d_in, const int* in_sizes, int n_in, void* d_out, int out_size, void* d_ws,
                              size_t ws_size, hipStream_t stream) {
  Params P;
  P.x = (const char*)d_in[0]; P.wqkv = (const char*)d_in[1]; P.wo = (const char*)d_in[2];
  P.wg = (const char*)d_in[4]; P.wu = (const char*)d_in[5]; P.wd = (const char*)d_in[6];
  P.out = (char*)d_out; P.ws = (char*)d_ws;
  (void)hipFuncSetAttribute((const void*)fused_fwd, hipFuncAttributeMaxDynamicSharedMemorySize, LDS_BYTES);
  fused_fwd<<<256, 512, LDS_BYTES, stream>>>(P);
}
```

```cpp
#include <hip/hip_runtime.h>
#include <stdint.h>

#define GAS __attribute__((address_space(1)))
#define LAS __attribute__((address_space(3)))
#define DI __device__ __forceinline__
typedef unsigned v4u __attribute__((ext_vector_type(4)));
typedef unsigned v2u __attribute__((ext_vector_type(2)));
typedef float f32x16 __attribute__((ext_vector_type(16)));
typedef float f32x4 __attribute__((ext_vector_type(4)));
typedef float f32x2 __attribute__((ext_vector_type(2)));
typedef __bf16 bf16x8 __attribute__((ext_vector_type(8)));
typedef __bf16 bf16x2 __attribute__((ext_vector_type(2)));
typedef short s16x4 __attribute__((ext_vector_type(4)));

#define MAPMODE 1
#define LDEPTH 8
#define LPRIO 0
#define LSLEEP 1
#define AW 8
#define PKV 8
#define THIN 2
#define PD 2
#define PKVAT 16
#define THINB 18
#define KVP2 14
#define PACEB 10
#define AH 8

#define ROT(m) (((m) + 2 * p) & 7)
namespace {
constexpr int KB = 1024;
constexpr int WS_EPOCH = 0;
constexpr int WS_FKV = 4096;
constexpr int WS_FO = WS_FKV + 32 * KB;
constexpr int WS_FX1 = WS_FO + 32 * KB;
constexpr int WS_FH = WS_FX1 + 32 * KB;
constexpr int WS_FX2 = WS_FH + 32 * KB;
constexpr int WS_ST1 = WS_FX2 + 16 * KB;
constexpr int WS_ST2 = WS_ST1 + 512 * KB;
constexpr int KVW = 1152;
constexpr int WS_KVP = WS_ST2 + 256 * KB;
constexpr int WS_CSP = WS_KVP + 2 * 16 * 16 * 2 * KVW;
constexpr int WS_O = WS_CSP + 16 * 2 * 16 * 768;
constexpr int WS_X1 = WS_O + 4096 * KB;
constexpr int WS_X2 = WS_X1 + 4096 * KB;
constexpr int WS_H = WS_X2 + 2048 * KB;
constexpr int WS_RQ = WS_H + 2 * 64 * 86 * KB;
constexpr int WS_RG = WS_RQ + 1536 * KB;
constexpr int WS_RU = WS_RG + 43 * 32 * KB;
constexpr int WS_DBG = WS_RU + 43 * 32 * KB;
constexpr int WS_END = WS_DBG + 256 * 8 * 64 * 8;
constexpr int NSLOT = 8;
constexpr int L_RING = 0;
constexpr int L_SCR = NSLOT * 16 * KB;
constexpr int L_KVS = L_SCR + 16 * KB;
constexpr int L_CSF = L_KVS + 8 * KB;
constexpr int L_SA = L_CSF + 5120;
constexpr int L_KVR = L_SA + 512;
constexpr int L_ZR = L_KVR + 1024;
constexpr int L_ZS = L_ZR + 64;
constexpr int L_DUMMY = L_ZS + 512;
constexpr int L_RDY = L_DUMMY + 448;
constexpr int L_CSC = L_RDY + 32;
constexpr int L_FCNT = L_CSC + 32;
constexpr int L_F1 = L_FCNT + 16;
constexpr int L_F2 = L_F1 + 16;
constexpr int L_F3 = L_F2 + 16;
constexpr int L_RPD = L_F3 + 16;
constexpr int L_END = L_RPD + 16;
static_assert(L_END <= 160 * KB, "LDS");
constexpr int LDS_BYTES = (L_END + 15) / 16 * 16;
constexpr unsigned CODE_BYTES = 40960;
constexpr unsigned BUDGET = 1u << 18;

struct Params {
  const char* x; const char* wqkv; const char* wo; const char* wg; const char* wu; const char* wd;
  char* out; char* ws;
};

extern __shared__ __attribute__((aligned(16))) char smem[];
template <class T> DI LAS T* lp(unsigned off) { return (LAS T*)((LAS char*)smem + off); }
template <class T> DI T vld(unsigned off) { return *(volatile LAS T*)((LAS char*)smem + off); }
template <class T> DI void vst(unsigned off, T v) { *(volatile LAS T*)((LAS char*)smem + off) = v; }
DI unsigned rfl(unsigned v) { return __builtin_amdgcn_readfirstlane(v); }
#define SCHED() __builtin_amdgcn_sched_barrier(0)
#define CFENCE() asm volatile("" ::: "memory")
#define DRAIN() asm volatile("s_waitcnt vmcnt(0)" ::: "memory")
#define PIN(x) asm volatile("" : "+v"(x))

#define STAMP(i) do {} while (0)
#define STAMPV(i, v) do {} while (0)
#define STAMPB(i) do {} while (0)
#define STAMP_END() do {} while (0)

DI bf16x8 ldw(unsigned a, int second) {
  s16x4 lo = __builtin_amdgcn_ds_read_tr16_b64_v4i16(lp<s16x4>(a));
  s16x4 hi = __builtin_amdgcn_ds_read_tr16_b64_v4i16(lp<s16x4>(a + second));
  return __builtin_bit_cast(bf16x8, __builtin_shufflevector(lo, hi, 0, 1, 2, 3, 4, 5, 6, 7));
}
DI f32x16 mfma(bf16x8 a, bf16x8 b, f32x16 c) { return __builtin_amdgcn_mfma_f32_32x32x16_bf16(a, b, c, 0, 0, 0); }
DI bf16x8 asb(v4u v) { return __builtin_bit_cast(bf16x8, v); }
DI unsigned pk2(float lo, float hi) { bf16x2 r = {(__bf16)lo, (__bf16)hi}; return __builtin_bit_cast(unsigned, r); }
DI unsigned pk2(f32x2 v) { return pk2(v.x, v.y); }
DI float dot2(unsigned a, unsigned b, float c) { return __builtin_amdgcn_fdot2_f32_bf16(__builtin_bit_cast(bf16x2, a), __builtin_bit_cast(bf16x2, b), c, false); }
DI float u2f(unsigned u) { return __builtin_bit_cast(float, u); }
DI unsigned f2u(float f) { return __builtin_bit_cast(unsigned, f); }
DI float sel(bool c, float a, float b) { asm volatile("" : "+v"(a), "+v"(b)); return c ? a : b; }
DI int opaque(int v) { asm volatile("" : "+v"(v)); return v; }
DI int sopaque(int v) { asm volatile("" : "+s"(v)); return v; }
DI float blo(unsigned u) { return u2f(u << 16); }
DI float bhi(unsigned u) { return u2f(u & 0xffff0000u); }
DI float sum_halves(float v) {
  auto r = __builtin_amdgcn_permlane32_swap(f2u(v), f2u(v), false, false);
  float o; asm("v_add_f32 %0, %1, %2" : "=v"(o) : "v"(r[0]), "v"(r[1])); return o;
}
template <int CTRL> DI float dpp_add(float v) { return v + u2f(__builtin_amdgcn_update_dpp(0u, f2u(v), CTRL, 0xf, 0xf, false)); }
DI float row_sum(float v) { v = dpp_add<0x128>(v); v = dpp_add<0x124>(v); v = dpp_add<0x122>(v); return dpp_add<0x121>(v); }
DI f32x2 pair(const f32x16& a, int j) { return f32x2{a[2 * j], a[2 * j + 1]}; }
DI unsigned phi2(f32x2 x, f32x2 a) {
  const f32x2 e2 = x * (a * 1.4426950408889634f);
  const f32x2 t = x * a + 1e-6f;
  const f32x2 e = {__builtin_amdgcn_fmed3f(__builtin_amdgcn_exp2f(e2.x), 0.f, 1.f), __builtin_amdgcn_fmed3f(__builtin_amdgcn_exp2f(e2.y), 0.f, 1.f)};
  const f32x2 mx = {fmaxf(t.x, 1e-6f), fmaxf(t.y, 1e-6f)};
  return pk2(mx + e);
}
constexpr unsigned ONES = 0x3f803f80u;
DI float fadd1(float a, float b) { float r; asm("v_add_f32 %0, %1, %2" : "=v"(r) : "v"(a), "v"(b)); return r; }
DI float fmul1(float a, float b) { float r; asm("v_mul_f32 %0, %1, %2" : "=v"(r) : "v"(a), "v"(b)); return r; }

typedef __amdgpu_buffer_rsrc_t rsrc_t;
DI v4u ld16(rsrc_t rs, int voff, int soff) { return __builtin_amdgcn_raw_buffer_load_b128(rs, voff, soff, 16); }
DI v2u ld8(rsrc_t rs, int voff, int soff) { return __builtin_amdgcn_raw_buffer_load_b64(rs, voff, soff, 16); }
DI void st16(rsrc_t rs, v4u v, int voff, int soff) {
  __builtin_amdgcn_raw_buffer_store_b128(v, rs, voff, soff, 16);
  asm volatile("s_nop 1" ::"v"(v) : "memory");
}
DI void st8(rsrc_t rs, v2u v, int voff, int soff) { __builtin_amdgcn_raw_buffer_store_b64(v, rs, voff, soff, 16); }

struct CW {
  unsigned s, pos, rdy;
  unsigned lane_tr;
  unsigned fa;
  unsigned tag, perm;
  int budget;
  int lane, w;
  char* ws;
};
DI bool spent(CW& c) { c.budget -= 1; return c.budget < 0; }
DI unsigned min4(v4u v) { return rfl(min(min(v.x, v.y), min(v.z, v.w))); }
DI void wait_count(CW& c, unsigned have, unsigned addr, unsigned need) {
  if (__builtin_expect(rfl(have) < need, 0)) {
    for (;;) { if (rfl(vld<unsigned>(addr)) >= need || spent(c)) break; }
  }
}
DI void peek(CW& c, unsigned ahead) { c.rdy = vld<unsigned>(L_RDY + ((c.pos + ahead) & (NSLOT - 1)) * 4); }
DI void wait_full(CW& c) { wait_count(c, c.rdy, L_RDY + c.pos * 4, (c.s / NSLOT + 1) * 4); }
DI void post(CW& c, int word, unsigned v) { vst<unsigned>(c.fa + (word - L_FCNT), v); }
DI void release(CW& c) { c.s += 1; c.pos = (c.pos + 1) & (NSLOT - 1); post(c, L_FCNT, min(c.s + NSLOT, c.perm)); }
DI void permit(CW& c, unsigned v) { c.perm = v; post(c, L_FCNT, min(c.s + NSLOT, v)); }
DI void set_flag(CW& c, int off) {
  const char* base = c.ws + off;
  asm volatile("s_mov_b64 exec, 1\n\ts_nop 4\n\tglobal_atomic_swap %0, %1, %2\n\ts_mov_b64 exec, -1" ::"v"(0u), "v"(c.tag), "s"(base) : "memory");
}
DI void wait_flags(CW& c, int off, unsigned long long mask) {
  unsigned f, bud = rfl(c.budget);
  const unsigned voff = c.lane * 4;
  const char* base = c.ws + off;
  asm volatile(
      "s_mov_b64 exec, %5\n\ts_nop 4\n\t"
      "1:\n\t"
      "global_atomic_or %0, %2, %6, %3 sc0\n\t"
      "s_waitcnt vmcnt(0)\n\t"
      "v_cmp_ne_u32 vcc, %4, %0\n\t"
      "s_cbranch_vccz 2f\n\t"
      "s_sub_i32 %1, %1, 8\n\t"
      "s_cmp_ge_i32 %1, 0\n\t"
      "s_cbranch_scc1 1b\n\t"
      "2:\n\t"
      "s_mov_b64 exec, -1\n\t"
      : "=&v"(f), "+s"(bud) : "v"(voff), "s"(base), "s"(c.tag), "s"(mask), "v"(0u) : "memory", "vcc", "scc");
  c.budget = rfl(bud);
}
DI unsigned poll_issue(CW& c, int off, unsigned long long mask) {
  unsigned f = c.tag;
  const unsigned voff = c.lane * 4;
  const char* base = c.ws + off;
  asm volatile("s_mov_b64 exec, %3\n\tv_not_b32 %0, %0\n\ts_nop 4\n\tglobal_atomic_or %0, %1, %4, %2 sc0\n\ts_mov_b64 exec, -1"
               : "+v"(f) : "v"(voff), "s"(base), "s"(mask), "v"(0u) : "memory");
  return f;
}
template <int N> DI void poll_done(CW& c, unsigned f, int off, unsigned long long mask) {
  asm volatile("s_waitcnt vmcnt(%c1)" : "+v"(f) : "i"(N) : "memory");
  if (__builtin_expect(__builtin_amdgcn_ballot_w64(f != c.tag) != 0ull, 0)) wait_flags(c, off, mask);
}

template <int V> struct IC { static constexpr int value = V; DI constexpr operator int() const { return V; } };
template <int I, int E, class F> DI void static_for(F&& f) { if constexpr (I < E) { f(IC<I>{}); static_for<I + 1, E>(f); } }
struct Pipe { bf16x8 wa[PD + 1], wb[PD + 1]; unsigned ad, ad2, adn, need, pn, have, rel; };
#define SPIN(x) asm volatile("" : "+s"(x))
DI void slot_first(CW& c, Pipe& q) { peek(c, 0); wait_full(c); q.adn = c.lane_tr + c.pos * 16384u; }
template <int K, bool NX> DI void book(CW& c, Pipe& q) {
  if constexpr (NX && K == 0) { q.pn = (c.pos + 1) & (NSLOT - 1); c.rdy = vld<unsigned>(L_RDY + q.pn * 4); }
  if constexpr (NX && K == 1) { q.need = ((c.s + 1) / NSLOT + 1) * 4; SPIN(q.need); }
  if constexpr (NX && K == 2) { q.adn = c.lane_tr + q.pn * 16384u; PIN(q.adn); }
  if constexpr (NX && K == 3) { q.have = rfl(c.rdy); SPIN(q.have); }
  if constexpr (NX && K == 4) wait_count(c, q.have, L_RDY + q.pn * 4, q.need);
  if constexpr (K == 5) { c.s += 1; c.pos = (c.pos + 1) & (NSLOT - 1); q.rel = min(c.s + NSLOT, c.perm); PIN(q.rel); }
  if constexpr (K == 6) post(c, L_FCNT, q.rel);
}
template <bool SWAP, int I0, int I1, int NEND, bool MORE, class HOOK>
DI void pair_steps(CW& c, Pipe& q, v4u (&B)[32], f32x16& acc0, f32x16& acc1, HOOK hook) {
  static_for<I0, I1>([&](auto ic) __attribute__((always_inline)) {
    constexpr int i = ic.value, n = i + PD;
    constexpr bool F = n < NEND, NX = F && (MORE || n + 8 - n % 8 < NEND);
    if constexpr (n == 0) slot_first(c, q);
    if constexpr (i >= 0) acc0 = SWAP ? mfma(asb(B[i % 32]), q.wa[i % (PD + 1)], acc0) : mfma(q.wa[i % (PD + 1)], asb(B[i % 32]), acc0);
    SCHED();
    if constexpr (F && n % 8 == 0) q.ad = q.adn;
    if constexpr (F) q.wa[n % (PD + 1)] = ldw(q.ad + (n % 8) * 1024, 512);
    if constexpr (F && n % 8 >= 2) book<n % 8 - 2, NX>(c, q);
    hook(IC<(i < 0 ? i : i % 32)>{}, IC<0>{});
    SCHED();
    if constexpr (i >= 0) acc1 = SWAP ? mfma(asb(B[i % 32]), q.wb[i % (PD + 1)], acc1) : mfma(q.wb[i % (PD + 1)], asb(B[i % 32]), acc1);
    SCHED();
    if constexpr (F) q.wb[n % (PD + 1)] = ldw(q.ad + 8192 + (n % 8) * 1024, 512);
    if constexpr (F && n % 8 == 7) book<6, NX>(c, q);
    hook(IC<(i < 0 ? i : i % 32)>{}, IC<1>{});
    SCHED();
  });
}
template <bool SWAP, class HOOK>
DI void gemm_pair(CW& c, v4u (&B)[32], f32x16& acc0, f32x16& acc1, HOOK hook) {
  Pipe q;
  pair_steps<SWAP, -PD, 32, 32, false>(c, q, B, acc0, acc1, hook);
}
template <class HOOK>
DI void gemm_single(CW& c, v4u (&B)[32], f32x16& acc, HOOK hook) {
  bf16x8 wa[5];
  Pipe q;
  static_for<-4, 32>([&](auto ic) __attribute__((always_inline)) {
    constexpr int i = ic.value, n = i + 4;
    if constexpr (n == 0) slot_first(c, q);
    if constexpr (i >= 0) acc = mfma(wa[i % 5], asb(B[i]), acc);
    SCHED();
    if constexpr (n < 32) {
      if constexpr (n % 16 == 0) q.ad = q.adn;
      wa[n % 5] = ldw(q.ad + (n % 16) * 1024, 512);
      if constexpr (n % 16 == 6) book<0, (n < 16)>(c, q);
      if constexpr (n % 16 >= 10) book<n % 16 - 9, (n < 16)>(c, q);
    }
    hook(ic);
    SCHED();
  });
}
template <int PW> struct Wide { bf16x8 wa[PW + 1], wb[PW + 1]; Pipe q; unsigned dO, lt; };
template <int NK, int I0, int I1, int PW, class HOOK>
DI void wide_steps(CW& c, Wide<PW>& d, v4u (&B)[32], f32x16& accM, f32x16& accO, int kh, HOOK hook) {
  Pipe& q = d.q;
  const unsigned keep = c.lane_tr;
  if constexpr (I0 == -PW) { d.lt = c.lane_tr + kh * (8192u + 512u); d.dO = kh ? -512u : 512u; }
  c.lane_tr = d.lt;
  static_for<I0, I1>([&](auto ic) __attribute__((always_inline)) {
    constexpr int i = ic.value, n = i + PW;
    constexpr bool F = n < NK, NX = F && n + 4 - n % 4 < NK;
    if constexpr (n == 0) slot_first(c, q);
    if constexpr (i >= 0) accO = mfma(d.wb[i % (PW + 1)], asb(B[i % 16]), accO);
    SCHED();
    if constexpr (F && n % 4 == 0) { q.ad = q.adn; if constexpr (n + 3 == NK) q.ad -= kh * 2048u; q.ad2 = q.ad + d.dO; }
    if constexpr (F) d.wb[n % (PW + 1)] = ldw(q.ad2 + (n % 4) * 2048, 1024);
    if constexpr (F && n % 4 == 1) { book<1, NX>(c, q); }
    if constexpr (F && n % 4 == 2) { book<2, NX>(c, q); book<3, NX>(c, q); }
    if constexpr (F && (n % 4 == 3 || n == NK - 1)) { book<5, NX>(c, q); }
    hook(ic, IC<0>{});
    SCHED();
    if constexpr (i >= 0) accM = mfma(d.wa[i % (PW + 1)], asb(B[i % 16]), accM);
    SCHED();
    if constexpr (F) d.wa[n % (PW + 1)] = ldw(q.ad + (n % 4) * 2048, 1024);
    if constexpr (F && n % 4 == 0) book<0, NX>(c, q);
    if constexpr (F && n % 4 == 2) book<4, NX>(c, q);
    if constexpr (F && (n % 4 == 3 || n == NK - 1)) book<6, NX>(c, q);
    hook(ic, IC<1>{});
    SCHED();
  });
  c.lane_tr = keep;
}
struct NoHook { template <class A, class B> DI void operator()(A, B) const {} };

DI void ln_factors(float sum, float sq, float& a, unsigned& mf, bool up) {
  float mean = sum * (1.0f / 512.0f);
  PIN(mean);
  const float var = fmaxf(sq * (1.0f / 512.0f) - mean * mean, 0.f);
  a = __builtin_amdgcn_rsqf(var + 1e-5f);
  const unsigned hi = pk2(-mean, -mean);
  const float lo = -mean - blo(hi);
  mf = up ? pk2(lo, lo) : hi;
}
struct Stats { v4u sv[4]; f32x4 t; f32x2 u[4]; float mean, var; };
constexpr int K0 = 34;
DI void stats_hook(CW& c, Stats& t, int k, rsrc_t rs, int soff, float& a, unsigned& mf) {
  if (k == 3 || k == 7 || k == 11 || k == 15) t.sv[k >> 2] = ld16(rs, c.lane * 16 + (k >> 2) * 1024, soff);
  const unsigned sc = L_SCR + (c.w ^ 2) * 4096;
  if (k >= K0 && k < K0 + 4) { const int e = k - K0; t.t[e] = fadd1(fadd1(u2f(t.sv[0][e]), u2f(t.sv[1][e])), fadd1(u2f(t.sv[2][e]), u2f(t.sv[3][e]))); PIN(t.t[e]); }
  if (k == K0 + 4) vst<f32x4>(sc + c.lane * 16, t.t);
  if (k == K0 + 5) { t.u[0] = vld<f32x2>(sc + (c.lane & 31) * 8); t.u[1] = vld<f32x2>(sc + 256 + (c.lane & 31) * 8); }
  if (k == K0 + 6) { t.u[2] = vld<f32x2>(sc + 512 + (c.lane & 31) * 8); t.u[3] = vld<f32x2>(sc + 768 + (c.lane & 31) * 8); }
  if (k == K0 + 10) { t.mean = fadd1(fadd1(t.u[0].x, t.u[1].x), fadd1(t.u[2].x, t.u[3].x)); PIN(t.mean); }
  if (k == K0 + 11) { t.var = fadd1(fadd1(t.u[0].y, t.u[1].y), fadd1(t.u[2].y, t.u[3].y)); PIN(t.var); }
  if (k == K0 + 12) { t.mean *= -1.0f / 512.0f; PIN(t.mean); t.var = t.var * (1.0f / 512.0f) + 1e-5f; PIN(t.var); }
  if (k == K0 + 13) { t.var = fmaxf(t.var - t.mean * t.mean, 1e-5f); PIN(t.var); a = __builtin_amdgcn_rsqf(t.var); PIN(a); }
  if (k == K0 + 14) { mf = pk2(t.mean, t.mean); t.mean -= blo(mf); PIN(mf); PIN(t.mean); }
  if (k == K0 + 15) { mf = c.lane >= 32 ? pk2(t.mean, t.mean) : mf; PIN(mf); }
}
DI void finish_wide(CW& c, const f32x16& accM, const f32x16& accO, unsigned (&R)[8], unsigned seq) {
  const unsigned mine = L_SCR + c.w * 4096 + c.lane * 16, other = L_SCR + (c.w ^ 2) * 4096 + c.lane * 16;
#pragma unroll
  for (int g = 0; g < 4; ++g) vst<f32x4>(mine + g * 1024, f32x4{accO[4 * g], accO[4 * g + 1], accO[4 * g + 2], accO[4 * g + 3]});
  post(c, L_F3, seq);
  f32x2 y[8];
#pragma unroll
  for (int j = 0; j < 8; ++j) { y[j] = pair(accM, j) + f32x2{blo(R[j]), bhi(R[j])}; PIN(y[j]); }
  f32x4 v[4];
  for (;;) {
    const unsigned fl = vld<unsigned>(L_F3 + (c.w ^ 2) * 4);
#pragma unroll
    for (int g = 0; g < 4; ++g) v[g] = vld<f32x4>(other + g * 1024);
    if (rfl(fl) >= seq || spent(c)) break;
  }
#pragma unroll
  for (int g = 0; g < 4; ++g) {
    R[2 * g] = pk2(y[2 * g] + f32x2{v[g].x, v[g].y});
    R[2 * g + 1] = pk2(y[2 * g + 1] + f32x2{v[g].z, v[g].w});
  }
}
DI void publish_x(CW& c, rsrc_t rs, const unsigned (&R)[8], int xoff, int stoff, int floff) {
  st16(rs, v4u{R[0], R[1], R[2], R[3]}, c.lane * 16, xoff);
  st16(rs, v4u{R[4], R[5], R[6], R[7]}, c.lane * 16 + 1024, xoff);
  float s0 = 0.f, q0 = 0.f, s1 = 0.f, q1 = 0.f;
#pragma unroll
  for (int i = 0; i < 4; ++i) { s0 = dot2(R[i], ONES, s0); q0 = dot2(R[i], R[i], q0); s1 = dot2(R[4 + i], ONES, s1); q1 = dot2(R[4 + i], R[4 + i], q1); }
  const float s = sum_halves(s0 + s1), q = sum_halves(q0 + q1);
  st8(rs, v2u{f2u(s), f2u(q)}, (c.lane & 31) * 8, stoff);
  DRAIN();
  set_flag(c, floff);
}

DI void dma4(const char* base, unsigned v0, unsigned v1, unsigned v2, unsigned v3, unsigned dst) {
  asm volatile("s_mov_b32 m0, %5\n\ts_nop 3\n\t"
               "global_load_lds_dwordx4 %0, %4\n\tglobal_load_lds_dwordx4 %1, %4 offset:1024\n\t"
               "global_load_lds_dwordx4 %2, %4 offset:2048\n\tglobal_load_lds_dwordx4 %3, %4 offset:3072"
               ::"v"(v0), "v"(v1), "v"(v2), "v"(v3), "s"(base), "s"(dst) : "memory");
}
#define PACE "s_sleep 1\n\t"
DI void dma4p(const char* base, unsigned v0, unsigned v1, unsigned v2, unsigned v3, unsigned dst) {
  asm volatile("s_mov_b32 m0, %5\n\ts_nop 3\n\t"
               "global_load_lds_dwordx4 %0, %4\n\t" PACE "global_load_lds_dwordx4 %1, %4 offset:1024\n\t" PACE
               "global_load_lds_dwordx4 %2, %4 offset:2048\n\t" PACE "global_load_lds_dwordx4 %3, %4 offset:3072\n\t" PACE
               ::"v"(v0), "v"(v1), "v"(v2), "v"(v3), "s"(base), "s"(dst) : "memory");
}
DI void dma4_sc1(const char* base, unsigned v, unsigned dst) {
  asm volatile("s_mov_b32 m0, %2\n\ts_nop 3\n\t"
               "global_load_lds_dwordx4 %0, %1 sc1\n\t" PACE "global_load_lds_dwordx4 %0, %1 offset:1024 sc1\n\t" PACE
               "global_load_lds_dwordx4 %0, %1 offset:2048 sc1\n\t" PACE "global_load_lds_dwordx4 %0, %1 offset:3072 sc1\n\t" PACE
               ::"v"(v), "s"(base), "s"(dst) : "memory");
}
DI void dma4_sc1f(const char* base, unsigned v, unsigned dst) {
  asm volatile("s_mov_b32 m0, %2\n\ts_nop 3\n\t"
               "global_load_lds_dwordx4 %0, %1 sc1\n\tglobal_load_lds_dwordx4 %0, %1 offset:1024 sc1\n\t"
               "global_load_lds_dwordx4 %0, %1 offset:2048 sc1\n\tglobal_load_lds_dwordx4 %0, %1 offset:3072 sc1"
               ::"v"(v), "s"(base), "s"(dst) : "memory");
}
template <int N> DI void wait_vm() { asm volatile("s_waitcnt vmcnt(%c0)" ::"i"(N) : "memory"); }
DI unsigned tr_lane(int lane) { return (4 * (lane >> 5) + ((lane & 15) >> 2)) * 64 + ((lane >> 4) & 1) * 32 + (lane & 3) * 8; }
DI int fresh_lane() { int l; asm volatile("v_mbcnt_lo_u32_b32 %0, -1, 0\n\tv_mbcnt_hi_u32_b32 %0, -1, %0" : "=v"(l)); return l; }
#define LSTAMP(v, s) do {} while (0)
DI void lds_add(unsigned pa, unsigned off, unsigned n) { CFENCE(); (void)__hip_atomic_fetch_add(lp<unsigned>(pa + off), n, __ATOMIC_RELAXED, __HIP_MEMORY_SCOPE_WORKGROUP); CFENCE(); }
DI void loader(const Params& P, int p, int m, int lw) {
  const int lane = fresh_lane();
  const unsigned smem_base = (unsigned)(uintptr_t)(LAS char*)smem;
  const unsigned nr = m < 11 ? 3 : 2, NS = 21 + 4 * nr, NT = 2 * NS;
  const unsigned v_lin = lane * 16, v_wide = ((lane & 31) >> 2) * 1024 + (lane >> 5) * 64 + (lane & 3) * 16, v_raw = (lane >> 2) * 3072 + (lane & 3) * 16;
  const unsigned pa = lane == 0 ? L_RDY : L_DUMMY + lane * 4;
  __builtin_amdgcn_s_setprio(LPRIO);
  unsigned limit = 0;
  int budget = BUDGET;
  bool pend = false;
  for (unsigned s = 4 + lw; s < NT; s += 2) {
    const bool l2 = s >= NS;
    const unsigned i = l2 ? s - NS : s, g = i - 10u, d = g - 4u * nr, r4 = 4u * ROT(m);
    const char* src[4];
    unsigned kind;
    if (i < 6u) {
      kind = l2 ? 1u : 0u;
#pragma unroll
      for (unsigned q = 0; q < 4; ++q) {
        const unsigned blk = i < 4u ? q >> 1 : 2u, ks0 = ((i < 4u ? 8u * i + (q & 1) * 4u : 16u * (i - 4u) + q * 4u) + r4) & 31u;
        src[q] = l2 ? P.ws + WS_RQ + ((m * 3 + blk) * 32u + ks0) * 1024u : P.wqkv + m * 64 + ks0 * (16u * 3072u);
      }
    } else if (i < 10u) {
      kind = 2u;
#pragma unroll
      for (unsigned q = 0; q < 4; ++q) src[q] = P.wo + ((i - 6u) * 128u + q * 32u) * 1024u + (m & 7) * 128;
    } else if (g < 4u * nr) {
      kind = 1u;
#pragma unroll
      for (unsigned q = 0; q < 4; ++q) src[q] = P.ws + ((q >> 1) ? WS_RU : WS_RG) + ((16u * (g >> 2) + m) * 32u + ((8u * (g & 3u) + (q & 1) * 4u + r4) & 31u)) * 1024u;
    } else {
      kind = 2u;
#pragma unroll
      for (unsigned q = 0; q < 4; ++q) src[q] = P.wd + min((d < 8u ? (d & 4u) | ((d + ROT(m)) & 3u) : d) * 128u + q * 32u, 1344u) * 1024u + (m & 7) * 128;
    }
    if (pend && i < THINB) { wait_vm<0>(); lds_add(pa, ((s - 2) & (NSLOT - 1)) * 4, 4); LSTAMP(tlan, s - 2); pend = false; }
    while (s >= limit) {
      limit = min4(vld<v4u>(L_FCNT));
      if (s < limit) break;
      if (pend) { wait_vm<0>(); lds_add(pa, ((s - 2) & (NSLOT - 1)) * 4, 4); LSTAMP(tlan, s - 2); pend = false; continue; }
      __builtin_amdgcn_s_sleep(LSLEEP);
      if (--budget < 0) break;
    }
    LSTAMP(tiss, s);
    const unsigned dst = smem_base + L_RING + (s & (NSLOT - 1)) * 16384u;
    const unsigned v0 = kind == 2u ? v_wide : v_raw, st = kind == 2u ? 7168u : 48128u;
    if (i >= PACEB && i < 18u && s >= 6u) {
      if (kind == 1u) { dma4_sc1(src[0], v_lin, dst); dma4_sc1(src[1], v_lin, dst + 4096); dma4_sc1(src[2], v_lin, dst + 8192); dma4_sc1(src[3], v_lin, dst + 12288); }
      else {
#pragma unroll
        for (int q = 0; q < 4; ++q) dma4p(src[q], v0, v0 + st, v0 + 2 * st, v0 + 3 * st, dst + q * 4096);
      }
    } else {
      if (kind == 1u) { dma4_sc1f(src[0], v_lin, dst); dma4_sc1f(src[1], v_lin, dst + 4096); dma4_sc1f(src[2], v_lin, dst + 8192); dma4_sc1f(src[3], v_lin, dst + 12288); }
      else {
#pragma unroll
        for (int q = 0; q < 4; ++q) dma4(src[q], v0, v0 + st, v0 + 2 * st, v0 + 3 * st, dst + q * 4096);
      }
    }
    if (pend) { wait_vm<16>(); lds_add(pa, ((s - 2) & (NSLOT - 1)) * 4, 4); LSTAMP(tlan, s - 2); }
    LSTAMP(tis2, s);
    pend = true;
    if (s < 6u) {
      wait_vm<0>(); lds_add(pa, s * 4, 4); LSTAMP(tlan, s); pend = false;
      const unsigned a = tr_lane(lane) + s * 16384u;
      float c0 = 0.f, c1 = 0.f;
#pragma unroll
      for (int n = 0; n < 16; ++n) {
        const v4u f = __builtin_bit_cast(v4u, ldw(a + n * 1024, 512));
        c0 = dot2(f.x, ONES, c0); c1 = dot2(f.y, ONES, c1); c0 = dot2(f.z, ONES, c0); c1 = dot2(f.w, ONES, c1);
      }
      const float t = sum_halves(c0 + c1);
      vst<unsigned>(L_CSF + 2 * 512 + (lane & 31) * 16 + lw * 4, pk2(t, t - blo(pk2(t, t))));
      lds_add(pa, L_CSC - L_RDY + 4, 1);
    }
    if (s + 2 >= NT) { wait_vm<0>(); lds_add(pa, (s & (NSLOT - 1)) * 4, 4); }
  }
}
DI void server(const Params& P, int p, int m, int sw) {
  const int lane = fresh_lane();
  unsigned epoch;
  asm volatile("s_load_dword %0, %1, 0x0" : "=s"(epoch) : "s"(P.ws) : "memory");
  const unsigned pa = lane == 0 ? L_RDY : L_DUMMY + lane * 4;
  const rsrc_t rs = __builtin_amdgcn_make_buffer_rsrc((void*)P.ws, 0, WS_END, 0x00020000);
  CW c;
  c.lane = lane; c.ws = P.ws; c.budget = BUDGET;
  asm volatile("s_waitcnt lgkmcnt(0)" : "+s"(epoch) :: "memory");
  c.tag = epoch + 1u;
  const int co = WS_CSP + (m * 2 + sw) * 16 * 768;
  {
    const int ks = 2 * p + sw, r2 = m < 11 ? 2 : 1;
    const GAS char* sq = (const GAS char*)P.wqkv + m * 64 + ks * (16 * 3072) + (lane >> 2) * 3072 + (lane & 3) * 16;
    const unsigned vg = m * 64 + ks * (16 * 2752) + (lane >> 2) * 2752 + (lane & 3) * 16;
    const GAS char* sg = (const GAS char*)P.wg + vg; const GAS char* su = (const GAS char*)P.wu + vg;
    v4u rp[9];
    rp[0] = *(const GAS v4u*)(sg); rp[1] = *(const GAS v4u*)(su); rp[2] = *(const GAS v4u*)(sg + 1024); rp[3] = *(const GAS v4u*)(su + 1024);
    rp[4] = *(const GAS v4u*)(sg + r2 * 1024); rp[5] = *(const GAS v4u*)(su + r2 * 1024);
    rp[6] = *(const GAS v4u*)(sq + 1024); rp[7] = *(const GAS v4u*)(sq + 2048); rp[8] = *(const GAS v4u*)(sq);
    SCHED();
    const int dg = (m * 32 + ks) * 1024, dq = WS_RQ + (m * 96 + ks) * 1024;
    st16(rs, rp[0], lane * 16, WS_RG + dg); st16(rs, rp[1], lane * 16, WS_RU + dg);
    st16(rs, rp[2], lane * 16, WS_RG + dg + 16 * 32768); st16(rs, rp[3], lane * 16, WS_RU + dg + 16 * 32768);
    st16(rs, rp[4], lane * 16, WS_RG + dg + r2 * (16 * 32768)); st16(rs, rp[5], lane * 16, WS_RU + dg + r2 * (16 * 32768));
    st16(rs, rp[6], lane * 16, dq); st16(rs, rp[7], lane * 16, dq + 32768); st16(rs, rp[8], lane * 16, dq + 65536);
    const unsigned SCX = L_CSF + 3072 + sw * 1024;
    float cs[6];
#pragma unroll
    for (int u = 0; u < 6; ++u) {
      vst<v4u>(SCX + lane * 16, rp[u]);
      CFENCE();
      const v4u f = __builtin_bit_cast(v4u, ldw(SCX + tr_lane(lane), 512));
      CFENCE();
      cs[u] = sum_halves(dot2(f.x, ONES, dot2(f.y, ONES, 0.f)) + dot2(f.z, ONES, dot2(f.w, ONES, 0.f)));
    }
#pragma unroll
    for (int u = 0; u < 3; ++u) __builtin_amdgcn_raw_buffer_store_b32(f2u(lane < 32 ? cs[2 * u] : cs[2 * u + 1]), rs, lane * 4 + u * 256, co + p * 768, 16);
    vst<v4u>(SCX + lane * 16, v4u{0u, 0u, 0u, 0u});
    {
      const unsigned b = blockIdx.x;
      const char* tb = sw ? P.wo : P.wd;
      const unsigned to = sw ? (b * 16u + (lane & 15)) * 128u : (b * 43u + min(lane, 42)) * 128u;
      asm volatile("s_mov_b32 m0, %2\n\ts_nop 3\n\tglobal_load_lds_dword %0, %1" :: "v"(to), "s"(tb), "s"((unsigned)(uintptr_t)(LAS char*)smem + L_DUMMY) : "memory");
    }
    DRAIN();
    lds_add(pa, L_RPD - L_RDY, 1);
    while (rfl(vld<unsigned>(L_RPD)) < 2u && !spent(c)) __builtin_amdgcn_s_sleep(1);
  }
  for (unsigned layer = 0; layer < 2; ++layer) {
    __builtin_amdgcn_s_setprio(3);
    f32x4 tk[4], tz[4];
    for (;;) {
      const v4u fl = vld<v4u>(L_F1);
      if (min4(fl) > layer || spent(c)) break;
      __builtin_amdgcn_s_sleep(1);
    }
#pragma unroll
    for (int u = 0; u < 4; ++u) { tk[u] = vld<f32x4>(L_KVS + u * 2048 + sw * 1024 + lane * 16); tz[u] = vld<f32x4>(L_ZS + u * 128 + (lane & 3) * 32 + sw * 16); }
    const int go = WS_KVP + ((layer * 16 + m) * 2 + sw) * 16 * KVW, fo = WS_FKV + ((layer * 16 + m) * 2 + sw) * 256;
    v4u sk, sz;
#pragma unroll
    for (int e = 0; e < 4; ++e) { sk[e] = f2u(fadd1(fadd1(tk[0][e], tk[1][e]), fadd1(tk[2][e], tk[3][e]))); sz[e] = f2u(fadd1(fadd1(tz[0][e], tz[1][e]), fadd1(tz[2][e], tz[3][e]))); }
    st16(rs, sk, lane * 16, go + p * KVW);
    st16(rs, sz, 1024 + (lane & 7) * 16, go + p * KVW);
    DRAIN();
    set_flag(c, fo + p * 4);
    wait_flags(c, fo, 0xffffull);
    const v4u zl = ld16(rs, 1024 + (lane & 15) * KVW + (lane >> 4) * 16, go);
    v4u g[16];
#pragma unroll
    for (int i = 0; i < 16; ++i) g[i] = ld16(rs, lane * 16, go + i * KVW);
    SCHED();
    const float z0 = row_sum(u2f(zl.x)), z1 = row_sum(u2f(zl.y)), z2 = row_sum(u2f(zl.z)), z3 = row_sum(u2f(zl.w));
    if ((lane & 15) == 0) vst<v2u>(L_ZR + (lane >> 4) * 16 + sw * 8, v2u{pk2(z0, z1), pk2(z2, z3)});
    f32x4 r = __builtin_bit_cast(f32x4, g[0]);
#pragma unroll
    for (int i = 1; i < 16; ++i) r += __builtin_bit_cast(f32x4, g[i]);
    vst<v2u>(L_KVR + lane * 16 + sw * 8, v2u{pk2(r.x, r.y), pk2(r.z, r.w)});
    lds_add(pa, L_F2 - L_RDY, 2);
    __builtin_amdgcn_s_setprio(0);
    if (layer == 0) {
      v4u t[16];
#pragma unroll
      for (int i = 0; i < 16; ++i) t[i] = ld16(rs, min(lane, 47) * 16, co + i * 768);
      SCHED();
      f32x4 v = __builtin_bit_cast(f32x4, t[0]);
#pragma unroll
      for (int i = 1; i < 16; ++i) v += __builtin_bit_cast(f32x4, t[i]);
      if (lane < 48) {
#pragma unroll
        for (int e = 0; e < 4; ++e) vst<unsigned>(L_CSF + 2048 + (lane * 4 + e) * 16 + sw * 4, pk2(v[e], v[e] - blo(pk2(v[e], v[e]))));
      }
      lds_add(pa, L_CSC - L_RDY + 8, 2); lds_add(pa, L_CSC - L_RDY + 12, 2); lds_add(pa, L_CSC - L_RDY + 16, 2);
    }
  }
}
template <int AT, bool TWO> DI void cs_hook(CW& c, int i, unsigned k, v4u& a, v4u& b) {
  if (i == AT) a.x = vld<unsigned>(L_CSC + k * 4);
  if (i == AT + 3) {
    wait_count(c, a.x, L_CSC + k * 4, TWO ? 4u : 2u);
    a = vld<v4u>(L_CSF + k * 1024 + (c.lane & 31) * 16);
    if (TWO) b = vld<v4u>(L_CSF + k * 1024 + 512 + (c.lane & 31) * 16);
  }
}

__global__ void __launch_bounds__(512, 2) fused_fwd(Params P) {
  const int tid = threadIdx.x; int lane = tid & 63; const int wv = __builtin_amdgcn_readfirstlane(tid >> 6);
  const int b = blockIdx.x;
  {
    const unsigned co = CODE_BYTES - 256u - min(((b >> 3) * 8 + wv) * 256u, CODE_BYTES - 256u) + lane * 4;
    unsigned long long pc;
    asm volatile("s_mov_b32 m0, %2\n\ts_getpc_b64 %0\n\tglobal_load_lds_dword %1, %0" : "=&s"(pc) : "v"(co), "s"((unsigned)(uintptr_t)(LAS char*)smem + L_DUMMY) : "memory");
  }
  int p, m;
  if (MAPMODE == 0) { p = (b & 7) * 2 + (b >> 7); m = (b >> 3) & 15; }
  else if (MAPMODE == 1) { const int x = b & 7, i = b >> 3; p = (x >> 1) * 4 + (i >> 3); m = (x & 1) * 8 + (i & 7); }
  else { const int x = b & 7, i = b >> 3; p = i >> 1; m = x * 2 + (i & 1); }
  if (tid < (L_END - L_RDY) / 4) vst<unsigned>(L_RDY + tid * 4, 0u);
  if (tid < 320) vst<v4u>(L_CSF + tid * 16, v4u{0u, 0u, 0u, 0u});
  asm volatile("s_waitcnt lgkmcnt(0)\n\ts_barrier" ::: "memory");
  const unsigned smem_base = (unsigned)(uintptr_t)(LAS char*)smem;
  int w = wv & 3;
  unsigned xv[4];
  const char* xbase = (wv < 4 ? P.x : P.wqkv) + (unsigned)sopaque(wv < 4 ? (4 * p + w) * 32768 : ((w >> 1) + 1) * 1024 + m * 64);
  auto xbuf = [&](int q) -> unsigned { const int j = q % 5; return (j == 0 ? L_SCR : j == 1 ? 7 * 16384 : j == 2 ? 6 * 16384 : j == 3 ? 4 * 16384 : 5 * 16384) + w * 4096; };
  {
#pragma unroll
    for (int k = 0; k < 4; ++k) {
      const unsigned a = (8 * k + (lane >> 3)) * 1024 + (((lane & 7) ^ ((4 * k + (lane >> 4)) & 7)) * 16) - k * 1024;
      const unsigned c = (lane >> 2) * 3072 + (lane & 3) * 16 + k * 48128;
      xv[k] = wv < 4 ? a : c;
    }
    const unsigned cw = wv < 4, stride = sopaque(cw ? 128 : 64 * 3072), r0 = ROT(m) + (cw ? 0 : (w & 1)), sh = cw ? 0 : 1;
#pragma unroll
    for (int j = 0; j < 2; ++j)
      dma4(xbase + (((j << sh) + r0) & 7) * stride, xv[0], xv[1], xv[2], xv[3], smem_base + (cw ? xbuf(j) : j * 16384 + w * 4096));
    if (!cw) {
      const unsigned pa = smem_base + (lane == 0 ? L_RDY : L_DUMMY + lane * 4), a = tr_lane(lane) + w * 4096u;
      float c0 = 0.f, c1 = 0.f;
      auto cs = [&](int j) __attribute__((always_inline)) {
#pragma unroll
        for (int n = 0; n < 4; ++n) {
          const v4u f = __builtin_bit_cast(v4u, ldw(a + j * 16384 + n * 1024, 512));
          c0 = dot2(f.x, ONES, c0); c1 = dot2(f.y, ONES, c1); c0 = dot2(f.z, ONES, c0); c1 = dot2(f.w, ONES, c1);
        }
        PIN(c0); PIN(c1);
      };
      asm volatile("s_waitcnt vmcnt(4)\n\tds_add_u32 %0, %1" ::"v"(pa), "v"(1u) : "memory");
      dma4(xbase + ((4 + r0) & 7) * stride, xv[0], xv[1], xv[2], xv[3], smem_base + 2 * 16384 + w * 4096);
      asm volatile("s_waitcnt vmcnt(4)\n\tds_add_u32 %0, %1 offset:4" ::"v"(pa), "v"(1u) : "memory");
      dma4(xbase + ((6 + r0) & 7) * stride, xv[0], xv[1], xv[2], xv[3], smem_base + 3 * 16384 + w * 4096);
      cs(0);
      asm volatile("s_waitcnt vmcnt(4)\n\tds_add_u32 %0, %1 offset:8" ::"v"(pa), "v"(1u) : "memory");
      cs(1);
      asm volatile("s_waitcnt vmcnt(0)\n\tds_add_u32 %0, %1 offset:12" ::"v"(pa), "v"(1u) : "memory");
      cs(2); cs(3);
      const float t = sum_halves(c0 + c1);
      vst<unsigned>(L_CSF + (w >> 1) * 512 + (lane & 31) * 16 + (w & 1) * 4, pk2(t, t - blo(pk2(t, t))));
      asm volatile("ds_add_u32 %0, %1 offset:%c2" ::"v"(pa), "v"(1u), "i"(L_CSC - L_RDY) : "memory");
    }
  }
  STAMPV(0, t00);
  STAMP(61);
  if (sopaque(wv) >= 6) { server(P, p, m, wv - 6); STAMP(1); STAMP_END(); return; }
  if (sopaque(wv) >= 4) { loader(P, p, m, wv - 4); STAMP(1); STAMP_END(); return; }
  __builtin_amdgcn_s_setprio(2);

  unsigned epoch;
  asm volatile("s_load_dword %0, %1, 0x0" : "=s"(epoch) : "s"(P.ws) : "memory");
  v4u B[32];
  auto xdma = [&](int q) __attribute__((always_inline)) { dma4(xbase + ((q + ROT(m)) & 7) * 128, xv[0], xv[1], xv[2], xv[3], smem_base + xbuf(q)); };
  xdma(2);
  const rsrc_t rs = __builtin_amdgcn_make_buffer_rsrc((void*)P.ws, 0, WS_END, 0x00020000);
  asm volatile("s_waitcnt lgkmcnt(0)" : "+s"(epoch) :: "memory");
  STAMP(19);
  CW c;
  c.s = 0; c.pos = 0; c.rdy = 0; c.perm = 4; c.lane_tr = tr_lane(lane); c.fa = lane == 0 ? L_FCNT + w * 4 : L_DUMMY + lane * 4;
  c.budget = BUDGET; c.lane = lane; c.w = w; c.ws = P.ws; c.tag = epoch + 1u;
  int h, t32, tbq, tbw, kh, cb;
#define FRESH() do { lane = opaque(lane); h = lane >> 5; t32 = lane & 31; c.lane = lane; p = sopaque(p); m = sopaque(m); w = sopaque(w); c.w = w; \
    tbq = layer * 64 + 4 * p + w; kh = w >> 1; tbw = layer * 64 + 4 * p + 2 * (m >> 3) + (w & 1); cb = 2 * (m & 7) + kh; } while (0)
  unsigned R[8];

  int layer = 0;
    f32x16 aK = {}, aV = {};
    float la; unsigned mf;
    f32x4 ar[4];
    v4u ck, cv;
    FRESH();
    auto rows_ab = [&]() __attribute__((always_inline)) {
      vst<float>(L_SA + w * 128 + t32 * 4, la);
#pragma unroll
      for (int g = 0; g < 4; ++g) ar[g] = vld<f32x4>(L_SA + w * 128 + (8 * g + 4 * h) * 4);
    };
    {
      float s1 = 0.f, s2 = 0.f, s3 = 0.f, s4 = 0.f;
      const unsigned xl = t32 * 128 + 8 * h, xf = (t32 >> 1) & 7;
      auto hook = [&](int i, int hf) __attribute__((always_inline)) {
        const int n = i + 2;
        if (n >= 0 && n < 32 && hf == 0) {
          const int q = n / 4, j = n % 4;
          if (j == 0) {
            if (q == 0) wait_vm<8>();
            if (q == 1 || q == 2 || q == 3) wait_vm<12>();
            if (q == 4) wait_vm<12>();
            if (q == 5) wait_vm<8>();
            if (q == 6) wait_vm<4>();
            if (q == 7) wait_vm<0>();
          }
          const v2u lo = vld<v2u>(xbuf(q) + xl + (((2 * j) ^ xf) * 16)), hi = vld<v2u>(xbuf(q) + xl + (((2 * j + 1) ^ xf) * 16));
          B[n] = v4u{lo.x, lo.y, hi.x, hi.y};
        }
        if (hf == 1) {
          if (i == -1) xdma(3);
          if (i == 1) xdma(4);
          if (i == 3 || i == 7 || i == 11) xdma(5 + i / 4);
          if (i >= 0 && i % 4 == 3) {
#pragma unroll
            for (int u = i - 3; u <= i; ++u) {
              const v4u f = B[u];
              s1 = dot2(f.x, ONES, s1); s2 = dot2(f.x, f.x, s2); s3 = dot2(f.y, ONES, s3); s4 = dot2(f.y, f.y, s4);
              s1 = dot2(f.z, ONES, s1); s2 = dot2(f.z, f.z, s2); s3 = dot2(f.w, ONES, s3); s4 = dot2(f.w, f.w, s4);
            }
            PIN(s1); PIN(s2); PIN(s3); PIN(s4);
          }
          cs_hook<26, true>(c, i, 0, ck, cv);
          if (n == 15 || n == 19) permit(c, n / 4 + 2);
          if (i == 30) permit(c, 10);
        }
      };
      gemm_pair<true>(c, B, aK, aV, hook);
      ln_factors(sum_halves(s1 + s3), sum_halves(s2 + s4), la, mf, h != 0);
      rows_ab();
    }
  for (; layer < 2; ++layer) {
    const bf16x8 mfr = asb(v4u{mf, mf, mf, mf});
    aK = mfma(mfr, asb(ck), aK);
    aV = mfma(mfr, asb(cv), aV);
    STAMPB(2 + 20 * layer);
    FRESH();
    {
      unsigned pkk[8], pkv[8];
#pragma unroll
      for (int j = 0; j < 8; ++j) {
        const f32x2 a = (j & 1) ? f32x2{ar[j >> 1].z, ar[j >> 1].w} : f32x2{ar[j >> 1].x, ar[j >> 1].y};
        pkk[j] = phi2(pair(aK, j), a);
        pkv[j] = pk2(pair(aV, j) * a);
      }
      const bf16x8 k0 = asb(v4u{pkk[0], pkk[1], pkk[2], pkk[3]}), k1 = asb(v4u{pkk[4], pkk[5], pkk[6], pkk[7]});
      const bf16x8 v0 = asb(v4u{pkv[0], pkv[1], pkv[2], pkv[3]}), v1 = asb(v4u{pkv[4], pkv[5], pkv[6], pkv[7]});
      const bf16x8 one = asb(v4u{ONES, ONES, ONES, ONES});
      f32x16 kv = {}, zz = {};
      kv = mfma(k0, v0, kv); zz = mfma(k0, one, zz); kv = mfma(k1, v1, kv); zz = mfma(k1, one, zz);
      const bool hd = (lane & 16) != 0;
      f32x4 q0, q1, z0, z1;
#pragma unroll
      for (int j = 0; j < 4; ++j) { q0[j] = sel(hd, kv[8 + j], kv[j]); q1[j] = sel(hd, kv[12 + j], kv[4 + j]); }
      vst<f32x4>(L_KVS + w * 2048 + lane * 16, q0);
      vst<f32x4>(L_KVS + w * 2048 + 1024 + lane * 16, q1);
#pragma unroll
      for (int j = 0; j < 4; ++j) { z0[j] = sel(hd, zz[8 + j], zz[j]); z1[j] = sel(hd, zz[12 + j], zz[4 + j]); }
      vst<f32x4>(L_ZS + w * 128 + (lane >> 4) * 32, z0);
      vst<f32x4>(L_ZS + w * 128 + (lane >> 4) * 32 + 16, z1);
      post(c, L_F1, layer + 1);
    }
    STAMPB(3 + 20 * layer);
    unsigned pq[8];
    {
      f32x16 aQ = {};
      v4u c0;
      auto hook = [&](int i) __attribute__((always_inline)) {
        if (i == 20) {
          if (layer == 0) {
            const GAS char* xr = (const GAS char*)P.x + (size_t)(tbw * 32 + t32) * 1024 + cb * 64 + h * 8;
#pragma unroll
            for (int g = 0; g < 4; ++g) { const v2u v = *(const GAS v2u*)(xr + g * 16); R[2 * g] = v.x; R[2 * g + 1] = v.y; }
          }
        }
        cs_hook<24, false>(c, i, 1, c0, c0);
      };
      gemm_single(c, B, aQ, hook);
      aQ = mfma(asb(c0), mfr, aQ);
#pragma unroll
      for (int j = 0; j < 8; ++j) { pq[j] = phi2(pair(aQ, j), f32x2{la, la}); PIN(pq[j]); }
    }
    STAMPB(4 + 20 * layer);
    {
      FRESH();
      f32x16 aM = {}, aO = {};
      Wide<16> d;
      const unsigned s0 = c.s;
      wide_steps<16, -16, 0>(c, d, B, aM, aO, kh, NoHook());
      STAMPB(54);
      v4u fk, fz;
      for (;;) {
        const unsigned fl = vld<unsigned>(L_F2);
        fk = vld<v4u>(L_KVR + lane * 16); fz = vld<v4u>(L_ZR + (lane >> 4) * 16);
        if (rfl(fl) >= 4u * layer + 4u || spent(c)) break;
      }
      STAMPB(5 + 20 * layer);
      permit(c, s0 + 8);
      const bool hd = (lane & 16) != 0;
      const v4u zero = {0u, 0u, 0u, 0u};
      const bf16x8 p0 = asb(v4u{pq[0], pq[1], pq[2], pq[3]}), p1 = asb(v4u{pq[4], pq[5], pq[6], pq[7]});
      f32x16 num = {}, den = {};
      den = mfma(asb(hd ? zero : fz), p0, den); den = mfma(asb(hd ? fz : zero), p1, den);
      num = mfma(asb(hd ? zero : fk), p0, num); num = mfma(asb(hd ? fk : zero), p1, num);
      const float i0 = __builtin_amdgcn_rcpf(den[0] + 1e-6f), i1 = __builtin_amdgcn_rcpf(den[8] + 1e-6f);
      const int os = WS_O + (tbq * 32 + 2 * m) * 1024;
      st16(rs, v4u{pk2(pair(num, 0) * i0), pk2(pair(num, 1) * i0), pk2(pair(num, 2) * i0), pk2(pair(num, 3) * i0)}, lane * 16, os);
      st16(rs, v4u{pk2(pair(num, 4) * i1), pk2(pair(num, 5) * i1), pk2(pair(num, 6) * i1), pk2(pair(num, 7) * i1)}, lane * 16 + 1024, os);
      DRAIN();
      set_flag(c, WS_FO + tbq * 256 + m * 4);
      STAMPB(6 + 20 * layer);
      FRESH();
      wait_flags(c, WS_FO + tbw * 256, kh ? 0xccccull : 0x3333ull);
      STAMPB(7 + 20 * layer);
      const int oo = WS_O + (tbw * 32 + 4 * kh) * 1024;
#pragma unroll
      for (int i = 0; i < AW; ++i) B[i] = ld16(rs, lane * 16 + (i & 3) * 1024, oo + (i >> 2) * 8192);
      SCHED();
      auto hook = [&](int i, int hf) __attribute__((always_inline)) {
        const int n = i + AW;
        if (hf == 0) return;
        if (n < 16) B[n] = ld16(rs, lane * 16 + (n & 3) * 1024, oo + (n >> 2) * 8192);
        if (n == 16) permit(c, s0 + 12);
      };
      wide_steps<16, 0, 16>(c, d, B, aM, aO, kh, hook);
      STAMPB(8 + 20 * layer);
      FRESH();
      finish_wide(c, aM, aO, R, 2 * layer + 1);
      publish_x(c, rs, R, WS_X1 + (tbw * 32 + 2 * cb) * 1024, WS_ST1 + (tbw * 16 + cb) * 256, WS_FX1 + tbw * 256 + cb * 4);
      vst<v4u>(L_KVS + w * 2048 + lane * 16, v4u{R[0], R[1], R[2], R[3]});
      vst<v4u>(L_KVS + w * 2048 + 1024 + lane * 16, v4u{R[4], R[5], R[6], R[7]});
    }
    STAMPB(9 + 20 * layer);
    unsigned pf;
    f32x16 aM = {}, aO = {};
    Wide<3> d;
    unsigned sd;
    int fd, hd0, fl2; unsigned long long mk;
    auto ldh = [&](int n) __attribute__((always_inline)) {
      if (n < 32) B[n % 16] = ld16(rs, lane * 16 + (n & 3) * 1024, hd0 + (((n >> 2) & 4) | (((n >> 2) + ROT(m)) & 3)) * 8192);
      else if (n < 40) B[n % 16] = ld16(rs, lane * 16 + (n & 3) * 1024, hd0 + (n >> 2) * 8192);
      else B[n % 16] = ld16(rs, lane * 16 + (n & 3) * 1024, hd0 + 81920 - kh * 1024);
    };
    {
      FRESH();
      f32x16 G0 = {}, U0 = {}, G1 = {}, U1 = {};
      Pipe q;
      const unsigned s0 = c.s;
      pair_steps<false, -PD, 0, 64, false>(c, q, B, G0, U0, NoHook());
      wait_flags(c, WS_FX1 + tbq * 256, 0xffffull);
      STAMPB(10 + 20 * layer);
      Stats sv;
      const int xo = WS_X1 + tbq * 32768;
#pragma unroll
      for (int i = 0; i < AH; ++i) B[i] = ld16(rs, lane * 16 + (i & 3) * 1024, xo + (((i >> 2) + ROT(m)) & 7) * 4096);
      SCHED();
      float a2; unsigned gm;
      v4u cg, cu;
      unsigned hp[8];
      const int ho = WS_H + (tbq * 86 + 2 * m) * 1024, fo = WS_FH + tbq * 256 + m * 4;
      float hv;
      fd = WS_FH + tbw * 256; mk = kh ? 0xccccull : 0x3333ull; hd0 = WS_H + (tbw * 86 + 4 * kh) * 1024;
      fl2 = fo + 128;
      auto piece = [&](int e, const f32x16& G, const f32x16& U) __attribute__((always_inline)) {
        float g;
        if (e & 1) asm volatile("v_max_f32 %1, 0, %2\n\tv_mul_f32 %0, %3, %4\n\tv_mul_f32 %1, %1, %0\n\tv_cvt_pk_bf16_f32 %0, %5, %1" : "=&v"(hp[e >> 1]), "=&v"(g) : "v"(G[e]), "v"(U[e]), "v"(a2), "v"(hv));
        else asm volatile("v_max_f32 %1, 0, %2\n\tv_mul_f32 %0, %3, %4\n\tv_mul_f32 %0, %1, %0" : "=&v"(hv), "=&v"(g) : "v"(G[e]), "v"(U[e]), "v"(a2));
      };
      auto tail = [&](const f32x16& G, const f32x16& U) __attribute__((always_inline)) {
#pragma unroll
        for (int j = 0; j < 8; ++j) {
          const f32x2 g = {fmaxf(G[2 * j], 0.f), fmaxf(G[2 * j + 1], 0.f)};
          hp[j] = pk2(g * (pair(U, j) * a2));
        }
      };
      auto store_h = [&](int r, int half) __attribute__((always_inline)) { st16(rs, v4u{hp[4 * half], hp[4 * half + 1], hp[4 * half + 2], hp[4 * half + 3]}, lane * 16 + half * 1024, ho + r * 32768); };
      auto correct = [&](f32x16& G, f32x16& U) __attribute__((always_inline)) {
        const bf16x8 f = asb(v4u{gm, gm, gm, gm});
        G = mfma(asb(cg), f, G); U = mfma(asb(cu), f, U);
      };
      auto shadow = [&](int i, int hf, int r, const f32x16& G, const f32x16& U) __attribute__((always_inline)) {
        constexpr int at[16] = {3, 5, 7, 9, 12, 13, 14, 15, 17, 19, 21, 23, 25, 28, 29, 30};
#pragma unroll
        for (int e = 0; e < 16; ++e) if (2 * i + hf == at[e]) piece(e, G, U);
        if (2 * i + hf == 31) store_h(r, 0);
        if (2 * i + hf == 33) store_h(r, 1);
        if (hf == 0 && i == 25) { if (r == 0) DRAIN(); else wait_vm<8>(); set_flag(c, fo + r * 64); }
        if (hf == 1) cs_hook<25, true>(c, i, 3 + r, cg, cu);
        if (r == 1) {
          if (hf == 0 && i == 8) pf = poll_issue(c, fd, mk);
          if (hf == 0 && i == 17) { poll_done<2>(c, pf, fd, mk); if (layer) STAMPB(51); }
          if (hf == 1 && i >= 17) ldh(i - 17);
          if (hf == 0 && i == 30) pf = poll_issue(c, fd, mk << 16);
        }
      };
      pair_steps<false, 0, 32, 64, false>(c, q, B, G0, U0, [&](int i, int hf) __attribute__((always_inline)) {
        const int n = i + AH;
        stats_hook(c, sv, 2 * i + hf, rs, WS_ST1 + tbq * 4096, a2, gm);
        if (hf == 0) return;
        if (n < 32) B[n] = ld16(rs, lane * 16 + (n & 3) * 1024, xo + (((n >> 2) + ROT(m)) & 7) * 4096);
        if (n == 32) permit(c, s0 + (m < 11 ? 23 : 19));
        if (i == 26) { a2 = fmul1(a2, a2); PIN(a2); }
        cs_hook<25, true>(c, i, 2, cg, cu);
      });
      correct(G0, U0);
      STAMPB(11 + 20 * layer);
      pair_steps<false, 32, 64, 96, false>(c, q, B, G1, U1, [&](int i, int hf) __attribute__((always_inline)) { shadow(i, hf, 0, G0, U0); });
      correct(G1, U1);
      STAMPB(12 + 20 * layer);
      pf = ~c.tag;
      if (sopaque(m) >= 11) {
        tail(G1, U1);
        store_h(1, 0); store_h(1, 1);
        DRAIN();
        set_flag(c, fo + 64);
        wait_flags(c, fd, mk);
      }
      if (sopaque(m) < 11) {
        G0 = f32x16{}; U0 = f32x16{};
        pair_steps<false, 64, 96, 96, false>(c, q, B, G0, U0, [&](int i, int hf) __attribute__((always_inline)) { shadow(i, hf, 1, G1, U1); });
        correct(G0, U0);
        tail(G0, U0);
        store_h(2, 0); store_h(2, 1);
        STAMPB(13 + 20 * layer);
      }
      if (sopaque(m) >= 11) {
#pragma unroll
        for (int i = 0; i < 15; ++i) ldh(i);
      }
      sd = c.s;
      wide_steps<43, -3, 0>(c, d, B, aM, aO, kh, NoHook());
    }
    STAMPB(14 + 20 * layer);
    {
      unsigned pf2;
      auto hook = [&](int i, int hf) __attribute__((always_inline)) {
        if (hf == 0) {
          if (i == 7) { wait_vm<7>(); set_flag(c, fl2); }
          if (i == 13) pf2 = poll_issue(c, fd, 0x7ffull << 32);
          return;
        }
        if (i == 1) { poll_done<5>(c, pf, fd, mk << 16); if (layer) STAMPB(47); }
        if (i == 19 && layer) STAMPB(48);
        if (i == 31 && layer) STAMPB(50);
        if (i + 15 < 32) ldh(i + 15);
        if (i == 20) { poll_done<4>(c, pf2, fd, 0x7ffull << 32); if (layer) STAMPB(49); }
        if (i >= 20 && i <= 23) { ldh(2 * i - 8); ldh(2 * i - 7); }
        if (i >= 24 && i <= 26) ldh(i + 16);
        if (i == PKVAT) permit(c, sd + 11 + PKV);
        if (i == 36) {
          const v4u r0 = vld<v4u>(L_KVS + w * 2048 + lane * 16), r1 = vld<v4u>(L_KVS + w * 2048 + 1024 + lane * 16);
          R[0] = r0.x; R[1] = r0.y; R[2] = r0.z; R[3] = r0.w; R[4] = r1.x; R[5] = r1.y; R[6] = r1.z; R[7] = r1.w;
        }
      };
      wide_steps<43, 0, 43>(c, d, B, aM, aO, kh, hook);
      STAMPB(17 + 20 * layer);
      FRESH();
      finish_wide(c, aM, aO, R, 2 * layer + 2);
    }
#pragma unroll
    for (int i = 0; i < 32; ++i) asm volatile("" : "=v"(B[i]));
    asm volatile("" : "=v"(aK), "=v"(aV), "=v"(la), "=v"(mf), "=v"(ck), "=v"(cv));
    asm volatile("" : "=v"(ar[0]), "=v"(ar[1]), "=v"(ar[2]), "=v"(ar[3]));
    if (layer == 0) {
      publish_x(c, rs, R, WS_X2 + (tbw * 32 + 2 * cb) * 1024, WS_ST2 + (tbw * 16 + cb) * 256, WS_FX2 + tbw * 256 + cb * 4);
      STAMPB(18);
      FRESH();
      aK = f32x16{}; aV = f32x16{};
      Pipe q;
      const unsigned s0 = c.s;
      pair_steps<true, -PD, 0, 32, false>(c, q, B, aK, aV, NoHook());
      wait_flags(c, WS_FX2 + tbq * 256, 0xffffull);
      STAMPB(20);
      Stats sv;
      const int xo = WS_X2 + tbq * 32768;
#pragma unroll
      for (int i = 0; i < AH; ++i) B[i] = ld16(rs, lane * 16 + (i & 3) * 1024, xo + (((i >> 2) + ROT(m)) & 7) * 4096);
      SCHED();
      auto hook = [&](int i, int hf) __attribute__((always_inline)) {
        const int n = i + AH;
        stats_hook(c, sv, 2 * i + hf, rs, WS_ST2 + tbq * 4096, la, mf);
        if (hf == 0) {
          if (i == 25) vst<float>(L_SA + w * 128 + t32 * 4, la);
          if (i == 26) { ar[0] = vld<f32x4>(L_SA + w * 128 + 4 * h * 4); ar[1] = vld<f32x4>(L_SA + w * 128 + (8 + 4 * h) * 4); }
          if (i == 27) { ar[2] = vld<f32x4>(L_SA + w * 128 + (16 + 4 * h) * 4); ar[3] = vld<f32x4>(L_SA + w * 128 + (24 + 4 * h) * 4); }
          return;
        }
        if (n < 32) B[n] = ld16(rs, lane * 16 + (n & 3) * 1024, xo + (((n >> 2) + ROT(m)) & 7) * 4096);
        if (n == 32) permit(c, s0 + KVP2);
        cs_hook<26, true>(c, i, 0, ck, cv);
      };
      pair_steps<true, 0, 32, 32, false>(c, q, B, aK, aV, hook);
    }
  }
  {
    layer = 0;
    FRESH();
    GAS char* o = (GAS char*)P.out + (size_t)(tbw * 32 + t32) * 1024 + cb * 64 + h * 8;
#pragma unroll
    for (int g = 0; g < 4; ++g) *(GAS v2u*)(o + g * 16) = v2u{R[2 * g], R[2 * g + 1]};
  }
  STAMPB(38);
  STAMP_END();
  STAMPV(42, (unsigned long long)(BUDGET - c.budget));
  if (b == 0 && w == 0 && lane == 0) *(GAS unsigned*)(P.ws + WS_EPOCH) = c.tag;
}
}

extern "C" void kernel_launch(void* const* d_in, const int* in_sizes, int n_in, void* d_out, int out_size, void* d_ws,
                              size_t ws_size, hipStream_t stream) {
  Params P;
  P.x = (const char*)d_in[0]; P.wqkv = (const char*)d_in[1]; P.wo = (const char*)d_in[2];
  P.wg = (const char*)d_in[4]; P.wu = (const char*)d_in[5]; P.wd = (const char*)d_in[6];
  P.out = (char*)d_out; P.ws = (char*)d_ws;
  (void)hipFuncSetAttribute((const void*)fused_fwd, hipFuncAttributeMaxDynamicSharedMemorySize, LDS_BYTES);
  fused_fwd<<<256, 512, LDS_BYTES, stream>>>(P);
}
```

```cpp
#include <hip/hip_runtime.h>
#include <stdint.h>

#define GAS __attribute__((address_space(1)))
#define LAS __attribute__((address_space(3)))
#define DI __device__ __forceinline__
typedef unsigned v4u __attribute__((ext_vector_type(4)));
typedef unsigned v2u __attribute__((ext_vector_type(2)));
typedef float f32x16 __attribute__((ext_vector_type(16)));
typedef float f32x4 __attribute__((ext_vector_type(4)));
typedef float f32x2 __attribute__((ext_vector_type(2)));
typedef __bf16 bf16x8 __attribute__((ext_vector_type(8)));
typedef __bf16 bf16x2 __attribute__((ext_vector_type(2)));
typedef short s16x4 __attribute__((ext_vector_type(4)));

#define MAPMODE 1
#define LDEPTH 8
#define LPRIO 0
#define LSLEEP 1
#define AW 8
#define PKV 8
#define THIN 2
#define PD 2
#define PKVAT 16
#define THINB 18
#define KVP2 14
#define PACEB 10
#define AH 8

#define ROT(m) (((m) + 2 * p) & 7)
namespace {
constexpr int KB = 1024;
constexpr int WS_EPOCH = 0;
constexpr int WS_FKV = 4096;
constexpr int WS_FO = WS_FKV + 32 * KB;
constexpr int WS_FX1 = WS_FO + 32 * KB;
constexpr int WS_FH = WS_FX1 + 32 * KB;
constexpr int WS_FX2 = WS_FH + 32 * KB;
constexpr int WS_ST1 = WS_FX2 + 16 * KB;
constexpr int WS_ST2 = WS_ST1 + 512 * KB;
constexpr int KVW = 1152;
constexpr int WS_KVP = WS_ST2 + 256 * KB;
constexpr int WS_CSP = WS_KVP + 2 * 16 * 16 * 2 * KVW;
constexpr int WS_O = WS_CSP + 16 * 2 * 16 * 768;
constexpr int WS_X1 = WS_O + 4096 * KB;
constexpr int WS_X2 = WS_X1 + 4096 * KB;
constexpr int WS_H = WS_X2 + 2048 * KB;
constexpr int WS_RQ = WS_H + 2 * 64 * 86 * KB;
constexpr int WS_RG = WS_RQ + 1536 * KB;
constexpr int WS_RU = WS_RG + 43 * 32 * KB;
constexpr int WS_DBG = WS_RU + 43 * 32 * KB;
constexpr int WS_END = WS_DBG + 256 * 8 * 64 * 8;
constexpr int NSLOT = 8;
constexpr int L_RING = 0;
constexpr int L_SCR = NSLOT * 16 * KB;
constexpr int L_KVS = L_SCR + 16 * KB;
constexpr int L_CSF = L_KVS + 8 * KB;
constexpr int L_SA = L_CSF + 5120;
constexpr int L_KVR = L_SA + 512;
constexpr int L_ZR = L_KVR + 1024;
constexpr int L_ZS = L_ZR + 64;
constexpr int L_DUMMY = L_ZS + 512;
constexpr int L_RDY = L_DUMMY + 448;
constexpr int L_CSC = L_RDY + 32;
constexpr int L_FCNT = L_CSC + 32;
constexpr int L_F1 = L_FCNT + 16;
constexpr int L_F2 = L_F1 + 16;
constexpr int L_F3 = L_F2 + 16;
constexpr int L_RPD = L_F3 + 16;
constexpr int L_END = L_RPD + 16;
static_assert(L_END <= 160 * KB, "LDS");
constexpr int LDS_BYTES = (L_END + 15) / 16 * 16;
constexpr unsigned CODE_BYTES = 43776;
constexpr unsigned BUDGET = 1u << 18;

struct Params {
  const char* x; const char* wqkv; const char* wo; const char* wg; const char* wu; const char* wd;
  char* out; char* ws;
};

extern __shared__ __attribute__((aligned(16))) char smem[];
template <class T> DI LAS T* lp(unsigned off) { return (LAS T*)((LAS char*)smem + off); }
template <class T> DI T vld(unsigned off) { return *(volatile LAS T*)((LAS char*)smem + off); }
template <class T> DI void vst(unsigned off, T v) { *(volatile LAS T*)((LAS char*)smem + off) = v; }
DI unsigned rfl(unsigned v) { return __builtin_amdgcn_readfirstlane(v); }
DI const char* uni(const char* p) { const unsigned long long v = (unsigned long long)p; return (const char*)(((unsigned long long)rfl((unsigned)(v >> 32)) << 32) | rfl((unsigned)v)); }
#define SCHED() __builtin_amdgcn_sched_barrier(0)
#define CFENCE() asm volatile("" ::: "memory")
#define DRAIN() asm volatile("s_waitcnt vmcnt(0)" ::: "memory")
#define PIN(x) asm volatile("" : "+v"(x))

#define STAMP(i) do {} while (0)
#define STAMPV(i, v) do {} while (0)
#define STAMPB(i) do {} while (0)
#define STAMP_END() do {} while (0)

DI bf16x8 ldw(unsigned a, int second) {
  s16x4 lo = __builtin_amdgcn_ds_read_tr16_b64_v4i16(lp<s16x4>(a));
  s16x4 hi = __builtin_amdgcn_ds_read_tr16_b64_v4i16(lp<s16x4>(a + second));
  return __builtin_bit_cast(bf16x8, __builtin_shufflevector(lo, hi, 0, 1, 2, 3, 4, 5, 6, 7));
}
DI f32x16 mfma(bf16x8 a, bf16x8 b, f32x16 c) { return __builtin_amdgcn_mfma_f32_32x32x16_bf16(a, b, c, 0, 0, 0); }
DI bf16x8 asb(v4u v) { return __builtin_bit_cast(bf16x8, v); }
DI unsigned pk2(float lo, float hi) { bf16x2 r = {(__bf16)lo, (__bf16)hi}; return __builtin_bit_cast(unsigned, r); }
DI unsigned pk2(f32x2 v) { return pk2(v.x, v.y); }
DI float dot2(unsigned a, unsigned b, float c) { return __builtin_amdgcn_fdot2_f32_bf16(__builtin_bit_cast(bf16x2, a), __builtin_bit_cast(bf16x2, b), c, false); }
DI float u2f(unsigned u) { return __builtin_bit_cast(float, u); }
DI unsigned f2u(float f) { return __builtin_bit_cast(unsigned, f); }
DI float sel(bool c, float a, float b) { asm volatile("" : "+v"(a), "+v"(b)); return c ? a : b; }
DI int opaque(int v) { asm volatile("" : "+v"(v)); return v; }
DI int sopaque(int v) { asm volatile("" : "+s"(v)); return v; }
DI float blo(unsigned u) { return u2f(u << 16); }
DI float bhi(unsigned u) { return u2f(u & 0xffff0000u); }
DI float sum_halves(float v) {
  auto r = __builtin_amdgcn_permlane32_swap(f2u(v), f2u(v), false, false);
  float o; asm("v_add_f32 %0, %1, %2" : "=v"(o) : "v"(r[0]), "v"(r[1])); return o;
}
template <int CTRL> DI float dpp_add(float v) { return v + u2f(__builtin_amdgcn_update_dpp(0u, f2u(v), CTRL, 0xf, 0xf, false)); }
DI float row_sum(float v) { v = dpp_add<0x128>(v); v = dpp_add<0x124>(v); v = dpp_add<0x122>(v); return dpp_add<0x121>(v); }
DI f32x2 pair(const f32x16& a, int j) { return f32x2{a[2 * j], a[2 * j + 1]}; }
DI unsigned phi2(f32x2 x, f32x2 a) {
  const f32x2 e2 = x * (a * 1.4426950408889634f);
  const f32x2 t = x * a + 1e-6f;
  const f32x2 e = {__builtin_amdgcn_fmed3f(__builtin_amdgcn_exp2f(e2.x), 0.f, 1.f), __builtin_amdgcn_fmed3f(__builtin_amdgcn_exp2f(e2.y), 0.f, 1.f)};
  const f32x2 mx = {fmaxf(t.x, 1e-6f), fmaxf(t.y, 1e-6f)};
  return pk2(mx + e);
}
constexpr unsigned ONES = 0x3f803f80u;
DI float fadd1(float a, float b) { float r; asm("v_add_f32 %0, %1, %2" : "=v"(r) : "v"(a), "v"(b)); return r; }
DI float fmul1(float a, float b) { float r; asm("v_mul_f32 %0, %1, %2" : "=v"(r) : "v"(a), "v"(b)); return r; }

typedef __amdgpu_buffer_rsrc_t rsrc_t;
DI v4u ld16(rsrc_t rs, int voff, int soff) { return __builtin_amdgcn_raw_buffer_load_b128(rs, voff, soff, 16); }
DI v2u ld8(rsrc_t rs, int voff, int soff) { return __builtin_amdgcn_raw_buffer_load_b64(rs, voff, soff, 16); }
DI void st16(rsrc_t rs, v4u v, int voff, int soff) {
  __builtin_amdgcn_raw_buffer_store_b128(v, rs, voff, soff, 16);
  asm volatile("s_nop 1" ::"v"(v) : "memory");
}
DI void st8(rsrc_t rs, v2u v, int voff, int soff) { __builtin_amdgcn_raw_buffer_store_b64(v, rs, voff, soff, 16); }

struct CW {
  unsigned s, pos, rdy;
  unsigned lane_tr;
  unsigned fa;
  unsigned tag, perm;
  int budget;
  int lane, w;
  char* ws;
};
DI bool spent(CW& c) { c.budget -= 1; return c.budget < 0; }
DI unsigned min4(v4u v) { return rfl(min(min(v.x, v.y), min(v.z, v.w))); }
DI void wait_count(CW& c, unsigned have, unsigned addr, unsigned need) {
  if (__builtin_expect(rfl(have) < need, 0)) {
    for (;;) { if (rfl(vld<unsigned>(addr)) >= need || spent(c)) break; }
  }
}
DI void peek(CW& c, unsigned ahead) { c.rdy = vld<unsigned>(L_RDY + ((c.pos + ahead) & (NSLOT - 1)) * 4); }
DI void wait_full(CW& c) { wait_count(c, c.rdy, L_RDY + c.pos * 4, (c.s / NSLOT + 1) * 4); }
DI void post(CW& c, int word, unsigned v) { vst<unsigned>(c.fa + (word - L_FCNT), v); }
DI void release(CW& c) { c.s += 1; c.pos = (c.pos + 1) & (NSLOT - 1); post(c, L_FCNT, min(c.s + NSLOT, c.perm)); }
DI void permit(CW& c, unsigned v) { c.perm = v; post(c, L_FCNT, min(c.s + NSLOT, v)); }
DI void set_flag(CW& c, int off) {
  const char* base = uni(c.ws + off);
  asm volatile("s_mov_b64 exec, 1\n\ts_nop 4\n\tglobal_atomic_swap %0, %1, %2\n\ts_mov_b64 exec, -1" ::"v"(0u), "v"(c.tag), "s"(base) : "memory");
}
DI void wait_flags(CW& c, int off, unsigned long long mask) {
  unsigned f, bud = rfl(c.budget);
  const unsigned voff = c.lane * 4;
  const char* base = uni(c.ws + off);
  asm volatile(
      "s_mov_b64 exec, %5\n\ts_nop 4\n\t"
      "1:\n\t"
      "global_atomic_or %0, %2, %6, %3 sc0\n\t"
      "s_waitcnt vmcnt(0)\n\t"
      "v_cmp_ne_u32 vcc, %4, %0\n\t"
      "s_cbranch_vccz 2f\n\t"
      "s_sub_i32 %1, %1, 8\n\t"
      "s_cmp_ge_i32 %1, 0\n\t"
      "s_cbranch_scc1 1b\n\t"
      "2:\n\t"
      "s_mov_b64 exec, -1\n\t"
      : "=&v"(f), "+s"(bud) : "v"(voff), "s"(base), "s"(c.tag), "s"(mask), "v"(0u) : "memory", "vcc", "scc");
  c.budget = rfl(bud);
}
DI unsigned poll_issue(CW& c, int off, unsigned long long mask) {
  unsigned f = c.tag;
  const unsigned voff = c.lane * 4;
  const char* base = uni(c.ws + off);
  asm volatile("s_mov_b64 exec, %3\n\tv_not_b32 %0, %0\n\ts_nop 4\n\tglobal_atomic_or %0, %1, %4, %2 sc0\n\ts_mov_b64 exec, -1"
               : "+v"(f) : "v"(voff), "s"(base), "s"(mask), "v"(0u) : "memory");
  return f;
}
template <int N> DI void poll_done(CW& c, unsigned f, int off, unsigned long long mask) {
  asm volatile("s_waitcnt vmcnt(%c1)" : "+v"(f) : "i"(N) : "memory");
  if (__builtin_expect(__builtin_amdgcn_ballot_w64(f != c.tag) != 0ull, 0)) wait_flags(c, off, mask);
}

template <int V> struct IC { static constexpr int value = V; DI constexpr operator int() const { return V; } };
template <int I, int E, class F> DI void static_for(F&& f) { if constexpr (I < E) { f(IC<I>{}); static_for<I + 1, E>(f); } }
struct Pipe { bf16x8 wa[PD + 1], wb[PD + 1]; unsigned ad, ad2, adn, need, pn, have, rel; };
#define SPIN(x) asm volatile("" : "+s"(x))
DI void slot_first(CW& c, Pipe& q) { peek(c, 0); wait_full(c); q.adn = c.lane_tr + c.pos * 16384u; }
template <int K, bool NX> DI void book(CW& c, Pipe& q) {
  if constexpr (NX && K == 0) { q.pn = (c.pos + 1) & (NSLOT - 1); c.rdy = vld<unsigned>(L_RDY + q.pn * 4); }
  if constexpr (NX && K == 1) { q.need = ((c.s + 1) / NSLOT + 1) * 4; SPIN(q.need); }
  if constexpr (NX && K == 2) { q.adn = c.lane_tr + q.pn * 16384u; PIN(q.adn); }
  if constexpr (NX && K == 3) { q.have = rfl(c.rdy); SPIN(q.have); }
  if constexpr (NX && K == 4) wait_count(c, q.have, L_RDY + q.pn * 4, q.need);
  if constexpr (K == 5) { c.s += 1; c.pos = (c.pos + 1) & (NSLOT - 1); q.rel = min(c.s + NSLOT, c.perm); PIN(q.rel); }
  if constexpr (K == 6) post(c, L_FCNT, q.rel);
}
template <bool SWAP, int I0, int I1, int NEND, bool MORE, class HOOK>
DI void pair_steps(CW& c, Pipe& q, v4u (&B)[32], f32x16& acc0, f32x16& acc1, HOOK hook) {
  static_for<I0, I1>([&](auto ic) __attribute__((always_inline)) {
    constexpr int i = ic.value, n = i + PD;
    constexpr bool F = n < NEND, NX = F && (MORE || n + 8 - n % 8 < NEND);
    if constexpr (n == 0) slot_first(c, q);
    if constexpr (i >= 0) acc0 = SWAP ? mfma(asb(B[i % 32]), q.wa[i % (PD + 1)], acc0) : mfma(q.wa[i % (PD + 1)], asb(B[i % 32]), acc0);
    SCHED();
    if constexpr (F && n % 8 == 0) q.ad = q.adn;
    if constexpr (F) q.wa[n % (PD + 1)] = ldw(q.ad + (n % 8) * 1024, 512);
    if constexpr (F && n % 8 >= 2) book<n % 8 - 2, NX>(c, q);
    hook(IC<(i < 0 ? i : i % 32)>{}, IC<0>{});
    SCHED();
    if constexpr (i >= 0) acc1 = SWAP ? mfma(asb(B[i % 32]), q.wb[i % (PD + 1)], acc1) : mfma(q.wb[i % (PD + 1)], asb(B[i % 32]), acc1);
    SCHED();
    if constexpr (F) q.wb[n % (PD + 1)] = ldw(q.ad + 8192 + (n % 8) * 1024, 512);
    if constexpr (F && n % 8 == 7) book<6, NX>(c, q);
    hook(IC<(i < 0 ? i : i % 32)>{}, IC<1>{});
    SCHED();
  });
}
template <bool SWAP, class HOOK>
DI void gemm_pair(CW& c, v4u (&B)[32], f32x16& acc0, f32x16& acc1, HOOK hook) {
  Pipe q;
  pair_steps<SWAP, -PD, 32, 32, false>(c, q, B, acc0, acc1, hook);
}
template <class HOOK>
DI void gemm_single(CW& c, v4u (&B)[32], f32x16& acc, HOOK hook) {
  bf16x8 wa[5];
  Pipe q;
  static_for<-4, 32>([&](auto ic) __attribute__((always_inline)) {
    constexpr int i = ic.value, n = i + 4;
    if constexpr (n == 0) slot_first(c, q);
    if constexpr (i >= 0) acc = mfma(wa[i % 5], asb(B[i]), acc);
    SCHED();
    if constexpr (n < 32) {
      if constexpr (n % 16 == 0) q.ad = q.adn;
      wa[n % 5] = ldw(q.ad + (n % 16) * 1024, 512);
      if constexpr (n % 16 == 6) book<0, (n < 16)>(c, q);
      if constexpr (n % 16 >= 10) book<n % 16 - 9, (n < 16)>(c, q);
    }
    hook(ic);
    SCHED();
  });
}
template <int PW> struct Wide { bf16x8 wa[PW + 1], wb[PW + 1]; Pipe q; unsigned dO, lt; };
template <int NK, int I0, int I1, int PW, class HOOK>
DI void wide_steps(CW& c, Wide<PW>& d, v4u (&B)[32], f32x16& accM, f32x16& accO, int kh, HOOK hook) {
  Pipe& q = d.q;
  const unsigned keep = c.lane_tr;
  if constexpr (I0 == -PW) { d.lt = c.lane_tr + kh * (8192u + 512u); d.dO = kh ? -512u : 512u; }
  c.lane_tr = d.lt;
  static_for<I0, I1>([&](auto ic) __attribute__((always_inline)) {
    constexpr int i = ic.value, n = i + PW;
    constexpr bool F = n < NK, NX = F && n + 4 - n % 4 < NK;
    if constexpr (n == 0) slot_first(c, q);
    if constexpr (i >= 0) accO = mfma(d.wb[i % (PW + 1)], asb(B[i % 16]), accO);
    SCHED();
    if constexpr (F && n % 4 == 0) { q.ad = q.adn; if constexpr (n + 3 == NK) q.ad -= kh * 2048u; q.ad2 = q.ad + d.dO; }
    if constexpr (F) d.wb[n % (PW + 1)] = ldw(q.ad2 + (n % 4) * 2048, 1024);
    if constexpr (F && n % 4 == 1) { book<1, NX>(c, q); }
    if constexpr (F && n % 4 == 2) { book<2, NX>(c, q); book<3, NX>(c, q); }
    if constexpr (F && (n % 4 == 3 || n == NK - 1)) { book<5, NX>(c, q); }
    hook(ic, IC<0>{});
    SCHED();
    if constexpr (i >= 0) accM = mfma(d.wa[i % (PW + 1)], asb(B[i % 16]), accM);
    SCHED();
    if constexpr (F) d.wa[n % (PW + 1)] = ldw(q.ad + (n % 4) * 2048, 1024);
    if constexpr (F && n % 4 == 0) book<0, NX>(c, q);
    if constexpr (F && n % 4 == 2) book<4, NX>(c, q);
    if constexpr (F && (n % 4 == 3 || n == NK - 1)) book<6, NX>(c, q);
    hook(ic, IC<1>{});
    SCHED();
  });
  c.lane_tr = keep;
}
struct NoHook { template <class A, class B> DI void operator()(A, B) const {} };

DI void ln_factors(float sum, float sq, float& a, unsigned& mf, bool up) {
  float mean = sum * (1.0f / 512.0f);
  PIN(mean);
  const float var = fmaxf(sq * (1.0f / 512.0f) - mean * mean, 0.f);
  a = __builtin_amdgcn_rsqf(var + 1e-5f);
  const unsigned hi = pk2(-mean, -mean);
  const float lo = -mean - blo(hi);
  mf = up ? pk2(lo, lo) : hi;
}
struct Stats { v4u sv[4]; f32x4 t; f32x2 u[4]; float mean, var; };
constexpr int K0 = 34;
DI void stats_hook(CW& c, Stats& t, int k, rsrc_t rs, int soff, float& a, unsigned& mf) {
  if (k == 3 || k == 7 || k == 11 || k == 15) t.sv[k >> 2] = ld16(rs, c.lane * 16 + (k >> 2) * 1024, soff);
  const unsigned sc = L_SCR + (c.w ^ 2) * 4096;
  if (k >= K0 && k < K0 + 4) { const int e = k - K0; t.t[e] = fadd1(fadd1(u2f(t.sv[0][e]), u2f(t.sv[1][e])), fadd1(u2f(t.sv[2][e]), u2f(t.sv[3][e]))); PIN(t.t[e]); }
  if (k == K0 + 4) vst<f32x4>(sc + c.lane * 16, t.t);
  if (k == K0 + 5) { t.u[0] = vld<f32x2>(sc + (c.lane & 31) * 8); t.u[1] = vld<f32x2>(sc + 256 + (c.lane & 31) * 8); }
  if (k == K0 + 6) { t.u[2] = vld<f32x2>(sc + 512 + (c.lane & 31) * 8); t.u[3] = vld<f32x2>(sc + 768 + (c.lane & 31) * 8); }
  if (k == K0 + 10) { t.mean = fadd1(fadd1(t.u[0].x, t.u[1].x), fadd1(t.u[2].x, t.u[3].x)); PIN(t.mean); }
  if (k == K0 + 11) { t.var = fadd1(fadd1(t.u[0].y, t.u[1].y), fadd1(t.u[2].y, t.u[3].y)); PIN(t.var); }
  if (k == K0 + 12) { t.mean *= -1.0f / 512.0f; PIN(t.mean); t.var = t.var * (1.0f / 512.0f) + 1e-5f; PIN(t.var); }
  if (k == K0 + 13) { t.var = fmaxf(t.var - t.mean * t.mean, 1e-5f); PIN(t.var); a = __builtin_amdgcn_rsqf(t.var); PIN(a); }
  if (k == K0 + 14) { mf = pk2(t.mean, t.mean); t.mean -= blo(mf); PIN(mf); PIN(t.mean); }
  if (k == K0 + 15) { mf = c.lane >= 32 ? pk2(t.mean, t.mean) : mf; PIN(mf); }
}
DI void finish_wide(CW& c, const f32x16& accM, const f32x16& accO, unsigned (&R)[8], unsigned seq) {
  const unsigned mine = L_SCR + c.w * 4096 + c.lane * 16, other = L_SCR + (c.w ^ 2) * 4096 + c.lane * 16;
#pragma unroll
  for (int g = 0; g < 4; ++g) vst<f32x4>(mine + g * 1024, f32x4{accO[4 * g], accO[4 * g + 1], accO[4 * g + 2], accO[4 * g + 3]});
  post(c, L_F3, seq);
  f32x2 y[8];
#pragma unroll
  for (int j = 0; j < 8; ++j) { y[j] = pair(accM, j) + f32x2{blo(R[j]), bhi(R[j])}; PIN(y[j]); }
  f32x4 v[4];
  for (;;) {
    const unsigned fl = vld<unsigned>(L_F3 + (c.w ^ 2) * 4);
#pragma unroll
    for (int g = 0; g < 4; ++g) v[g] = vld<f32x4>(other + g * 1024);
    if (rfl(fl) >= seq || spent(c)) break;
  }
#pragma unroll
  for (int g = 0; g < 4; ++g) {
    R[2 * g] = pk2(y[2 * g] + f32x2{v[g].x, v[g].y});
    R[2 * g + 1] = pk2(y[2 * g + 1] + f32x2{v[g].z, v[g].w});
  }
}
DI void publish_x(CW& c, rsrc_t rs, const unsigned (&R)[8], int xoff, int stoff, int floff) {
  st16(rs, v4u{R[0], R[1], R[2], R[3]}, c.lane * 16, xoff);
  st16(rs, v4u{R[4], R[5], R[6], R[7]}, c.lane * 16 + 1024, xoff);
  float s0 = 0.f, q0 = 0.f, s1 = 0.f, q1 = 0.f;
#pragma unroll
  for (int i = 0; i < 4; ++i) { s0 = dot2(R[i], ONES, s0); q0 = dot2(R[i], R[i], q0); s1 = dot2(R[4 + i], ONES, s1); q1 = dot2(R[4 + i], R[4 + i], q1); }
  const float s = sum_halves(s0 + s1), q = sum_halves(q0 + q1);
  st8(rs, v2u{f2u(s), f2u(q)}, (c.lane & 31) * 8, stoff);
  DRAIN();
  set_flag(c, floff);
}

DI void dma4(const char* base, unsigned v0, unsigned v1, unsigned v2, unsigned v3, unsigned dst) {
  asm volatile("s_mov_b32 m0, %5\n\ts_nop 3\n\t"
               "global_load_lds_dwordx4 %0, %4\n\tglobal_load_lds_dwordx4 %1, %4 offset:1024\n\t"
               "global_load_lds_dwordx4 %2, %4 offset:2048\n\tglobal_load_lds_dwordx4 %3, %4 offset:3072"
               ::"v"(v0), "v"(v1), "v"(v2), "v"(v3), "s"(uni(base)), "s"(rfl(dst)) : "memory");
}
#define PACE "s_sleep 1\n\t"
DI void dma4p(const char* base, unsigned v0, unsigned v1, unsigned v2, unsigned v3, unsigned dst) {
  asm volatile("s_mov_b32 m0, %5\n\ts_nop 3\n\t"
               "global_load_lds_dwordx4 %0, %4\n\t" PACE "global_load_lds_dwordx4 %1, %4 offset:1024\n\t" PACE
               "global_load_lds_dwordx4 %2, %4 offset:2048\n\t" PACE "global_load_lds_dwordx4 %3, %4 offset:3072\n\t" PACE
               ::"v"(v0), "v"(v1), "v"(v2), "v"(v3), "s"(uni(base)), "s"(rfl(dst)) : "memory");
}
DI void dma4_sc1(const char* base, unsigned v, unsigned dst) {
  asm volatile("s_mov_b32 m0, %2\n\ts_nop 3\n\t"
               "global_load_lds_dwordx4 %0, %1 sc1\n\t" PACE "global_load_lds_dwordx4 %0, %1 offset:1024 sc1\n\t" PACE
               "global_load_lds_dwordx4 %0, %1 offset:2048 sc1\n\t" PACE "global_load_lds_dwordx4 %0, %1 offset:3072 sc1\n\t" PACE
               ::"v"(v), "s"(uni(base)), "s"(rfl(dst)) : "memory");
}
DI void dma4_sc1f(const char* base, unsigned v, unsigned dst) {
  asm volatile("s_mov_b32 m0, %2\n\ts_nop 3\n\t"
               "global_load_lds_dwordx4 %0, %1 sc1\n\tglobal_load_lds_dwordx4 %0, %1 offset:1024 sc1\n\t"
               "global_load_lds_dwordx4 %0, %1 offset:2048 sc1\n\tglobal_load_lds_dwordx4 %0, %1 offset:3072 sc1"
               ::"v"(v), "s"(uni(base)), "s"(rfl(dst)) : "memory");
}
template <int N> DI void wait_vm() { asm volatile("s_waitcnt vmcnt(%c0)" ::"i"(N) : "memory"); }
DI unsigned tr_lane(int lane) { return (4 * (lane >> 5) + ((lane & 15) >> 2)) * 64 + ((lane >> 4) & 1) * 32 + (lane & 3) * 8; }
DI int fresh_lane() { int l; asm volatile("v_mbcnt_lo_u32_b32 %0, -1, 0\n\tv_mbcnt_hi_u32_b32 %0, -1, %0" : "=v"(l)); return l; }
#define LSTAMP(v, s) do {} while (0)
DI void lds_add(unsigned pa, unsigned off, unsigned n) { CFENCE(); (void)__hip_atomic_fetch_add(lp<unsigned>(pa + off), n, __ATOMIC_RELAXED, __HIP_MEMORY_SCOPE_WORKGROUP); CFENCE(); }
DI void loader(const Params& P, int p, int m, int lw) {
  const int lane = fresh_lane();
  const unsigned smem_base = (unsigned)(uintptr_t)(LAS char*)smem;
  const unsigned nr = m < 11 ? 3 : 2, NS = 21 + 4 * nr, NT = 2 * NS;
  const unsigned v_lin = lane * 16, v_wide = ((lane & 31) >> 2) * 1024 + (lane >> 5) * 64 + (lane & 3) * 16, v_raw = (lane >> 2) * 3072 + (lane & 3) * 16;
  const unsigned pa = lane == 0 ? L_RDY : L_DUMMY + lane * 4;
  __builtin_amdgcn_s_setprio(LPRIO);
  unsigned limit = 0;
  int budget = BUDGET;
  bool pend = false;
  for (unsigned s = 4 + lw; s < NT; s += 2) {
    const bool l2 = s >= NS;
    const unsigned i = l2 ? s - NS : s, g = i - 10u, d = g - 4u * nr, r4 = 4u * ROT(m);
    const char* src[4];
    unsigned kind;
    if (i < 6u) {
      kind = l2 ? 1u : 0u;
#pragma unroll
      for (unsigned q = 0; q < 4; ++q) {
        const unsigned blk = i < 4u ? q >> 1 : 2u, ks0 = ((i < 4u ? 8u * i + (q & 1) * 4u : 16u * (i - 4u) + q * 4u) + r4) & 31u;
        src[q] = l2 ? P.ws + WS_RQ + ((m * 3 + blk) * 32u + ks0) * 1024u : P.wqkv + m * 64 + ks0 * (16u * 3072u);
      }
    } else if (i < 10u) {
      kind = 2u;
#pragma unroll
      for (unsigned q = 0; q < 4; ++q) src[q] = P.wo + ((i - 6u) * 128u + q * 32u) * 1024u + (m & 7) * 128;
    } else if (g < 4u * nr) {
      kind = 1u;
#pragma unroll
      for (unsigned q = 0; q < 4; ++q) src[q] = P.ws + ((q >> 1) ? WS_RU : WS_RG) + ((16u * (g >> 2) + m) * 32u + ((8u * (g & 3u) + (q & 1) * 4u + r4) & 31u)) * 1024u;
    } else {
      kind = 2u;
#pragma unroll
      for (unsigned q = 0; q < 4; ++q) src[q] = P.wd + min((d < 8u ? (d & 4u) | ((d + ROT(m)) & 3u) : d) * 128u + q * 32u, 1344u) * 1024u + (m & 7) * 128;
    }
    if (pend && i < THINB) { wait_vm<0>(); lds_add(pa, ((s - 2) & (NSLOT - 1)) * 4, 4); LSTAMP(tlan, s - 2); pend = false; }
    while (s >= limit) {
      limit = min4(vld<v4u>(L_FCNT));
      if (s < limit) break;
      if (pend) { wait_vm<0>(); lds_add(pa, ((s - 2) & (NSLOT - 1)) * 4, 4); LSTAMP(tlan, s - 2); pend = false; continue; }
      __builtin_amdgcn_s_sleep(LSLEEP);
      if (--budget < 0) break;
    }
    LSTAMP(tiss, s);
    const unsigned dst = smem_base + L_RING + (s & (NSLOT - 1)) * 16384u;
    const unsigned v0 = kind == 2u ? v_wide : v_raw, st = kind == 2u ? 7168u : 48128u;
    if (i >= PACEB && i < 18u && s >= 6u) {
      if (kind == 1u) { dma4_sc1(src[0], v_lin, dst); dma4_sc1(src[1], v_lin, dst + 4096); dma4_sc1(src[2], v_lin, dst + 8192); dma4_sc1(src[3], v_lin, dst + 12288); }
      else {
#pragma unroll
        for (int q = 0; q < 4; ++q) dma4p(src[q], v0, v0 + st, v0 + 2 * st, v0 + 3 * st, dst + q * 4096);
      }
    } else {
      if (kind == 1u) { dma4_sc1f(src[0], v_lin, dst); dma4_sc1f(src[1], v_lin, dst + 4096); dma4_sc1f(src[2], v_lin, dst + 8192); dma4_sc1f(src[3], v_lin, dst + 12288); }
      else {
#pragma unroll
        for (int q = 0; q < 4; ++q) dma4(src[q], v0, v0 + st, v0 + 2 * st, v0 + 3 * st, dst + q * 4096);
      }
    }
    if (pend) { wait_vm<16>(); lds_add(pa, ((s - 2) & (NSLOT - 1)) * 4, 4); LSTAMP(tlan, s - 2); }
    LSTAMP(tis2, s);
    pend = true;
    if (s < 6u) {
      wait_vm<0>(); lds_add(pa, s * 4, 4); LSTAMP(tlan, s); pend = false;
      const unsigned a = tr_lane(lane) + s * 16384u;
      float c0 = 0.f, c1 = 0.f;
#pragma unroll
      for (int n = 0; n < 16; ++n) {
        const v4u f = __builtin_bit_cast(v4u, ldw(a + n * 1024, 512));
        c0 = dot2(f.x, ONES, c0); c1 = dot2(f.y, ONES, c1); c0 = dot2(f.z, ONES, c0); c1 = dot2(f.w, ONES, c1);
      }
      const float t = sum_halves(c0 + c1);
      vst<unsigned>(L_CSF + 2 * 512 + (lane & 31) * 16 + lw * 4, pk2(t, t - blo(pk2(t, t))));
      lds_add(pa, L_CSC - L_RDY + 4, 1);
    }
    if (s + 2 >= NT) { wait_vm<0>(); lds_add(pa, (s & (NSLOT - 1)) * 4, 4); }
  }
}
DI void server(const Params& P, int p, int m, int sw) {
  const int lane = fresh_lane();
  unsigned epoch;
  asm volatile("s_load_dword %0, %1, 0x0" : "=s"(epoch) : "s"(P.ws) : "memory");
  const unsigned pa = lane == 0 ? L_RDY : L_DUMMY + lane * 4;
  const rsrc_t rs = __builtin_amdgcn_make_buffer_rsrc((void*)P.ws, 0, WS_END, 0x00020000);
  CW c;
  c.lane = lane; c.ws = P.ws; c.budget = BUDGET;
  asm volatile("s_waitcnt lgkmcnt(0)" : "+s"(epoch) :: "memory");
  c.tag = epoch + 1u;
  const int co = WS_CSP + (m * 2 + sw) * 16 * 768;
  {
    const int ks = 2 * p + sw, r2 = m < 11 ? 2 : 1;
    const GAS char* sq = (const GAS char*)P.wqkv + m * 64 + ks * (16 * 3072) + (lane >> 2) * 3072 + (lane & 3) * 16;
    const unsigned vg = m * 64 + ks * (16 * 2752) + (lane >> 2) * 2752 + (lane & 3) * 16;
    const GAS char* sg = (const GAS char*)P.wg + vg; const GAS char* su = (const GAS char*)P.wu + vg;
    v4u rp[9];
    rp[0] = *(const GAS v4u*)(sg); rp[1] = *(const GAS v4u*)(su); rp[2] = *(const GAS v4u*)(sg + 1024); rp[3] = *(const GAS v4u*)(su + 1024);
    rp[4] = *(const GAS v4u*)(sg + r2 * 1024); rp[5] = *(const GAS v4u*)(su + r2 * 1024);
    rp[6] = *(const GAS v4u*)(sq + 1024); rp[7] = *(const GAS v4u*)(sq + 2048); rp[8] = *(const GAS v4u*)(sq);
    SCHED();
    const int dg = (m * 32 + ks) * 1024, dq = WS_RQ + (m * 96 + ks) * 1024;
    st16(rs, rp[0], lane * 16, WS_RG + dg); st16(rs, rp[1], lane * 16, WS_RU + dg);
    st16(rs, rp[2], lane * 16, WS_RG + dg + 16 * 32768); st16(rs, rp[3], lane * 16, WS_RU + dg + 16 * 32768);
    st16(rs, rp[4], lane * 16, WS_RG + dg + r2 * (16 * 32768)); st16(rs, rp[5], lane * 16, WS_RU + dg + r2 * (16 * 32768));
    st16(rs, rp[6], lane * 16, dq); st16(rs, rp[7], lane * 16, dq + 32768); st16(rs, rp[8], lane * 16, dq + 65536);
    const unsigned SCX = L_CSF + 3072 + sw * 1024;
    float cs[6];
#pragma unroll
    for (int u = 0; u < 6; ++u) {
      vst<v4u>(SCX + lane * 16, rp[u]);
      CFENCE();
      const v4u f = __builtin_bit_cast(v4u, ldw(SCX + tr_lane(lane), 512));
      CFENCE();
      cs[u] = sum_halves(dot2(f.x, ONES, dot2(f.y, ONES, 0.f)) + dot2(f.z, ONES, dot2(f.w, ONES, 0.f)));
    }
#pragma unroll
    for (int u = 0; u < 3; ++u) __builtin_amdgcn_raw_buffer_store_b32(f2u(lane < 32 ? cs[2 * u] : cs[2 * u + 1]), rs, lane * 4 + u * 256, co + p * 768, 16);
    vst<v4u>(SCX + lane * 16, v4u{0u, 0u, 0u, 0u});
    {
      const unsigned b = blockIdx.x;
      const char* tb = sw ? P.wo : P.wd;
      const unsigned to = sw ? (b * 16u + (lane & 15)) * 128u : (b * 43u + min(lane, 42)) * 128u;
      asm volatile("s_mov_b32 m0, %2\n\ts_nop 3\n\tglobal_load_lds_dword %0, %1" :: "v"(to), "s"(uni(tb)), "s"((unsigned)(uintptr_t)(LAS char*)smem + L_DUMMY) : "memory");
    }
    DRAIN();
    lds_add(pa, L_RPD - L_RDY, 1);
    while (rfl(vld<unsigned>(L_RPD)) < 2u && !spent(c)) __builtin_amdgcn_s_sleep(1);
  }
  for (unsigned layer = 0; layer < 2; ++layer) {
    __builtin_amdgcn_s_setprio(3);
    f32x4 tk[4], tz[4];
    for (;;) {
      const v4u fl = vld<v4u>(L_F1);
      if (min4(fl) > layer || spent(c)) break;
      __builtin_amdgcn_s_sleep(1);
    }
#pragma unroll
    for (int u = 0; u < 4; ++u) { tk[u] = vld<f32x4>(L_KVS + u * 2048 + sw * 1024 + lane * 16); tz[u] = vld<f32x4>(L_ZS + u * 128 + (lane & 3) * 32 + sw * 16); }
    const int go = WS_KVP + ((layer * 16 + m) * 2 + sw) * 16 * KVW, fo = WS_FKV + ((layer * 16 + m) * 2 + sw) * 256;
    v4u sk, sz;
#pragma unroll
    for (int e = 0; e < 4; ++e) { sk[e] = f2u(fadd1(fadd1(tk[0][e], tk[1][e]), fadd1(tk[2][e], tk[3][e]))); sz[e] = f2u(fadd1(fadd1(tz[0][e], tz[1][e]), fadd1(tz[2][e], tz[3][e]))); }
    st16(rs, sk, lane * 16, go + p * KVW);
    st16(rs, sz, 1024 + (lane & 7) * 16, go + p * KVW);
    DRAIN();
    set_flag(c, fo + p * 4);
    wait_flags(c, fo, 0xffffull);
    const v4u zl = ld16(rs, 1024 + (lane & 15) * KVW + (lane >> 4) * 16, go);
    v4u g[16];
#pragma unroll
    for (int i = 0; i < 16; ++i) g[i] = ld16(rs, lane * 16, go + i * KVW);
    SCHED();
    const float z0 = row_sum(u2f(zl.x)), z1 = row_sum(u2f(zl.y)), z2 = row_sum(u2f(zl.z)), z3 = row_sum(u2f(zl.w));
    if ((lane & 15) == 0) vst<v2u>(L_ZR + (lane >> 4) * 16 + sw * 8, v2u{pk2(z0, z1), pk2(z2, z3)});
    f32x4 r = __builtin_bit_cast(f32x4, g[0]);
#pragma unroll
    for (int i = 1; i < 16; ++i) r += __builtin_bit_cast(f32x4, g[i]);
    vst<v2u>(L_KVR + lane * 16 + sw * 8, v2u{pk2(r.x, r.y), pk2(r.z, r.w)});
    lds_add(pa, L_F2 - L_RDY, 2);
    __builtin_amdgcn_s_setprio(0);
    if (layer == 0) {
      v4u t[16];
#pragma unroll
      for (int i = 0; i < 16; ++i) t[i] = ld16(rs, min(lane, 47) * 16, co + i * 768);
      SCHED();
      f32x4 v = __builtin_bit_cast(f32x4, t[0]);
#pragma unroll
      for (int i = 1; i < 16; ++i) v += __builtin_bit_cast(f32x4, t[i]);
      if (lane < 48) {
#pragma unroll
        for (int e = 0; e < 4; ++e) vst<unsigned>(L_CSF + 2048 + (lane * 4 + e) * 16 + sw * 4, pk2(v[e], v[e] - blo(pk2(v[e], v[e]))));
      }
      lds_add(pa, L_CSC - L_RDY + 8, 2); lds_add(pa, L_CSC - L_RDY + 12, 2); lds_add(pa, L_CSC - L_RDY + 16, 2);
    }
  }
}
template <int AT, bool TWO> DI void cs_hook(CW& c, int i, unsigned k, v4u& a, v4u& b) {
  if (i == AT) a.x = vld<unsigned>(L_CSC + k * 4);
  if (i == AT + 3) {
    wait_count(c, a.x, L_CSC + k * 4, TWO ? 4u : 2u);
    a = vld<v4u>(L_CSF + k * 1024 + (c.lane & 31) * 16);
    if (TWO) b = vld<v4u>(L_CSF + k * 1024 + 512 + (c.lane & 31) * 16);
  }
}

__global__ void __launch_bounds__(512, 2) fused_fwd(Params P) {
  const int tid = threadIdx.x; int lane = tid & 63; const int wv = __builtin_amdgcn_readfirstlane(tid >> 6);
  const int b = blockIdx.x;
  {
    const unsigned co = CODE_BYTES - 256u - min(((b >> 3) * 8 + wv) * 256u, CODE_BYTES - 256u) + lane * 4;
    unsigned long long pc;
    asm volatile("s_mov_b32 m0, %2\n\ts_getpc_b64 %0\n\tglobal_load_lds_dword %1, %0" : "=&s"(pc) : "v"(co), "s"((unsigned)(uintptr_t)(LAS char*)smem + L_DUMMY) : "memory");
  }
  int p, m;
  if (MAPMODE == 0) { p = (b & 7) * 2 + (b >> 7); m = (b >> 3) & 15; }
  else if (MAPMODE == 1) { const int x = b & 7, i = b >> 3; p = (x >> 1) * 4 + (i >> 3); m = (x & 1) * 8 + (i & 7); }
  else { const int x = b & 7, i = b >> 3; p = i >> 1; m = x * 2 + (i & 1); }
  if (tid < (L_END - L_RDY) / 4) vst<unsigned>(L_RDY + tid * 4, 0u);
  if (tid < 320) vst<v4u>(L_CSF + tid * 16, v4u{0u, 0u, 0u, 0u});
  asm volatile("s_waitcnt lgkmcnt(0)\n\ts_barrier" ::: "memory");
  const unsigned smem_base = (unsigned)(uintptr_t)(LAS char*)smem;
  int w = wv & 3;
  unsigned xv[4];
  const char* xbase = (wv < 4 ? P.x : P.wqkv) + (unsigned)sopaque(wv < 4 ? (4 * p + w) * 32768 : ((w >> 1) + 1) * 1024 + m * 64);
  auto xbuf = [&](int q) -> unsigned { const int j = q % 5; return (j == 0 ? L_SCR : j == 1 ? 7 * 16384 : j == 2 ? 6 * 16384 : j == 3 ? 4 * 16384 : 5 * 16384) + w * 4096; };
  {
#pragma unroll
    for (int k = 0; k < 4; ++k) {
      const unsigned a = (8 * k + (lane >> 3)) * 1024 + (((lane & 7) ^ ((4 * k + (lane >> 4)) & 7)) * 16) - k * 1024;
      const unsigned c = (lane >> 2) * 3072 + (lane & 3) * 16 + k * 48128;
      xv[k] = wv < 4 ? a : c;
    }
    const unsigned cw = wv < 4, stride = sopaque(cw ? 128 : 64 * 3072), r0 = ROT(m) + (cw ? 0 : (w & 1)), sh = cw ? 0 : 1;
#pragma unroll
    for (int j = 0; j < 2; ++j)
      dma4(xbase + (((j << sh) + r0) & 7) * stride, xv[0], xv[1], xv[2], xv[3], smem_base + (cw ? xbuf(j) : j * 16384 + w * 4096));
    if (!cw) {
      const unsigned pa = smem_base + (lane == 0 ? L_RDY : L_DUMMY + lane * 4), a = tr_lane(lane) + w * 4096u;
      float c0 = 0.f, c1 = 0.f;
      auto cs = [&](int j) __attribute__((always_inline)) {
#pragma unroll
        for (int n = 0; n < 4; ++n) {
          const v4u f = __builtin_bit_cast(v4u, ldw(a + j * 16384 + n * 1024, 512));
          c0 = dot2(f.x, ONES, c0); c1 = dot2(f.y, ONES, c1); c0 = dot2(f.z, ONES, c0); c1 = dot2(f.w, ONES, c1);
        }
        PIN(c0); PIN(c1);
      };
      asm volatile("s_waitcnt vmcnt(4)\n\tds_add_u32 %0, %1" ::"v"(pa), "v"(1u) : "memory");
      dma4(xbase + ((4 + r0) & 7) * stride, xv[0], xv[1], xv[2], xv[3], smem_base + 2 * 16384 + w * 4096);
      asm volatile("s_waitcnt vmcnt(4)\n\tds_add_u32 %0, %1 offset:4" ::"v"(pa), "v"(1u) : "memory");
      dma4(xbase + ((6 + r0) & 7) * stride, xv[0], xv[1], xv[2], xv[3], smem_base + 3 * 16384 + w * 4096);
      cs(0);
      asm volatile("s_waitcnt vmcnt(4)\n\tds_add_u32 %0, %1 offset:8" ::"v"(pa), "v"(1u) : "memory");
      cs(1);
      asm volatile("s_waitcnt vmcnt(0)\n\tds_add_u32 %0, %1 offset:12" ::"v"(pa), "v"(1u) : "memory");
      cs(2); cs(3);
      const float t = sum_halves(c0 + c1);
      vst<unsigned>(L_CSF + (w >> 1) * 512 + (lane & 31) * 16 + (w & 1) * 4, pk2(t, t - blo(pk2(t, t))));
      asm volatile("ds_add_u32 %0, %1 offset:%c2" ::"v"(pa), "v"(1u), "i"(L_CSC - L_RDY) : "memory");
    }
  }
  STAMPV(0, t00);
  STAMP(61);
  if (sopaque(wv) >= 6) { server(P, p, m, wv - 6); STAMP(1); STAMP_END(); return; }
  if (sopaque(wv) >= 4) { loader(P, p, m, wv - 4); STAMP(1); STAMP_END(); return; }
  __builtin_amdgcn_s_setprio(2);

  unsigned epoch;
  asm volatile("s_load_dword %0, %1, 0x0" : "=s"(epoch) : "s"(P.ws) : "memory");
  v4u B[32];
  auto xdma = [&](int q) __attribute__((always_inline)) { dma4(xbase + ((q + ROT(m)) & 7) * 128, xv[0], xv[1], xv[2], xv[3], smem_base + xbuf(q)); };
  xdma(2);
  const rsrc_t rs = __builtin_amdgcn_make_buffer_rsrc((void*)P.ws, 0, WS_END, 0x00020000);
  asm volatile("s_waitcnt lgkmcnt(0)" : "+s"(epoch) :: "memory");
  STAMP(19);
  CW c;
  c.s = 0; c.pos = 0; c.rdy = 0; c.perm = 4; c.lane_tr = tr_lane(lane); c.fa = lane == 0 ? L_FCNT + w * 4 : L_DUMMY + lane * 4;
  c.budget = BUDGET; c.lane = lane; c.w = w; c.ws = P.ws; c.tag = epoch + 1u;
  int h, t32, tbq, tbw, kh, cb;
#define FRESH() do { lane = opaque(lane); h = lane >> 5; t32 = lane & 31; c.lane = lane; p = sopaque(p); m = sopaque(m); w = sopaque(w); c.w = w; \
    tbq = layer * 64 + 4 * p + w; kh = w >> 1; tbw = layer * 64 + 4 * p + 2 * (m >> 3) + (w & 1); cb = 2 * (m & 7) + kh; } while (0)
  unsigned R[8];

  int layer = 0;
    f32x16 aK = {}, aV = {};
    float la; unsigned mf;
    f32x4 ar[4];
    v4u ck, cv;
    FRESH();
    auto rows_ab = [&]() __attribute__((always_inline)) {
      vst<float>(L_SA + w * 128 + t32 * 4, la);
#pragma unroll
      for (int g = 0; g < 4; ++g) ar[g] = vld<f32x4>(L_SA + w * 128 + (8 * g + 4 * h) * 4);
    };
    {
      float s1 = 0.f, s2 = 0.f, s3 = 0.f, s4 = 0.f;
      const unsigned xl = t32 * 128 + 8 * h, xf = (t32 >> 1) & 7;
      auto hook = [&](int i, int hf) __attribute__((always_inline)) {
        const int n = i + 2;
        if (n >= 0 && n < 32 && hf == 0) {
          const int q = n / 4, j = n % 4;
          if (j == 0) {
            if (q == 0) wait_vm<8>();
            if (q == 1 || q == 2 || q == 3) wait_vm<12>();
            if (q == 4) wait_vm<12>();
            if (q == 5) wait_vm<8>();
            if (q == 6) wait_vm<4>();
            if (q == 7) wait_vm<0>();
          }
          const v2u lo = vld<v2u>(xbuf(q) + xl + (((2 * j) ^ xf) * 16)), hi = vld<v2u>(xbuf(q) + xl + (((2 * j + 1) ^ xf) * 16));
          B[n] = v4u{lo.x, lo.y, hi.x, hi.y};
        }
        if (hf == 1) {
          if (i == -1) xdma(3);
          if (i == 1) xdma(4);
          if (i == 3 || i == 7 || i == 11) xdma(5 + i / 4);
          if (i >= 0 && i % 4 == 3) {
#pragma unroll
            for (int u = i - 3; u <= i; ++u) {
              const v4u f = B[u];
              s1 = dot2(f.x, ONES, s1); s2 = dot2(f.x, f.x, s2); s3 = dot2(f.y, ONES, s3); s4 = dot2(f.y, f.y, s4);
              s1 = dot2(f.z, ONES, s1); s2 = dot2(f.z, f.z, s2); s3 = dot2(f.w, ONES, s3); s4 = dot2(f.w, f.w, s4);
            }
            PIN(s1); PIN(s2); PIN(s3); PIN(s4);
          }
          cs_hook<26, true>(c, i, 0, ck, cv);
          if (n == 15 || n == 19) permit(c, n / 4 + 2);
          if (i == 30) permit(c, 10);
        }
      };
      gemm_pair<true>(c, B, aK, aV, hook);
      ln_factors(sum_halves(s1 + s3), sum_halves(s2 + s4), la, mf, h != 0);
      rows_ab();
    }
  for (; layer < 2; ++layer) {
    const bf16x8 mfr = asb(v4u{mf, mf, mf, mf});
    aK = mfma(mfr, asb(ck), aK);
    aV = mfma(mfr, asb(cv), aV);
    STAMPB(2 + 20 * layer);
    FRESH();
    {
      unsigned pkk[8], pkv[8];
#pragma unroll
      for (int j = 0; j < 8; ++j) {
        const f32x2 a = (j & 1) ? f32x2{ar[j >> 1].z, ar[j >> 1].w} : f32x2{ar[j >> 1].x, ar[j >> 1].y};
        pkk[j] = phi2(pair(aK, j), a);
        pkv[j] = pk2(pair(aV, j) * a);
      }
      const bf16x8 k0 = asb(v4u{pkk[0], pkk[1], pkk[2], pkk[3]}), k1 = asb(v4u{pkk[4], pkk[5], pkk[6], pkk[7]});
      const bf16x8 v0 = asb(v4u{pkv[0], pkv[1], pkv[2], pkv[3]}), v1 = asb(v4u{pkv[4], pkv[5], pkv[6], pkv[7]});
      const bf16x8 one = asb(v4u{ONES, ONES, ONES, ONES});
      f32x16 kv = {}, zz = {};
      kv = mfma(k0, v0, kv); zz = mfma(k0, one, zz); kv = mfma(k1, v1, kv); zz = mfma(k1, one, zz);
      const bool hd = (lane & 16) != 0;
      f32x4 q0, q1, z0, z1;
#pragma unroll
      for (int j = 0; j < 4; ++j) { q0[j] = sel(hd, kv[8 + j], kv[j]); q1[j] = sel(hd, kv[12 + j], kv[4 + j]); }
      vst<f32x4>(L_KVS + w * 2048 + lane * 16, q0);
      vst<f32x4>(L_KVS + w * 2048 + 1024 + lane * 16, q1);
#pragma unroll
      for (int j = 0; j < 4; ++j) { z0[j] = sel(hd, zz[8 + j], zz[j]); z1[j] = sel(hd, zz[12 + j], zz[4 + j]); }
      vst<f32x4>(L_ZS + w * 128 + (lane >> 4) * 32, z0);
      vst<f32x4>(L_ZS + w * 128 + (lane >> 4) * 32 + 16, z1);
      post(c, L_F1, layer + 1);
    }
    STAMPB(3 + 20 * layer);
    unsigned pq[8];
    {
      f32x16 aQ = {};
      v4u c0;
      auto hook = [&](int i) __attribute__((always_inline)) {
        if (i == 20) {
          if (layer == 0) {
            const GAS char* xr = (const GAS char*)P.x + (size_t)(tbw * 32 + t32) * 1024 + cb * 64 + h * 8;
#pragma unroll
            for (int g = 0; g < 4; ++g) { const v2u v = *(const GAS v2u*)(xr + g * 16); R[2 * g] = v.x; R[2 * g + 1] = v.y; }
          }
        }
        cs_hook<24, false>(c, i, 1, c0, c0);
      };
      gemm_single(c, B, aQ, hook);
      aQ = mfma(asb(c0), mfr, aQ);
#pragma unroll
      for (int j = 0; j < 8; ++j) { pq[j] = phi2(pair(aQ, j), f32x2{la, la}); PIN(pq[j]); }
    }
    STAMPB(4 + 20 * layer);
    {
      FRESH();
      f32x16 aM = {}, aO = {};
      Wide<16> d;
      const unsigned s0 = c.s;
      wide_steps<16, -16, 0>(c, d, B, aM, aO, kh, NoHook());
      STAMPB(54);
      v4u fk, fz;
      for (;;) {
        const unsigned fl = vld<unsigned>(L_F2);
        fk = vld<v4u>(L_KVR + lane * 16); fz = vld<v4u>(L_ZR + (lane >> 4) * 16);
        if (rfl(fl) >= 4u * layer + 4u || spent(c)) break;
      }
      STAMPB(5 + 20 * layer);
      permit(c, s0 + 8);
      const bool hd = (lane & 16) != 0;
      const v4u zero = {0u, 0u, 0u, 0u};
      const bf16x8 p0 = asb(v4u{pq[0], pq[1], pq[2], pq[3]}), p1 = asb(v4u{pq[4], pq[5], pq[6], pq[7]});
      f32x16 num = {}, den = {};
      den = mfma(asb(hd ? zero : fz), p0, den); den = mfma(asb(hd ? fz : zero), p1, den);
      num = mfma(asb(hd ? zero : fk), p0, num); num = mfma(asb(hd ? fk : zero), p1, num);
      const float i0 = __builtin_amdgcn_rcpf(den[0] + 1e-6f), i1 = __builtin_amdgcn_rcpf(den[8] + 1e-6f);
      const int os = WS_O + (tbq * 32 + 2 * m) * 1024;
      st16(rs, v4u{pk2(pair(num, 0) * i0), pk2(pair(num, 1) * i0), pk2(pair(num, 2) * i0), pk2(pair(num, 3) * i0)}, lane * 16, os);
      st16(rs, v4u{pk2(pair(num, 4) * i1), pk2(pair(num, 5) * i1), pk2(pair(num, 6) * i1), pk2(pair(num, 7) * i1)}, lane * 16 + 1024, os);
      DRAIN();
      set_flag(c, WS_FO + tbq * 256 + m * 4);
      STAMPB(6 + 20 * layer);
      FRESH();
      wait_flags(c, WS_FO + tbw * 256, kh ? 0xccccull : 0x3333ull);
      STAMPB(7 + 20 * layer);
      const int oo = WS_O + (tbw * 32 + 4 * kh) * 1024;
#pragma unroll
      for (int i = 0; i < AW; ++i) B[i] = ld16(rs, lane * 16 + (i & 3) * 1024, oo + (i >> 2) * 8192);
      SCHED();
      auto hook = [&](int i, int hf) __attribute__((always_inline)) {
        const int n = i + AW;
        if (hf == 0) return;
        if (n < 16) B[n] = ld16(rs, lane * 16 + (n & 3) * 1024, oo + (n >> 2) * 8192);
        if (n == 16) permit(c, s0 + 12);
      };
      wide_steps<16, 0, 16>(c, d, B, aM, aO, kh, hook);
      STAMPB(8 + 20 * layer);
      FRESH();
      finish_wide(c, aM, aO, R, 2 * layer + 1);
      publish_x(c, rs, R, WS_X1 + (tbw * 32 + 2 * cb) * 1024, WS_ST1 + (tbw * 16 + cb) * 256, WS_FX1 + tbw * 256 + cb * 4);
      vst<v4u>(L_KVS + w * 2048 + lane * 16, v4u{R[0], R[1], R[2], R[3]});
      vst<v4u>(L_KVS + w * 2048 + 1024 + lane * 16, v4u{R[4], R[5], R[6], R[7]});
    }
    STAMPB(9 + 20 * layer);
    unsigned pf;
    f32x16 aM = {}, aO = {};
    Wide<3> d;
    unsigned sd;
    int fd, hd0, fl2; unsigned long long mk;
    auto ldh = [&](int n) __attribute__((always_inline)) {
      if (n < 32) B[n % 16] = ld16(rs, lane * 16 + (n & 3) * 1024, hd0 + (((n >> 2) & 4) | (((n >> 2) + ROT(m)) & 3)) * 8192);
      else if (n < 40) B[n % 16] = ld16(rs, lane * 16 + (n & 3) * 1024, hd0 + (n >> 2) * 8192);
      else B[n % 16] = ld16(rs, lane * 16 + (n & 3) * 1024, hd0 + 81920 - kh * 1024);
    };
    {
      FRESH();
      f32x16 G0 = {}, U0 = {}, G1 = {}, U1 = {};
      Pipe q;
      const unsigned s0 = c.s;
      pair_steps<false, -PD, 0, 64, false>(c, q, B, G0, U0, NoHook());
      wait_flags(c, WS_FX1 + tbq * 256, 0xffffull);
      STAMPB(10 + 20 * layer);
      Stats sv;
      const int xo = WS_X1 + tbq * 32768;
#pragma unroll
      for (int i = 0; i < AH; ++i) B[i] = ld16(rs, lane * 16 + (i & 3) * 1024, xo + (((i >> 2) + ROT(m)) & 7) * 4096);
      SCHED();
      float a2; unsigned gm;
      v4u cg, cu;
      unsigned hp[8];
      const int ho = WS_H + (tbq * 86 + 2 * m) * 1024, fo = WS_FH + tbq * 256 + m * 4;
      float hv;
      fd = WS_FH + tbw * 256; mk = kh ? 0xccccull : 0x3333ull; hd0 = WS_H + (tbw * 86 + 4 * kh) * 1024;
      fl2 = fo + 128;
      auto piece = [&](int e, const f32x16& G, const f32x16& U) __attribute__((always_inline)) {
        float g;
        if (e & 1) asm volatile("v_max_f32 %1, 0, %2\n\tv_mul_f32 %0, %3, %4\n\tv_mul_f32 %1, %1, %0\n\tv_cvt_pk_bf16_f32 %0, %5, %1" : "=&v"(hp[e >> 1]), "=&v"(g) : "v"(G[e]), "v"(U[e]), "v"(a2), "v"(hv));
        else asm volatile("v_max_f32 %1, 0, %2\n\tv_mul_f32 %0, %3, %4\n\tv_mul_f32 %0, %1, %0" : "=&v"(hv), "=&v"(g) : "v"(G[e]), "v"(U[e]), "v"(a2));
      };
      auto tail = [&](const f32x16& G, const f32x16& U) __attribute__((always_inline)) {
#pragma unroll
        for (int j = 0; j < 8; ++j) {
          const f32x2 g = {fmaxf(G[2 * j], 0.f), fmaxf(G[2 * j + 1], 0.f)};
          hp[j] = pk2(g * (pair(U, j) * a2));
        }
      };
      auto store_h = [&](int r, int half) __attribute__((always_inline)) { st16(rs, v4u{hp[4 * half], hp[4 * half + 1], hp[4 * half + 2], hp[4 * half + 3]}, lane * 16 + half * 1024, ho + r * 32768); };
      auto correct = [&](f32x16& G, f32x16& U) __attribute__((always_inline)) {
        const bf16x8 f = asb(v4u{gm, gm, gm, gm});
        G = mfma(asb(cg), f, G); U = mfma(asb(cu), f, U);
      };
      auto shadow = [&](int i, int hf, int r, const f32x16& G, const f32x16& U) __attribute__((always_inline)) {
        constexpr int at[16] = {3, 5, 7, 9, 12, 13, 14, 15, 17, 19, 21, 23, 25, 28, 29, 30};
#pragma unroll
        for (int e = 0; e < 16; ++e) if (2 * i + hf == at[e]) piece(e, G, U);
        if (2 * i + hf == 31) store_h(r, 0);
        if (2 * i + hf == 33) store_h(r, 1);
        if (hf == 0 && i == 25) { if (r == 0) DRAIN(); else wait_vm<8>(); set_flag(c, fo + r * 64); }
        if (hf == 1) cs_hook<25, true>(c, i, 3 + r, cg, cu);
        if (r == 1) {
          if (hf == 0 && i == 8) pf = poll_issue(c, fd, mk);
          if (hf == 0 && i == 17) { poll_done<2>(c, pf, fd, mk); if (layer) STAMPB(51); }
          if (hf == 1 && i >= 17) ldh(i - 17);
          if (hf == 0 && i == 30) pf = poll_issue(c, fd, mk << 16);
        }
      };
      pair_steps<false, 0, 32, 64, false>(c, q, B, G0, U0, [&](int i, int hf) __attribute__((always_inline)) {
        const int n = i + AH;
        stats_hook(c, sv, 2 * i + hf, rs, WS_ST1 + tbq * 4096, a2, gm);
        if (hf == 0) return;
        if (n < 32) B[n] = ld16(rs, lane * 16 + (n & 3) * 1024, xo + (((n >> 2) + ROT(m)) & 7) * 4096);
        if (n == 32) permit(c, s0 + (m < 11 ? 23 : 19));
        if (i == 26) { a2 = fmul1(a2, a2); PIN(a2); }
        cs_hook<25, true>(c, i, 2, cg, cu);
      });
      correct(G0, U0);
      STAMPB(11 + 20 * layer);
      pair_steps<false, 32, 64, 96, false>(c, q, B, G1, U1, [&](int i, int hf) __attribute__((always_inline)) { shadow(i, hf, 0, G0, U0); });
      correct(G1, U1);
      STAMPB(12 + 20 * layer);
      pf = ~c.tag;
      if (sopaque(m) >= 11) {
        tail(G1, U1);
        store_h(1, 0); store_h(1, 1);
        DRAIN();
        set_flag(c, fo + 64);
        wait_flags(c, fd, mk);
      }
      if (sopaque(m) < 11) {
        G0 = f32x16{}; U0 = f32x16{};
        pair_steps<false, 64, 96, 96, false>(c, q, B, G0, U0, [&](int i, int hf) __attribute__((always_inline)) { shadow(i, hf, 1, G1, U1); });
        correct(G0, U0);
        tail(G0, U0);
        store_h(2, 0); store_h(2, 1);
        STAMPB(13 + 20 * layer);
      }
      if (sopaque(m) >= 11) {
#pragma unroll
        for (int i = 0; i < 15; ++i) ldh(i);
      }
      sd = c.s;
      wide_steps<43, -3, 0>(c, d, B, aM, aO, kh, NoHook());
    }
    STAMPB(14 + 20 * layer);
    {
      unsigned pf2;
      auto hook = [&](int i, int hf) __attribute__((always_inline)) {
        if (hf == 0) {
          if (i == 7) { wait_vm<7>(); set_flag(c, fl2); }
          if (i == 13) pf2 = poll_issue(c, fd, 0x7ffull << 32);
          return;
        }
        if (i == 1) { poll_done<5>(c, pf, fd, mk << 16); if (layer) STAMPB(47); }
        if (i == 19 && layer) STAMPB(48);
        if (i == 31 && layer) STAMPB(50);
        if (i + 15 < 32) ldh(i + 15);
        if (i == 20) { poll_done<4>(c, pf2, fd, 0x7ffull << 32); if (layer) STAMPB(49); }
        if (i >= 20 && i <= 23) { ldh(2 * i - 8); ldh(2 * i - 7); }
        if (i >= 24 && i <= 26) ldh(i + 16);
        if (i == PKVAT) permit(c, sd + 11 + PKV);
        if (i == 36) {
          const v4u r0 = vld<v4u>(L_KVS + w * 2048 + lane * 16), r1 = vld<v4u>(L_KVS + w * 2048 + 1024 + lane * 16);
          R[0] = r0.x; R[1] = r0.y; R[2] = r0.z; R[3] = r0.w; R[4] = r1.x; R[5] = r1.y; R[6] = r1.z; R[7] = r1.w;
        }
      };
      wide_steps<43, 0, 43>(c, d, B, aM, aO, kh, hook);
      STAMPB(17 + 20 * layer);
      FRESH();
      finish_wide(c, aM, aO, R, 2 * layer + 2);
    }
#pragma unroll
    for (int i = 0; i < 32; ++i) asm volatile("" : "=v"(B[i]));
    asm volatile("" : "=v"(aK), "=v"(aV), "=v"(la), "=v"(mf), "=v"(ck), "=v"(cv));
    asm volatile("" : "=v"(ar[0]), "=v"(ar[1]), "=v"(ar[2]), "=v"(ar[3]));
    if (layer == 0) {
      publish_x(c, rs, R, WS_X2 + (tbw * 32 + 2 * cb) * 1024, WS_ST2 + (tbw * 16 + cb) * 256, WS_FX2 + tbw * 256 + cb * 4);
      STAMPB(18);
      FRESH();
      aK = f32x16{}; aV = f32x16{};
      Pipe q;
      const unsigned s0 = c.s;
      pair_steps<true, -PD, 0, 32, false>(c, q, B, aK, aV, NoHook());
      wait_flags(c, WS_FX2 + tbq * 256, 0xffffull);
      STAMPB(20);
      Stats sv;
      const int xo = WS_X2 + tbq * 32768;
#pragma unroll
      for (int i = 0; i < AH; ++i) B[i] = ld16(rs, lane * 16 + (i & 3) * 1024, xo + (((i >> 2) + ROT(m)) & 7) * 4096);
      SCHED();
      auto hook = [&](int i, int hf) __attribute__((always_inline)) {
        const int n = i + AH;
        stats_hook(c, sv, 2 * i + hf, rs, WS_ST2 + tbq * 4096, la, mf);
        if (hf == 0) {
          if (i == 25) vst<float>(L_SA + w * 128 + t32 * 4, la);
          if (i == 26) { ar[0] = vld<f32x4>(L_SA + w * 128 + 4 * h * 4); ar[1] = vld<f32x4>(L_SA + w * 128 + (8 + 4 * h) * 4); }
          if (i == 27) { ar[2] = vld<f32x4>(L_SA + w * 128 + (16 + 4 * h) * 4); ar[3] = vld<f32x4>(L_SA + w * 128 + (24 + 4 * h) * 4); }
          return;
        }
        if (n < 32) B[n] = ld16(rs, lane * 16 + (n & 3) * 1024, xo + (((n >> 2) + ROT(m)) & 7) * 4096);
        if (n == 32) permit(c, s0 + KVP2);
        cs_hook<26, true>(c, i, 0, ck, cv);
      };
      pair_steps<true, 0, 32, 32, false>(c, q, B, aK, aV, hook);
    }
  }
  {
    layer = 0;
    FRESH();
    GAS char* o = (GAS char*)P.out + (size_t)(tbw * 32 + t32) * 1024 + cb * 64 + h * 8;
#pragma unroll
    for (int g = 0; g < 4; ++g) *(GAS v2u*)(o + g * 16) = v2u{R[2 * g], R[2 * g + 1]};
  }
  STAMPB(38);
  STAMP_END();
  STAMPV(42, (unsigned long long)(BUDGET - c.budget));
  if (b == 0 && w == 0 && lane == 0) *(GAS unsigned*)(P.ws + WS_EPOCH) = c.tag;
}
}

extern "C" void kernel_launch(void* const* d_in, const int* in_sizes, int n_in, void* d_out, int out_size, void* d_ws,
                              size_t ws_size, hipStream_t stream) {
  Params P;
  P.x = (const char*)d_in[0]; P.wqkv = (const char*)d_in[1]; P.wo = (const char*)d_in[2];
  P.wg = (const char*)d_in[4]; P.wu = (const char*)d_in[5]; P.wd = (const char*)d_in[6];
  P.out = (char*)d_out; P.ws = (char*)d_ws;
  (void)hipFuncSetAttribute((const void*)fused_fwd, hipFuncAttributeMaxDynamicSharedMemorySize, LDS_BYTES);
  fused_fwd<<<256, 512, LDS_BYTES, stream>>>(P);
}
```

```cpp
#include <hip/hip_runtime.h>
#include <stdint.h>

#define GAS __attribute__((address_space(1)))
#define LAS __attribute__((address_space(3)))
#define DI __device__ __forceinline__
typedef unsigned v4u __attribute__((ext_vector_type(4)));
typedef unsigned v2u __attribute__((ext_vector_type(2)));
typedef float f32x16 __attribute__((ext_vector_type(16)));
typedef float f32x4 __attribute__((ext_vector_type(4)));
typedef float f32x2 __attribute__((ext_vector_type(2)));
typedef __bf16 bf16x8 __attribute__((ext_vector_type(8)));
typedef __bf16 bf16x2 __attribute__((ext_vector_type(2)));
typedef short s16x4 __attribute__((ext_vector_type(4)));

#define MAPMODE 1
#define LDEPTH 8
#define LPRIO 0
#define LSLEEP 1
#define AW 8
#define PKV 8
#define THIN 2
#define PD 2
#define PKVAT 16
#define THINB 18
#define KVP2 14
#define PACEB 10
#define XUP 2
#define RPP 0
#define AH 8

#define ROT(m) (((m) + 2 * p) & 7)
namespace {
constexpr int KB = 1024;
constexpr int WS_EPOCH = 0;
constexpr int WS_FKV = 4096;
constexpr int WS_FO = WS_FKV + 32 * KB;
constexpr int WS_FX1 = WS_FO + 32 * KB;
constexpr int WS_FH = WS_FX1 + 32 * KB;
constexpr int WS_FX2 = WS_FH + 32 * KB;
constexpr int WS_ST1 = WS_FX2 + 16 * KB;
constexpr int WS_ST2 = WS_ST1 + 512 * KB;
constexpr int KVW = 1152;
constexpr int WS_KVP = WS_ST2 + 256 * KB;
constexpr int WS_CSP = WS_KVP + 2 * 16 * 16 * 2 * KVW;
constexpr int WS_O = WS_CSP + 16 * 2 * 16 * 768;
constexpr int WS_X1 = WS_O + 4096 * KB;
constexpr int WS_X2 = WS_X1 + 4096 * KB;
constexpr int WS_H = WS_X2 + 2048 * KB;
constexpr int WS_RQ = WS_H + 2 * 64 * 86 * KB;
constexpr int WS_RG = WS_RQ + 1536 * KB;
constexpr int WS_RU = WS_RG + 43 * 32 * KB;
constexpr int WS_DBG = WS_RU + 43 * 32 * KB;
constexpr int WS_END = WS_DBG + 256 * 8 * 64 * 8;
constexpr int NSLOT = 8;
constexpr int L_RING = 0;
constexpr int L_SCR = NSLOT * 16 * KB;
constexpr int L_KVS = L_SCR + 16 * KB;
constexpr int L_CSF = L_KVS + 8 * KB;
constexpr int L_SA = L_CSF + 5120;
constexpr int L_KVR = L_SA + 512;
constexpr int L_ZR = L_KVR + 1024;
constexpr int L_ZS = L_ZR + 64;
constexpr int L_DUMMY = L_ZS + 512;
constexpr int L_RDY = L_DUMMY + 448;
constexpr int L_CSC = L_RDY + 32;
constexpr int L_FCNT = L_CSC + 32;
constexpr int L_F1 = L_FCNT + 16;
constexpr int L_F2 = L_F1 + 16;
constexpr int L_F3 = L_F2 + 16;
constexpr int L_RPD = L_F3 + 16;
constexpr int L_END = L_RPD + 16;
static_assert(L_END <= 160 * KB, "LDS");
constexpr int LDS_BYTES = (L_END + 15) / 16 * 16;
constexpr unsigned CODE_BYTES = 43776;
constexpr unsigned BUDGET = 1u << 18;

struct Params {
  const char* x; const char* wqkv; const char* wo; const char* wg; const char* wu; const char* wd;
  char* out; char* ws;
};

extern __shared__ __attribute__((aligned(16))) char smem[];
template <class T> DI LAS T* lp(unsigned off) { return (LAS T*)((LAS char*)smem + off); }
template <class T> DI T vld(unsigned off) { return *(volatile LAS T*)((LAS char*)smem + off); }
template <class T> DI void vst(unsigned off, T v) { *(volatile LAS T*)((LAS char*)smem + off) = v; }
DI unsigned rfl(unsigned v) { return __builtin_amdgcn_readfirstlane(v); }
DI const char* uni(const char* p) { const unsigned long long v = (unsigned long long)p; return (const char*)(((unsigned long long)rfl((unsigned)(v >> 32)) << 32) | rfl((unsigned)v)); }
#define SCHED() __builtin_amdgcn_sched_barrier(0)
#define CFENCE() asm volatile("" ::: "memory")
#define DRAIN() asm volatile("s_waitcnt vmcnt(0)" ::: "memory")
#define PIN(x) asm volatile("" : "+v"(x))

#define STAMP(i) do {} while (0)
#define STAMPV(i, v) do {} while (0)
#define STAMPB(i) do {} while (0)
#define STAMP_END() do {} while (0)

DI bf16x8 ldw(unsigned a, int second) {
  s16x4 lo = __builtin_amdgcn_ds_read_tr16_b64_v4i16(lp<s16x4>(a));
  s16x4 hi = __builtin_amdgcn_ds_read_tr16_b64_v4i16(lp<s16x4>(a + second));
  return __builtin_bit_cast(bf16x8, __builtin_shufflevector(lo, hi, 0, 1, 2, 3, 4, 5, 6, 7));
}
DI f32x16 mfma(bf16x8 a, bf16x8 b, f32x16 c) { return __builtin_amdgcn_mfma_f32_32x32x16_bf16(a, b, c, 0, 0, 0); }
DI bf16x8 asb(v4u v) { return __builtin_bit_cast(bf16x8, v); }
DI unsigned pk2(float lo, float hi) { bf16x2 r = {(__bf16)lo, (__bf16)hi}; return __builtin_bit_cast(unsigned, r); }
DI unsigned pk2(f32x2 v) { return pk2(v.x, v.y); }
DI float dot2(unsigned a, unsigned b, float c) { return __builtin_amdgcn_fdot2_f32_bf16(__builtin_bit_cast(bf16x2, a), __builtin_bit_cast(bf16x2, b), c, false); }
DI float u2f(unsigned u) { return __builtin_bit_cast(float, u); }
DI unsigned f2u(float f) { return __builtin_bit_cast(unsigned, f); }
DI float sel(bool c, float a, float b) { asm volatile("" : "+v"(a), "+v"(b)); return c ? a : b; }
DI int opaque(int v) { asm volatile("" : "+v"(v)); return v; }
DI int sopaque(int v) { asm volatile("" : "+s"(v)); return v; }
DI float blo(unsigned u) { return u2f(u << 16); }
DI float bhi(unsigned u) { return u2f(u & 0xffff0000u); }
DI float sum_halves(float v) {
  auto r = __builtin_amdgcn_permlane32_swap(f2u(v), f2u(v), false, false);
  float o; asm("v_add_f32 %0, %1, %2" : "=v"(o) : "v"(r[0]), "v"(r[1])); return o;
}
template <int CTRL> DI float dpp_add(float v) { return v + u2f(__builtin_amdgcn_update_dpp(0u, f2u(v), CTRL, 0xf, 0xf, false)); }
DI float row_sum(float v) { v = dpp_add<0x128>(v); v = dpp_add<0x124>(v); v = dpp_add<0x122>(v); return dpp_add<0x121>(v); }
DI f32x2 pair(const f32x16& a, int j) { return f32x2{a[2 * j], a[2 * j + 1]}; }
DI unsigned phi2(f32x2 x, f32x2 a) {
  const f32x2 e2 = x * (a * 1.4426950408889634f);
  const f32x2 t = x * a + 1e-6f;
  const f32x2 e = {__builtin_amdgcn_fmed3f(__builtin_amdgcn_exp2f(e2.x), 0.f, 1.f), __builtin_amdgcn_fmed3f(__builtin_amdgcn_exp2f(e2.y), 0.f, 1.f)};
  const f32x2 mx = {fmaxf(t.x, 1e-6f), fmaxf(t.y, 1e-6f)};
  return pk2(mx + e);
}
constexpr unsigned ONES = 0x3f803f80u;
DI float fadd1(float a, float b) { float r; asm("v_add_f32 %0, %1, %2" : "=v"(r) : "v"(a), "v"(b)); return r; }
DI float fmul1(float a, float b) { float r; asm("v_mul_f32 %0, %1, %2" : "=v"(r) : "v"(a), "v"(b)); return r; }

typedef __amdgpu_buffer_rsrc_t rsrc_t;
DI v4u ld16(rsrc_t rs, int voff, int soff) { return __builtin_amdgcn_raw_buffer_load_b128(rs, voff, soff, 16); }
DI v2u ld8(rsrc_t rs, int voff, int soff) { return __builtin_amdgcn_raw_buffer_load_b64(rs, voff, soff, 16); }
DI void st16(rsrc_t rs, v4u v, int voff, int soff) {
  __builtin_amdgcn_raw_buffer_store_b128(v, rs, voff, soff, 16);
  asm volatile("s_nop 1" ::"v"(v) : "memory");
}
DI void st8(rsrc_t rs, v2u v, int voff, int soff) { __builtin_amdgcn_raw_buffer_store_b64(v, rs, voff, soff, 16); }

struct CW {
  unsigned s, pos, rdy;
  unsigned lane_tr;
  unsigned fa;
  unsigned tag, perm;
  int budget;
  int lane, w;
  char* ws;
};
DI bool spent(CW& c) { c.budget -= 1; return c.budget < 0; }
DI unsigned min4(v4u v) { return rfl(min(min(v.x, v.y), min(v.z, v.w))); }
DI void wait_count(CW& c, unsigned have, unsigned addr, unsigned need) {
  if (__builtin_expect(rfl(have) < need, 0)) {
    for (;;) { if (rfl(vld<unsigned>(addr)) >= need || spent(c)) break; }
  }
}
DI void peek(CW& c, unsigned ahead) { c.rdy = vld<unsigned>(L_RDY + ((c.pos + ahead) & (NSLOT - 1)) * 4); }
DI void wait_full(CW& c) { wait_count(c, c.rdy, L_RDY + c.pos * 4, (c.s / NSLOT + 1) * 4); }
DI void post(CW& c, int word, unsigned v) { vst<unsigned>(c.fa + (word - L_FCNT), v); }
DI void release(CW& c) { c.s += 1; c.pos = (c.pos + 1) & (NSLOT - 1); post(c, L_FCNT, min(c.s + NSLOT, c.perm)); }
DI void permit(CW& c, unsigned v) { c.perm = v; post(c, L_FCNT, min(c.s + NSLOT, v)); }
DI void set_flag(CW& c, int off) {
  const char* base = uni(c.ws + off);
  asm volatile("s_mov_b64 exec, 1\n\ts_nop 4\n\tglobal_atomic_swap %0, %1, %2\n\ts_mov_b64 exec, -1" ::"v"(0u), "v"(c.tag), "s"(base) : "memory");
}
DI void wait_flags(CW& c, int off, unsigned long long mask) {
  unsigned f, bud = rfl(c.budget);
  const unsigned voff = c.lane * 4;
  const char* base = uni(c.ws + off);
  asm volatile(
      "s_mov_b64 exec, %5\n\ts_nop 4\n\t"
      "1:\n\t"
      "global_atomic_or %0, %2, %6, %3 sc0\n\t"
      "s_waitcnt vmcnt(0)\n\t"
      "v_cmp_ne_u32 vcc, %4, %0\n\t"
      "s_cbranch_vccz 2f\n\t"
      "s_sub_i32 %1, %1, 8\n\t"
      "s_cmp_ge_i32 %1, 0\n\t"
      "s_cbranch_scc1 1b\n\t"
      "2:\n\t"
      "s_mov_b64 exec, -1\n\t"
      : "=&v"(f), "+s"(bud) : "v"(voff), "s"(base), "s"(c.tag), "s"(mask), "v"(0u) : "memory", "vcc", "scc");
  c.budget = rfl(bud);
}
DI unsigned poll_issue(CW& c, int off, unsigned long long mask) {
  unsigned f = c.tag;
  const unsigned voff = c.lane * 4;
  const char* base = uni(c.ws + off);
  asm volatile("s_mov_b64 exec, %3\n\tv_not_b32 %0, %0\n\ts_nop 4\n\tglobal_atomic_or %0, %1, %4, %2 sc0\n\ts_mov_b64 exec, -1"
               : "+v"(f) : "v"(voff), "s"(base), "s"(mask), "v"(0u) : "memory");
  return f;
}
template <int N> DI void poll_done(CW& c, unsigned f, int off, unsigned long long mask) {
  asm volatile("s_waitcnt vmcnt(%c1)" : "+v"(f) : "i"(N) : "memory");
  if (__builtin_expect(__builtin_amdgcn_ballot_w64(f != c.tag) != 0ull, 0)) wait_flags(c, off, mask);
}

template <int V> struct IC { static constexpr int value = V; DI constexpr operator int() const { return V; } };
template <int I, int E, class F> DI void static_for(F&& f) { if constexpr (I < E) { f(IC<I>{}); static_for<I + 1, E>(f); } }
struct Pipe { bf16x8 wa[PD + 1], wb[PD + 1]; unsigned ad, ad2, adn, need, pn, have, rel; };
#define SPIN(x) asm volatile("" : "+s"(x))
DI void slot_first(CW& c, Pipe& q) { peek(c, 0); wait_full(c); q.adn = c.lane_tr + c.pos * 16384u; }
template <int K, bool NX> DI void book(CW& c, Pipe& q) {
  if constexpr (NX && K == 0) { q.pn = (c.pos + 1) & (NSLOT - 1); c.rdy = vld<unsigned>(L_RDY + q.pn * 4); }
  if constexpr (NX && K == 1) { q.need = ((c.s + 1) / NSLOT + 1) * 4; SPIN(q.need); }
  if constexpr (NX && K == 2) { q.adn = c.lane_tr + q.pn * 16384u; PIN(q.adn); }
  if constexpr (NX && K == 3) { q.have = rfl(c.rdy); SPIN(q.have); }
  if constexpr (NX && K == 4) wait_count(c, q.have, L_RDY + q.pn * 4, q.need);
  if constexpr (K == 5) { c.s += 1; c.pos = (c.pos + 1) & (NSLOT - 1); q.rel = min(c.s + NSLOT, c.perm); PIN(q.rel); }
  if constexpr (K == 6) post(c, L_FCNT, q.rel);
}
template <bool SWAP, int I0, int I1, int NEND, bool MORE, class HOOK>
DI void pair_steps(CW& c, Pipe& q, v4u (&B)[32], f32x16& acc0, f32x16& acc1, HOOK hook) {
  static_for<I0, I1>([&](auto ic) __attribute__((always_inline)) {
    constexpr int i = ic.value, n = i + PD;
    constexpr bool F = n < NEND, NX = F && (MORE || n + 8 - n % 8 < NEND);
    if constexpr (n == 0) slot_first(c, q);
    if constexpr (i >= 0) acc0 = SWAP ? mfma(asb(B[i % 32]), q.wa[i % (PD + 1)], acc0) : mfma(q.wa[i % (PD + 1)], asb(B[i % 32]), acc0);
    SCHED();
    if constexpr (F && n % 8 == 0) q.ad = q.adn;
    if constexpr (F) q.wa[n % (PD + 1)] = ldw(q.ad + (n % 8) * 1024, 512);
    if constexpr (F && n % 8 >= 2) book<n % 8 - 2, NX>(c, q);
    hook(IC<(i < 0 ? i : i % 32)>{}, IC<0>{});
    SCHED();
    if constexpr (i >= 0) acc1 = SWAP ? mfma(asb(B[i % 32]), q.wb[i % (PD + 1)], acc1) : mfma(q.wb[i % (PD + 1)], asb(B[i % 32]), acc1);
    SCHED();
    if constexpr (F) q.wb[n % (PD + 1)] = ldw(q.ad + 8192 + (n % 8) * 1024, 512);
    if constexpr (F && n % 8 == 7) book<6, NX>(c, q);
    hook(IC<(i < 0 ? i : i % 32)>{}, IC<1>{});
    SCHED();
  });
}
template <bool SWAP, class HOOK>
DI void gemm_pair(CW& c, v4u (&B)[32], f32x16& acc0, f32x16& acc1, HOOK hook) {
  Pipe q;
  pair_steps<SWAP, -PD, 32, 32, false>(c, q, B, acc0, acc1, hook);
}
template <class HOOK>
DI void gemm_single(CW& c, v4u (&B)[32], f32x16& acc, HOOK hook) {
  bf16x8 wa[5];
  Pipe q;
  static_for<-4, 32>([&](auto ic) __attribute__((always_inline)) {
    constexpr int i = ic.value, n = i + 4;
    if constexpr (n == 0) slot_first(c, q);
    if constexpr (i >= 0) acc = mfma(wa[i % 5], asb(B[i]), acc);
    SCHED();
    if constexpr (n < 32) {
      if constexpr (n % 16 == 0) q.ad = q.adn;
      wa[n % 5] = ldw(q.ad + (n % 16) * 1024, 512);
      if constexpr (n % 16 == 6) book<0, (n < 16)>(c, q);
      if constexpr (n % 16 >= 10) book<n % 16 - 9, (n < 16)>(c, q);
    }
    hook(ic);
    SCHED();
  });
}
template <int PW> struct Wide { bf16x8 wa[PW + 1], wb[PW + 1]; Pipe q; unsigned dO, lt; };
template <int NK, int I0, int I1, int PW, class HOOK>
DI void wide_steps(CW& c, Wide<PW>& d, v4u (&B)[32], f32x16& accM, f32x16& accO, int kh, HOOK hook) {
  Pipe& q = d.q;
  const unsigned keep = c.lane_tr;
  if constexpr (I0 == -PW) { d.lt = c.lane_tr + kh * (8192u + 512u); d.dO = kh ? -512u : 512u; }
  c.lane_tr = d.lt;
  static_for<I0, I1>([&](auto ic) __attribute__((always_inline)) {
    constexpr int i = ic.value, n = i + PW;
    constexpr bool F = n < NK, NX = F && n + 4 - n % 4 < NK;
    if constexpr (n == 0) slot_first(c, q);
    if constexpr (i >= 0) accO = mfma(d.wb[i % (PW + 1)], asb(B[i % 16]), accO);
    SCHED();
    if constexpr (F && n % 4 == 0) { q.ad = q.adn; if constexpr (n + 3 == NK) q.ad -= kh * 2048u; q.ad2 = q.ad + d.dO; }
    if constexpr (F) d.wb[n % (PW + 1)] = ldw(q.ad2 + (n % 4) * 2048, 1024);
    if constexpr (F && n % 4 == 1) { book<1, NX>(c, q); }
    if constexpr (F && n % 4 == 2) { book<2, NX>(c, q); book<3, NX>(c, q); }
    if constexpr (F && (n % 4 == 3 || n == NK - 1)) { book<5, NX>(c, q); }
    hook(ic, IC<0>{});
    SCHED();
    if constexpr (i >= 0) accM = mfma(d.wa[i % (PW + 1)], asb(B[i % 16]), accM);
    SCHED();
    if constexpr (F) d.wa[n % (PW + 1)] = ldw(q.ad + (n % 4) * 2048, 1024);
    if constexpr (F && n % 4 == 0) book<0, NX>(c, q);
    if constexpr (F && n % 4 == 2) book<4, NX>(c, q);
    if constexpr (F && (n % 4 == 3 || n == NK - 1)) book<6, NX>(c, q);
    hook(ic, IC<1>{});
    SCHED();
  });
  c.lane_tr = keep;
}
struct NoHook { template <class A, class B> DI void operator()(A, B) const {} };

DI void ln_factors(float sum, float sq, float& a, unsigned& mf, bool up) {
  float mean = sum * (1.0f / 512.0f);
  PIN(mean);
  const float var = fmaxf(sq * (1.0f / 512.0f) - mean * mean, 0.f);
  a = __builtin_amdgcn_rsqf(var + 1e-5f);
  const unsigned hi = pk2(-mean, -mean);
  const float lo = -mean - blo(hi);
  mf = up ? pk2(lo, lo) : hi;
}
struct Stats { v4u sv[4]; f32x4 t; f32x2 u[4]; float mean, var; };
constexpr int K0 = 34;
DI void stats_hook(CW& c, Stats& t, int k, rsrc_t rs, int soff, float& a, unsigned& mf) {
  if (k == 3 || k == 7 || k == 11 || k == 15) t.sv[k >> 2] = ld16(rs, c.lane * 16 + (k >> 2) * 1024, soff);
  const unsigned sc = L_SCR + (c.w ^ 2) * 4096;
  if (k >= K0 && k < K0 + 4) { const int e = k - K0; t.t[e] = fadd1(fadd1(u2f(t.sv[0][e]), u2f(t.sv[1][e])), fadd1(u2f(t.sv[2][e]), u2f(t.sv[3][e]))); PIN(t.t[e]); }
  if (k == K0 + 4) vst<f32x4>(sc + c.lane * 16, t.t);
  if (k == K0 + 5) { t.u[0] = vld<f32x2>(sc + (c.lane & 31) * 8); t.u[1] = vld<f32x2>(sc + 256 + (c.lane & 31) * 8); }
  if (k == K0 + 6) { t.u[2] = vld<f32x2>(sc + 512 + (c.lane & 31) * 8); t.u[3] = vld<f32x2>(sc + 768 + (c.lane & 31) * 8); }
  if (k == K0 + 10) { t.mean = fadd1(fadd1(t.u[0].x, t.u[1].x), fadd1(t.u[2].x, t.u[3].x)); PIN(t.mean); }
  if (k == K0 + 11) { t.var = fadd1(fadd1(t.u[0].y, t.u[1].y), fadd1(t.u[2].y, t.u[3].y)); PIN(t.var); }
  if (k == K0 + 12) { t.mean *= -1.0f / 512.0f; PIN(t.mean); t.var = t.var * (1.0f / 512.0f) + 1e-5f; PIN(t.var); }
  if (k == K0 + 13) { t.var = fmaxf(t.var - t.mean * t.mean, 1e-5f); PIN(t.var); a = __builtin_amdgcn_rsqf(t.var); PIN(a); }
  if (k == K0 + 14) { mf = pk2(t.mean, t.mean); t.mean -= blo(mf); PIN(mf); PIN(t.mean); }
  if (k == K0 + 15) { mf = c.lane >= 32 ? pk2(t.mean, t.mean) : mf; PIN(mf); }
}
DI void finish_wide(CW& c, const f32x16& accM, const f32x16& accO, unsigned (&R)[8], unsigned seq) {
  const unsigned mine = L_SCR + c.w * 4096 + c.lane * 16, other = L_SCR + (c.w ^ 2) * 4096 + c.lane * 16;
#pragma unroll
  for (int g = 0; g < 4; ++g) vst<f32x4>(mine + g * 1024, f32x4{accO[4 * g], accO[4 * g + 1], accO[4 * g + 2], accO[4 * g + 3]});
  post(c, L_F3, seq);
  f32x2 y[8];
#pragma unroll
  for (int j = 0; j < 8; ++j) { y[j] = pair(accM, j) + f32x2{blo(R[j]), bhi(R[j])}; PIN(y[j]); }
  f32x4 v[4];
  for (;;) {
    const unsigned fl = vld<unsigned>(L_F3 + (c.w ^ 2) * 4);
#pragma unroll
    for (int g = 0; g < 4; ++g) v[g] = vld<f32x4>(other + g * 1024);
    if (rfl(fl) >= seq || spent(c)) break;
  }
#pragma unroll
  for (int g = 0; g < 4; ++g) {
    R[2 * g] = pk2(y[2 * g] + f32x2{v[g].x, v[g].y});
    R[2 * g + 1] = pk2(y[2 * g + 1] + f32x2{v[g].z, v[g].w});
  }
}
DI void publish_x(CW& c, rsrc_t rs, const unsigned (&R)[8], int xoff, int stoff, int floff) {
  st16(rs, v4u{R[0], R[1], R[2], R[3]}, c.lane * 16, xoff);
  st16(rs, v4u{R[4], R[5], R[6], R[7]}, c.lane * 16 + 1024, xoff);
  float s0 = 0.f, q0 = 0.f, s1 = 0.f, q1 = 0.f;
#pragma unroll
  for (int i = 0; i < 4; ++i) { s0 = dot2(R[i], ONES, s0); q0 = dot2(R[i], R[i], q0); s1 = dot2(R[4 + i], ONES, s1); q1 = dot2(R[4 + i], R[4 + i], q1); }
  const float s = sum_halves(s0 + s1), q = sum_halves(q0 + q1);
  st8(rs, v2u{f2u(s), f2u(q)}, (c.lane & 31) * 8, stoff);
  DRAIN();
  set_flag(c, floff);
}

DI void dma4(const char* base, unsigned v0, unsigned v1, unsigned v2, unsigned v3, unsigned dst) {
  asm volatile("s_mov_b32 m0, %5\n\ts_nop 3\n\t"
               "global_load_lds_dwordx4 %0, %4\n\tglobal_load_lds_dwordx4 %1, %4 offset:1024\n\t"
               "global_load_lds_dwordx4 %2, %4 offset:2048\n\tglobal_load_lds_dwordx4 %3, %4 offset:3072"
               ::"v"(v0), "v"(v1), "v"(v2), "v"(v3), "s"(uni(base)), "s"(rfl(dst)) : "memory");
}
#define PACE "s_sleep 1\n\t"
DI void dma4p(const char* base, unsigned v0, unsigned v1, unsigned v2, unsigned v3, unsigned dst) {
  asm volatile("s_mov_b32 m0, %5\n\ts_nop 3\n\t"
               "global_load_lds_dwordx4 %0, %4\n\t" PACE "global_load_lds_dwordx4 %1, %4 offset:1024\n\t" PACE
               "global_load_lds_dwordx4 %2, %4 offset:2048\n\t" PACE "global_load_lds_dwordx4 %3, %4 offset:3072\n\t" PACE
               ::"v"(v0), "v"(v1), "v"(v2), "v"(v3), "s"(uni(base)), "s"(rfl(dst)) : "memory");
}
DI void dma4_sc1(const char* base, unsigned v, unsigned dst) {
  asm volatile("s_mov_b32 m0, %2\n\ts_nop 3\n\t"
               "global_load_lds_dwordx4 %0, %1 sc1\n\t" PACE "global_load_lds_dwordx4 %0, %1 offset:1024 sc1\n\t" PACE
               "global_load_lds_dwordx4 %0, %1 offset:2048 sc1\n\t" PACE "global_load_lds_dwordx4 %0, %1 offset:3072 sc1\n\t" PACE
               ::"v"(v), "s"(uni(base)), "s"(rfl(dst)) : "memory");
}
DI void dma4_sc1f(const char* base, unsigned v, unsigned dst) {
  asm volatile("s_mov_b32 m0, %2\n\ts_nop 3\n\t"
               "global_load_lds_dwordx4 %0, %1 sc1\n\tglobal_load_lds_dwordx4 %0, %1 offset:1024 sc1\n\t"
               "global_load_lds_dwordx4 %0, %1 offset:2048 sc1\n\tglobal_load_lds_dwordx4 %0, %1 offset:3072 sc1"
               ::"v"(v), "s"(uni(base)), "s"(rfl(dst)) : "memory");
}
template <int N> DI void wait_vm() { asm volatile("s_waitcnt vmcnt(%c0)" ::"i"(N) : "memory"); }
DI unsigned tr_lane(int lane) { return (4 * (lane >> 5) + ((lane & 15) >> 2)) * 64 + ((lane >> 4) & 1) * 32 + (lane & 3) * 8; }
DI int fresh_lane() { int l; asm volatile("v_mbcnt_lo_u32_b32 %0, -1, 0\n\tv_mbcnt_hi_u32_b32 %0, -1, %0" : "=v"(l)); return l; }
#define LSTAMP(v, s) do {} while (0)
DI void lds_add(unsigned pa, unsigned off, unsigned n) { CFENCE(); (void)__hip_atomic_fetch_add(lp<unsigned>(pa + off), n, __ATOMIC_RELAXED, __HIP_MEMORY_SCOPE_WORKGROUP); CFENCE(); }
DI void loader(const Params& P, int p, int m, int lw) {
  const int lane = fresh_lane();
  const unsigned smem_base = (unsigned)(uintptr_t)(LAS char*)smem;
  const unsigned nr = m < 11 ? 3 : 2, NS = 21 + 4 * nr, NT = 2 * NS;
  const unsigned v_lin = lane * 16, v_wide = ((lane & 31) >> 2) * 1024 + (lane >> 5) * 64 + (lane & 3) * 16, v_raw = (lane >> 2) * 3072 + (lane & 3) * 16;
  const unsigned pa = lane == 0 ? L_RDY : L_DUMMY + lane * 4;
  __builtin_amdgcn_s_setprio(LPRIO);
  unsigned limit = 0;
  int budget = BUDGET;
  bool pend = false;
  for (unsigned s = 4 + lw; s < NT; s += 2) {
    const bool l2 = s >= NS;
    const unsigned i = l2 ? s - NS : s, g = i - 10u, d = g - 4u * nr, r4 = 4u * ROT(m);
    const char* src[4];
    unsigned kind;
    if (i < 6u) {
      kind = l2 ? 1u : 0u;
#pragma unroll
      for (unsigned q = 0; q < 4; ++q) {
        const unsigned blk = i < 4u ? q >> 1 : 2u, ks0 = ((i < 4u ? 8u * i + (q & 1) * 4u : 16u * (i - 4u) + q * 4u) + r4) & 31u;
        src[q] = l2 ? P.ws + WS_RQ + ((m * 3 + blk) * 32u + ks0) * 1024u : P.wqkv + m * 64 + ks0 * (16u * 3072u);
      }
    } else if (i < 10u) {
      kind = 2u;
#pragma unroll
      for (unsigned q = 0; q < 4; ++q) src[q] = P.wo + ((i - 6u) * 128u + q * 32u) * 1024u + (m & 7) * 128;
    } else if (g < 4u * nr) {
      kind = 1u;
#pragma unroll
      for (unsigned q = 0; q < 4; ++q) src[q] = P.ws + ((q >> 1) ? WS_RU : WS_RG) + ((16u * (g >> 2) + m) * 32u + ((8u * (g & 3u) + (q & 1) * 4u + r4) & 31u)) * 1024u;
    } else {
      kind = 2u;
#pragma unroll
      for (unsigned q = 0; q < 4; ++q) src[q] = P.wd + min((d < 8u ? (d & 4u) | ((d + ROT(m)) & 3u) : d) * 128u + q * 32u, 1344u) * 1024u + (m & 7) * 128;
    }
    if (pend && i < THINB) { wait_vm<0>(); lds_add(pa, ((s - 2) & (NSLOT - 1)) * 4, 4); LSTAMP(tlan, s - 2); pend = false; }
    while (s >= limit) {
      limit = min4(vld<v4u>(L_FCNT));
      if (s < limit) break;
      if (pend) { wait_vm<0>(); lds_add(pa, ((s - 2) & (NSLOT - 1)) * 4, 4); LSTAMP(tlan, s - 2); pend = false; continue; }
      __builtin_amdgcn_s_sleep(LSLEEP);
      if (--budget < 0) break;
    }
    LSTAMP(tiss, s);
    const unsigned dst = smem_base + L_RING + (s & (NSLOT - 1)) * 16384u;
    const unsigned v0 = kind == 2u ? v_wide : v_raw, st = kind == 2u ? 7168u : 48128u;
    if (i >= PACEB && i < 18u && s >= 6u) {
      if (kind == 1u) { dma4_sc1(src[0], v_lin, dst); dma4_sc1(src[1], v_lin, dst + 4096); dma4_sc1(src[2], v_lin, dst + 8192); dma4_sc1(src[3], v_lin, dst + 12288); }
      else {
#pragma unroll
        for (int q = 0; q < 4; ++q) dma4p(src[q], v0, v0 + st, v0 + 2 * st, v0 + 3 * st, dst + q * 4096);
      }
    } else {
      if (kind == 1u) { dma4_sc1f(src[0], v_lin, dst); dma4_sc1f(src[1], v_lin, dst + 4096); dma4_sc1f(src[2], v_lin, dst + 8192); dma4_sc1f(src[3], v_lin, dst + 12288); }
      else {
#pragma unroll
        for (int q = 0; q < 4; ++q) dma4(src[q], v0, v0 + st, v0 + 2 * st, v0 + 3 * st, dst + q * 4096);
      }
    }
    if (pend) { wait_vm<16>(); lds_add(pa, ((s - 2) & (NSLOT - 1)) * 4, 4); LSTAMP(tlan, s - 2); }
    LSTAMP(tis2, s);
    pend = true;
    if (s < 6u) {
      wait_vm<0>(); lds_add(pa, s * 4, 4); LSTAMP(tlan, s); pend = false;
      __builtin_amdgcn_s_setprio(3);
      const unsigned a = tr_lane(lane) + s * 16384u;
      float c0 = 0.f, c1 = 0.f;
#pragma unroll
      for (int n = 0; n < 16; ++n) {
        const v4u f = __builtin_bit_cast(v4u, ldw(a + n * 1024, 512));
        c0 = dot2(f.x, ONES, c0); c1 = dot2(f.y, ONES, c1); c0 = dot2(f.z, ONES, c0); c1 = dot2(f.w, ONES, c1);
      }
      const float t = sum_halves(c0 + c1);
      vst<unsigned>(L_CSF + 2 * 512 + (lane & 31) * 16 + lw * 4, pk2(t, t - blo(pk2(t, t))));
      lds_add(pa, L_CSC - L_RDY + 4, 1);
      __builtin_amdgcn_s_setprio(LPRIO);
    }
    if (s + 2 >= NT) { wait_vm<0>(); lds_add(pa, (s & (NSLOT - 1)) * 4, 4); }
  }
}
DI void server(const Params& P, int p, int m, int sw) {
  const int lane = fresh_lane();
  unsigned epoch;
  asm volatile("s_load_dword %0, %1, 0x0" : "=s"(epoch) : "s"(P.ws) : "memory");
  const unsigned pa = lane == 0 ? L_RDY : L_DUMMY + lane * 4;
  const rsrc_t rs = __builtin_amdgcn_make_buffer_rsrc((void*)P.ws, 0, WS_END, 0x00020000);
  CW c;
  c.lane = lane; c.ws = P.ws; c.budget = BUDGET;
  asm volatile("s_waitcnt lgkmcnt(0)" : "+s"(epoch) :: "memory");
  c.tag = epoch + 1u;
  const int co = WS_CSP + (m * 2 + sw) * 16 * 768;
  STAMP(7);
  DRAIN();
  STAMP(10);
  lds_add(pa, L_RPD - L_RDY, 1);
  {
    {
      const unsigned b = blockIdx.x;
      const char* tb = sw ? P.wo : P.wd;
      const unsigned to = sw ? (b * 16u + (lane & 15)) * 128u : (b * 43u + min(lane, 42)) * 128u;
      asm volatile("s_mov_b32 m0, %2\n\ts_nop 3\n\tglobal_load_lds_dword %0, %1" :: "v"(to), "s"(uni(tb)), "s"((unsigned)(uintptr_t)(LAS char*)smem + L_DUMMY) : "memory");
    }
    while (rfl(vld<unsigned>(L_RPD)) < 2u && !spent(c)) __builtin_amdgcn_s_sleep(1);
  }
  for (unsigned layer = 0; layer < 2; ++layer) {
    __builtin_amdgcn_s_setprio(3);
    f32x4 tk[4], tz[4];
    for (;;) {
      const v4u fl = vld<v4u>(L_F1);
      if (min4(fl) > layer || spent(c)) break;
      __builtin_amdgcn_s_sleep(1);
    }
    STAMP(11 + 4 * layer);
#pragma unroll
    for (int u = 0; u < 4; ++u) { tk[u] = vld<f32x4>(L_KVS + u * 2048 + sw * 1024 + lane * 16); tz[u] = vld<f32x4>(L_ZS + u * 128 + (lane & 3) * 32 + sw * 16); }
    const int go = WS_KVP + ((layer * 16 + m) * 2 + sw) * 16 * KVW, fo = WS_FKV + ((layer * 16 + m) * 2 + sw) * 256;
    v4u sk, sz;
#pragma unroll
    for (int e = 0; e < 4; ++e) { sk[e] = f2u(fadd1(fadd1(tk[0][e], tk[1][e]), fadd1(tk[2][e], tk[3][e]))); sz[e] = f2u(fadd1(fadd1(tz[0][e], tz[1][e]), fadd1(tz[2][e], tz[3][e]))); }
    st16(rs, sk, lane * 16, go + p * KVW);
    st16(rs, sz, 1024 + (lane & 7) * 16, go + p * KVW);
    DRAIN();
    set_flag(c, fo + p * 4);
    STAMP(12 + 4 * layer);
    wait_flags(c, fo, 0xffffull);
    STAMP(13 + 4 * layer);
    const v4u zl = ld16(rs, 1024 + (lane & 15) * KVW + (lane >> 4) * 16, go);
    v4u g[16];
#pragma unroll
    for (int i = 0; i < 16; ++i) g[i] = ld16(rs, lane * 16, go + i * KVW);
    SCHED();
    const float z0 = row_sum(u2f(zl.x)), z1 = row_sum(u2f(zl.y)), z2 = row_sum(u2f(zl.z)), z3 = row_sum(u2f(zl.w));
    if ((lane & 15) == 0) vst<v2u>(L_ZR + (lane >> 4) * 16 + sw * 8, v2u{pk2(z0, z1), pk2(z2, z3)});
    f32x4 r = __builtin_bit_cast(f32x4, g[0]);
#pragma unroll
    for (int i = 1; i < 16; ++i) r += __builtin_bit_cast(f32x4, g[i]);
    vst<v2u>(L_KVR + lane * 16 + sw * 8, v2u{pk2(r.x, r.y), pk2(r.z, r.w)});
    lds_add(pa, L_F2 - L_RDY, 2);
    STAMP(14 + 4 * layer);
    __builtin_amdgcn_s_setprio(0);
    if (layer == 0) {
      v4u t[16];
#pragma unroll
      for (int i = 0; i < 16; ++i) t[i] = ld16(rs, min(lane, 47) * 16, co + i * 768);
      SCHED();
      f32x4 v = __builtin_bit_cast(f32x4, t[0]);
#pragma unroll
      for (int i = 1; i < 16; ++i) v += __builtin_bit_cast(f32x4, t[i]);
      if (lane < 48) {
#pragma unroll
        for (int e = 0; e < 4; ++e) vst<unsigned>(L_CSF + 2048 + (lane * 4 + e) * 16 + sw * 4, pk2(v[e], v[e] - blo(pk2(v[e], v[e]))));
      }
      lds_add(pa, L_CSC - L_RDY + 8, 2); lds_add(pa, L_CSC - L_RDY + 12, 2); lds_add(pa, L_CSC - L_RDY + 16, 2);
    }
  }
}
template <int AT, bool TWO> DI void cs_hook(CW& c, int i, unsigned k, v4u& a, v4u& b) {
  if (i == AT) a.x = vld<unsigned>(L_CSC + k * 4);
  if (i == AT + 3) {
    wait_count(c, a.x, L_CSC + k * 4, TWO ? 4u : 2u);
    a = vld<v4u>(L_CSF + k * 1024 + (c.lane & 31) * 16);
    if (TWO) b = vld<v4u>(L_CSF + k * 1024 + 512 + (c.lane & 31) * 16);
  }
}

__global__ void __launch_bounds__(512, 2) fused_fwd(Params P) {
  const int tid = threadIdx.x; int lane = tid & 63; const int wv = __builtin_amdgcn_readfirstlane(tid >> 6);
  const int b = blockIdx.x;
  {
    const unsigned co = CODE_BYTES - 256u - min(((b >> 3) * 8 + wv) * 256u, CODE_BYTES - 256u) + lane * 4;
    unsigned long long pc;
    asm volatile("s_mov_b32 m0, %2\n\ts_getpc_b64 %0\n\tglobal_load_lds_dword %1, %0" : "=&s"(pc) : "v"(co), "s"((unsigned)(uintptr_t)(LAS char*)smem + L_DUMMY) : "memory");
  }
  int p, m;
  if (MAPMODE == 0) { p = (b & 7) * 2 + (b >> 7); m = (b >> 3) & 15; }
  else if (MAPMODE == 1) { const int x = b & 7, i = b >> 3; p = (x >> 1) * 4 + (i >> 3); m = (x & 1) * 8 + (i & 7); }
  else { const int x = b & 7, i = b >> 3; p = i >> 1; m = x * 2 + (i & 1); }
  if (tid < (L_END - L_RDY) / 4) vst<unsigned>(L_RDY + tid * 4, 0u);
  if (tid < 320) vst<v4u>(L_CSF + tid * 16, v4u{0u, 0u, 0u, 0u});
  asm volatile("s_waitcnt lgkmcnt(0)\n\ts_barrier" ::: "memory");
  if (wv >= 4) __builtin_amdgcn_s_setprio(3);
  STAMP(21);
  const unsigned smem_base = (unsigned)(uintptr_t)(LAS char*)smem;
  int w = wv & 3;
  unsigned xv[4];
  const char* xbase = (wv < 4 ? P.x : P.wqkv) + (unsigned)sopaque(wv < 4 ? (4 * p + w) * 32768 : ((w >> 1) + 1) * 1024 + m * 64);
  auto xbuf = [&](int q) -> unsigned { const int j = q % 5; return (j == 0 ? L_SCR : j == 1 ? 7 * 16384 : j == 2 ? 6 * 16384 : j == 3 ? 4 * 16384 : 5 * 16384) + w * 4096; };
  {
#pragma unroll
    for (int k = 0; k < 4; ++k) {
      const unsigned a = (8 * k + (lane >> 3)) * 1024 + (((lane & 7) ^ ((4 * k + (lane >> 4)) & 7)) * 16) - k * 1024;
      const unsigned c = (lane >> 2) * 3072 + (lane & 3) * 16 + k * 48128;
      xv[k] = wv < 4 ? a : c;
    }
    const unsigned cw = wv < 4, stride = sopaque(cw ? 128 : 64 * 3072), r0 = ROT(m) + (cw ? 0 : (w & 1));
    dma4(xbase + (r0 & 7) * stride, xv[0], xv[1], xv[2], xv[3], smem_base + (cw ? xbuf(0) : w * 4096));
    if (!cw) {
      const unsigned pa = smem_base + (lane == 0 ? L_RDY : L_DUMMY + lane * 4), a = tr_lane(lane) + w * 4096u;
      float c0 = 0.f, c1 = 0.f;
      auto cs = [&](int j) __attribute__((always_inline)) {
#pragma unroll
        for (int n = 0; n < 4; ++n) {
          const v4u f = __builtin_bit_cast(v4u, ldw(a + j * 16384 + n * 1024, 512));
          c0 = dot2(f.x, ONES, c0); c1 = dot2(f.y, ONES, c1); c0 = dot2(f.z, ONES, c0); c1 = dot2(f.w, ONES, c1);
        }
        PIN(c0); PIN(c1);
      };
      STAMP(2);
      asm volatile("s_waitcnt vmcnt(0)\n\tds_add_u32 %0, %1" ::"v"(pa), "v"(1u) : "memory");
      STAMP(3);
      dma4(xbase + ((2 + r0) & 7) * stride, xv[0], xv[1], xv[2], xv[3], smem_base + 1 * 16384 + w * 4096);
      cs(0);
      asm volatile("s_waitcnt vmcnt(0)\n\tds_add_u32 %0, %1 offset:4" ::"v"(pa), "v"(1u) : "memory");
      STAMP(4);
      dma4(xbase + ((4 + r0) & 7) * stride, xv[0], xv[1], xv[2], xv[3], smem_base + 2 * 16384 + w * 4096);
      cs(1);
      asm volatile("s_waitcnt vmcnt(0)\n\tds_add_u32 %0, %1 offset:8" ::"v"(pa), "v"(1u) : "memory");
      STAMP(5);
      dma4(xbase + ((6 + r0) & 7) * stride, xv[0], xv[1], xv[2], xv[3], smem_base + 3 * 16384 + w * 4096);
      cs(2);
      asm volatile("s_waitcnt vmcnt(0)\n\tds_add_u32 %0, %1 offset:12" ::"v"(pa), "v"(1u) : "memory");
      STAMP(6);
      const int ks = 2 * p + (w & 1), r2 = m < 11 ? 2 : 1;
      v4u rp[9];
      if (w >= 2) {
        __builtin_amdgcn_s_setprio(RPP);
        const GAS char* sq = (const GAS char*)P.wqkv + m * 64 + ks * (16 * 3072) + xv[0];
        const unsigned vg = m * 64 + ks * (16 * 2752) + (lane >> 2) * 2752 + (lane & 3) * 16;
        const GAS char* sg = (const GAS char*)P.wg + vg; const GAS char* su = (const GAS char*)P.wu + vg;
        rp[0] = *(const GAS v4u*)(sg); rp[1] = *(const GAS v4u*)(su); rp[2] = *(const GAS v4u*)(sg + 1024); rp[3] = *(const GAS v4u*)(su + 1024);
        rp[4] = *(const GAS v4u*)(sg + r2 * 1024); rp[5] = *(const GAS v4u*)(su + r2 * 1024);
        rp[6] = *(const GAS v4u*)(sq + 1024); rp[7] = *(const GAS v4u*)(sq + 2048); rp[8] = *(const GAS v4u*)(sq);
        SCHED();
        __builtin_amdgcn_s_setprio(3);
      }
      cs(3);
      const float t = sum_halves(c0 + c1);
      vst<unsigned>(L_CSF + (w >> 1) * 512 + (lane & 31) * 16 + (w & 1) * 4, pk2(t, t - blo(pk2(t, t))));
      asm volatile("ds_add_u32 %0, %1 offset:%c2" ::"v"(pa), "v"(1u), "i"(L_CSC - L_RDY) : "memory");
      if (sopaque(w) >= 2) {
        const rsrc_t rs = __builtin_amdgcn_make_buffer_rsrc((void*)P.ws, 0, WS_END, 0x00020000);
        const int dg = (m * 32 + ks) * 1024, dq = WS_RQ + (m * 96 + ks) * 1024;
        const unsigned SCX = L_CSF + 3072 + (w & 1) * 1024;
        v4u f[6];
#pragma unroll
        for (int u = 0; u < 6; ++u) {
          vst<v4u>(SCX + lane * 16, rp[u]);
          CFENCE();
          f[u] = __builtin_bit_cast(v4u, ldw(SCX + tr_lane(lane), 512));
          CFENCE();
        }
        vst<v4u>(SCX + lane * 16, v4u{0u, 0u, 0u, 0u});
        st16(rs, rp[0], lane * 16, WS_RG + dg); st16(rs, rp[1], lane * 16, WS_RU + dg);
        st16(rs, rp[2], lane * 16, WS_RG + dg + 16 * 32768); st16(rs, rp[3], lane * 16, WS_RU + dg + 16 * 32768);
        st16(rs, rp[4], lane * 16, WS_RG + dg + r2 * (16 * 32768)); st16(rs, rp[5], lane * 16, WS_RU + dg + r2 * (16 * 32768));
        st16(rs, rp[6], lane * 16, dq); st16(rs, rp[7], lane * 16, dq + 32768); st16(rs, rp[8], lane * 16, dq + 65536);
        STAMP(8);
        float cs6[6];
#pragma unroll
        for (int u = 0; u < 6; ++u) cs6[u] = dot2(f[u].x, ONES, dot2(f[u].y, ONES, dot2(f[u].z, ONES, dot2(f[u].w, ONES, 0.f))));
#pragma unroll
        for (int u = 0; u < 3; ++u) {
          const auto r = __builtin_amdgcn_permlane32_swap(f2u(cs6[2 * u]), f2u(cs6[2 * u + 1]), false, false);
          __builtin_amdgcn_raw_buffer_store_b32(f2u(u2f(r[0]) + u2f(r[1])), rs, lane * 4 + u * 256, WS_CSP + ((m * 2 + (w & 1)) * 16 + p) * 768, 16);
        }
        STAMP(9);
      }
    }
  }
  STAMPV(0, t00);
  STAMP(61);
  if (sopaque(wv) >= 6) {
    server(P, p, m, wv - 6);
    STAMP(1); STAMP_END(); return; }
  if (sopaque(wv) >= 4) { loader(P, p, m, wv - 4); STAMP(1); STAMP_END(); return; }
  __builtin_amdgcn_s_setprio(2);

  unsigned epoch;
  asm volatile("s_load_dword %0, %1, 0x0" : "=s"(epoch) : "s"(P.ws) : "memory");
  v4u B[32];
  auto xdma = [&](int q) __attribute__((always_inline)) { dma4(xbase + ((q + ROT(m)) & 7) * 128, xv[0], xv[1], xv[2], xv[3], smem_base + xbuf(q)); };
  xdma(1);
  const rsrc_t rs = __builtin_amdgcn_make_buffer_rsrc((void*)P.ws, 0, WS_END, 0x00020000);
  asm volatile("s_waitcnt lgkmcnt(0)" : "+s"(epoch) :: "memory");
  STAMP(19);
  CW c;
  c.s = 0; c.pos = 0; c.rdy = 0; c.perm = 4; c.lane_tr = tr_lane(lane); c.fa = lane == 0 ? L_FCNT + w * 4 : L_DUMMY + lane * 4;
  c.budget = BUDGET; c.lane = lane; c.w = w; c.ws = P.ws; c.tag = epoch + 1u;
  int h, t32, tbq, tbw, kh, cb;
#define FRESH() do { lane = opaque(lane); h = lane >> 5; t32 = lane & 31; c.lane = lane; p = sopaque(p); m = sopaque(m); w = sopaque(w); c.w = w; \
    tbq = layer * 64 + 4 * p + w; kh = w >> 1; tbw = layer * 64 + 4 * p + 2 * (m >> 3) + (w & 1); cb = 2 * (m & 7) + kh; } while (0)
  unsigned R[8];

  int layer = 0;
    f32x16 aK = {}, aV = {};
    float la; unsigned mf;
    f32x4 ar[4];
    v4u ck, cv;
    FRESH();
    auto rows_ab = [&]() __attribute__((always_inline)) {
      vst<float>(L_SA + w * 128 + t32 * 4, la);
#pragma unroll
      for (int g = 0; g < 4; ++g) ar[g] = vld<f32x4>(L_SA + w * 128 + (8 * g + 4 * h) * 4);
    };
    {
      float s1 = 0.f, s2 = 0.f, s3 = 0.f, s4 = 0.f;
      const unsigned xl = t32 * 128 + 8 * h, xf = (t32 >> 1) & 7;
      auto hook = [&](int i, int hf) __attribute__((always_inline)) {
        const int n = i + 2;
        if (n >= 0 && n < 32 && hf == 0) {
          const int q = n / 4, j = n % 4;
          if (j == 0) {
            if (q == 0) { STAMPB(39); wait_vm<4 * (XUP - 1)>(); STAMPB(40); }
            if (q == 1 || q == 2 || q == 3) wait_vm<12>();
            if (q == 4) wait_vm<12>();
            if (q == 5) wait_vm<8>();
            if (q == 6) wait_vm<4>();
            if (q == 7) wait_vm<0>();
          }
          const v2u lo = vld<v2u>(xbuf(q) + xl + (((2 * j) ^ xf) * 16)), hi = vld<v2u>(xbuf(q) + xl + (((2 * j + 1) ^ xf) * 16));
          B[n] = v4u{lo.x, lo.y, hi.x, hi.y};
        }
        if (hf == 1) {
          if (i == -1) xdma(2);
          if (i == 0) xdma(3);
          if (i == 1) xdma(4);
          if (i == 3 || i == 7 || i == 11) xdma(5 + i / 4);
          if (i >= 0 && i % 4 == 3) {
#pragma unroll
            for (int u = i - 3; u <= i; ++u) {
              const v4u f = B[u];
              s1 = dot2(f.x, ONES, s1); s2 = dot2(f.x, f.x, s2); s3 = dot2(f.y, ONES, s3); s4 = dot2(f.y, f.y, s4);
              s1 = dot2(f.z, ONES, s1); s2 = dot2(f.z, f.z, s2); s3 = dot2(f.w, ONES, s3); s4 = dot2(f.w, f.w, s4);
            }
            PIN(s1); PIN(s2); PIN(s3); PIN(s4);
          }
          cs_hook<26, true>(c, i, 0, ck, cv);
          if (n == 15 || n == 19) permit(c, n / 4 + 2);
          if (i == 30) permit(c, 10);
        }
      };
      gemm_pair<true>(c, B, aK, aV, hook);
      ln_factors(sum_halves(s1 + s3), sum_halves(s2 + s4), la, mf, h != 0);
      rows_ab();
    }
  for (; layer < 2; ++layer) {
    const bf16x8 mfr = asb(v4u{mf, mf, mf, mf});
    aK = mfma(mfr, asb(ck), aK);
    aV = mfma(mfr, asb(cv), aV);
    STAMPB(2 + 20 * layer);
    FRESH();
    {
      unsigned pkk[8], pkv[8];
#pragma unroll
      for (int j = 0; j < 8; ++j) {
        const f32x2 a = (j & 1) ? f32x2{ar[j >> 1].z, ar[j >> 1].w} : f32x2{ar[j >> 1].x, ar[j >> 1].y};
        pkk[j] = phi2(pair(aK, j), a);
        pkv[j] = pk2(pair(aV, j) * a);
      }
      const bf16x8 k0 = asb(v4u{pkk[0], pkk[1], pkk[2], pkk[3]}), k1 = asb(v4u{pkk[4], pkk[5], pkk[6], pkk[7]});
      const bf16x8 v0 = asb(v4u{pkv[0], pkv[1], pkv[2], pkv[3]}), v1 = asb(v4u{pkv[4], pkv[5], pkv[6], pkv[7]});
      const bf16x8 one = asb(v4u{ONES, ONES, ONES, ONES});
      f32x16 kv = {}, zz = {};
      kv = mfma(k0, v0, kv); zz = mfma(k0, one, zz); kv = mfma(k1, v1, kv); zz = mfma(k1, one, zz);
      const bool hd = (lane & 16) != 0;
      f32x4 q0, q1, z0, z1;
#pragma unroll
      for (int j = 0; j < 4; ++j) { q0[j] = sel(hd, kv[8 + j], kv[j]); q1[j] = sel(hd, kv[12 + j], kv[4 + j]); }
      vst<f32x4>(L_KVS + w * 2048 + lane * 16, q0);
      vst<f32x4>(L_KVS + w * 2048 + 1024 + lane * 16, q1);
#pragma unroll
      for (int j = 0; j < 4; ++j) { z0[j] = sel(hd, zz[8 + j], zz[j]); z1[j] = sel(hd, zz[12 + j], zz[4 + j]); }
      vst<f32x4>(L_ZS + w * 128 + (lane >> 4) * 32, z0);
      vst<f32x4>(L_ZS + w * 128 + (lane >> 4) * 32 + 16, z1);
      post(c, L_F1, layer + 1);
    }
    STAMPB(3 + 20 * layer);
    unsigned pq[8];
    {
      f32x16 aQ = {};
      v4u c0;
      auto hook = [&](int i) __attribute__((always_inline)) {
        if (i == 20) {
          if (layer == 0) {
            const GAS char* xr = (const GAS char*)P.x + (size_t)(tbw * 32 + t32) * 1024 + cb * 64 + h * 8;
#pragma unroll
            for (int g = 0; g < 4; ++g) { const v2u v = *(const GAS v2u*)(xr + g * 16); R[2 * g] = v.x; R[2 * g + 1] = v.y; }
          }
        }
        cs_hook<24, false>(c, i, 1, c0, c0);
      };
      gemm_single(c, B, aQ, hook);
      aQ = mfma(asb(c0), mfr, aQ);
#pragma unroll
      for (int j = 0; j < 8; ++j) { pq[j] = phi2(pair(aQ, j), f32x2{la, la}); PIN(pq[j]); }
    }
    STAMPB(4 + 20 * layer);
    {
      FRESH();
      f32x16 aM = {}, aO = {};
      Wide<16> d;
      const unsigned s0 = c.s;
      wide_steps<16, -16, 0>(c, d, B, aM, aO, kh, NoHook());
      STAMPB(54);
      v4u fk, fz;
      for (;;) {
        const unsigned fl = vld<unsigned>(L_F2);
        fk = vld<v4u>(L_KVR + lane * 16); fz = vld<v4u>(L_ZR + (lane >> 4) * 16);
        if (rfl(fl) >= 4u * layer + 4u || spent(c)) break;
      }
      STAMPB(5 + 20 * layer);
      permit(c, s0 + 8);
      const bool hd = (lane & 16) != 0;
      const v4u zero = {0u, 0u, 0u, 0u};
      const bf16x8 p0 = asb(v4u{pq[0], pq[1], pq[2], pq[3]}), p1 = asb(v4u{pq[4], pq[5], pq[6], pq[7]});
      f32x16 num = {}, den = {};
      den = mfma(asb(hd ? zero : fz), p0, den); den = mfma(asb(hd ? fz : zero), p1, den);
      num = mfma(asb(hd ? zero : fk), p0, num); num = mfma(asb(hd ? fk : zero), p1, num);
      const float i0 = __builtin_amdgcn_rcpf(den[0] + 1e-6f), i1 = __builtin_amdgcn_rcpf(den[8] + 1e-6f);
      const int os = WS_O + (tbq * 32 + 2 * m) * 1024;
      st16(rs, v4u{pk2(pair(num, 0) * i0), pk2(pair(num, 1) * i0), pk2(pair(num, 2) * i0), pk2(pair(num, 3) * i0)}, lane * 16, os);
      st16(rs, v4u{pk2(pair(num, 4) * i1), pk2(pair(num, 5) * i1), pk2(pair(num, 6) * i1), pk2(pair(num, 7) * i1)}, lane * 16 + 1024, os);
      DRAIN();
      set_flag(c, WS_FO + tbq * 256 + m * 4);
      STAMPB(6 + 20 * layer);
      FRESH();
      wait_flags(c, WS_FO + tbw * 256, kh ? 0xccccull : 0x3333ull);
      STAMPB(7 + 20 * layer);
      const int oo = WS_O + (tbw * 32 + 4 * kh) * 1024;
#pragma unroll
      for (int i = 0; i < AW; ++i) B[i] = ld16(rs, lane * 16 + (i & 3) * 1024, oo + (i >> 2) * 8192);
      SCHED();
      auto hook = [&](int i, int hf) __attribute__((always_inline)) {
        const int n = i + AW;
        if (hf == 0) return;
        if (n < 16) B[n] = ld16(rs, lane * 16 + (n & 3) * 1024, oo + (n >> 2) * 8192);
        if (n == 16) permit(c, s0 + 12);
      };
      wide_steps<16, 0, 16>(c, d, B, aM, aO, kh, hook);
      STAMPB(8 + 20 * layer);
      FRESH();
      finish_wide(c, aM, aO, R, 2 * layer + 1);
      publish_x(c, rs, R, WS_X1 + (tbw * 32 + 2 * cb) * 1024, WS_ST1 + (tbw * 16 + cb) * 256, WS_FX1 + tbw * 256 + cb * 4);
      vst<v4u>(L_KVS + w * 2048 + lane * 16, v4u{R[0], R[1], R[2], R[3]});
      vst<v4u>(L_KVS + w * 2048 + 1024 + lane * 16, v4u{R[4], R[5], R[6], R[7]});
    }
    STAMPB(9 + 20 * layer);
    unsigned pf;
    f32x16 aM = {}, aO = {};
    Wide<3> d;
    unsigned sd;
    int fd, hd0, fl2; unsigned long long mk;
    auto ldh = [&](int n) __attribute__((always_inline)) {
      if (n < 32) B[n % 16] = ld16(rs, lane * 16 + (n & 3) * 1024, hd0 + (((n >> 2) & 4) | (((n >> 2) + ROT(m)) & 3)) * 8192);
      else if (n < 40) B[n % 16] = ld16(rs, lane * 16 + (n & 3) * 1024, hd0 + (n >> 2) * 8192);
      else B[n % 16] = ld16(rs, lane * 16 + (n & 3) * 1024, hd0 + 81920 - kh * 1024);
    };
    {
      FRESH();
      f32x16 G0 = {}, U0 = {}, G1 = {}, U1 = {};
      Pipe q;
      const unsigned s0 = c.s;
      pair_steps<false, -PD, 0, 64, false>(c, q, B, G0, U0, NoHook());
      wait_flags(c, WS_FX1 + tbq * 256, 0xffffull);
      STAMPB(10 + 20 * layer);
      Stats sv;
      const int xo = WS_X1 + tbq * 32768;
#pragma unroll
      for (int i = 0; i < AH; ++i) B[i] = ld16(rs, lane * 16 + (i & 3) * 1024, xo + (((i >> 2) + ROT(m)) & 7) * 4096);
      SCHED();
      float a2; unsigned gm;
      v4u cg, cu;
      unsigned hp[8];
      const int ho = WS_H + (tbq * 86 + 2 * m) * 1024, fo = WS_FH + tbq * 256 + m * 4;
      float hv;
      fd = WS_FH + tbw * 256; mk = kh ? 0xccccull : 0x3333ull; hd0 = WS_H + (tbw * 86 + 4 * kh) * 1024;
      fl2 = fo + 128;
      auto piece = [&](int e, const f32x16& G, const f32x16& U) __attribute__((always_inline)) {
        float g;
        if (e & 1) asm volatile("v_max_f32 %1, 0, %2\n\tv_mul_f32 %0, %3, %4\n\tv_mul_f32 %1, %1, %0\n\tv_cvt_pk_bf16_f32 %0, %5, %1" : "=&v"(hp[e >> 1]), "=&v"(g) : "v"(G[e]), "v"(U[e]), "v"(a2), "v"(hv));
        else asm volatile("v_max_f32 %1, 0, %2\n\tv_mul_f32 %0, %3, %4\n\tv_mul_f32 %0, %1, %0" : "=&v"(hv), "=&v"(g) : "v"(G[e]), "v"(U[e]), "v"(a2));
      };
      auto tail = [&](const f32x16& G, const f32x16& U) __attribute__((always_inline)) {
#pragma unroll
        for (int j = 0; j < 8; ++j) {
          const f32x2 g = {fmaxf(G[2 * j], 0.f), fmaxf(G[2 * j + 1], 0.f)};
          hp[j] = pk2(g * (pair(U, j) * a2));
        }
      };
      auto store_h = [&](int r, int half) __attribute__((always_inline)) { st16(rs, v4u{hp[4 * half], hp[4 * half + 1], hp[4 * half + 2], hp[4 * half + 3]}, lane * 16 + half * 1024, ho + r * 32768); };
      auto correct = [&](f32x16& G, f32x16& U) __attribute__((always_inline)) {
        const bf16x8 f = asb(v4u{gm, gm, gm, gm});
        G = mfma(asb(cg), f, G); U = mfma(asb(cu), f, U);
      };
      auto shadow = [&](int i, int hf, int r, const f32x16& G, const f32x16& U) __attribute__((always_inline)) {
        constexpr int at[16] = {3, 5, 7, 9, 12, 13, 14, 15, 17, 19, 21, 23, 25, 28, 29, 30};
#pragma unroll
        for (int e = 0; e < 16; ++e) if (2 * i + hf == at[e]) piece(e, G, U);
        if (2 * i + hf == 31) store_h(r, 0);
        if (2 * i + hf == 33) store_h(r, 1);
        if (hf == 0 && i == 25) { if (r == 0) DRAIN(); else wait_vm<8>(); set_flag(c, fo + r * 64); }
        if (hf == 1) cs_hook<25, true>(c, i, 3 + r, cg, cu);
        if (r == 1) {
          if (hf == 0 && i == 8) pf = poll_issue(c, fd, mk);
          if (hf == 0 && i == 17) { poll_done<2>(c, pf, fd, mk); if (layer) STAMPB(51); }
          if (hf == 1 && i >= 17) ldh(i - 17);
          if (hf == 0 && i == 30) pf = poll_issue(c, fd, mk << 16);
        }
      };
      pair_steps<false, 0, 32, 64, false>(c, q, B, G0, U0, [&](int i, int hf) __attribute__((always_inline)) {
        const int n = i + AH;
        stats_hook(c, sv, 2 * i + hf, rs, WS_ST1 + tbq * 4096, a2, gm);
        if (hf == 0) return;
        if (n < 32) B[n] = ld16(rs, lane * 16 + (n & 3) * 1024, xo + (((n >> 2) + ROT(m)) & 7) * 4096);
        if (n == 32) permit(c, s0 + (m < 11 ? 23 : 19));
        if (i == 26) { a2 = fmul1(a2, a2); PIN(a2); }
        cs_hook<25, true>(c, i, 2, cg, cu);
      });
      correct(G0, U0);
      STAMPB(11 + 20 * layer);
      pair_steps<false, 32, 64, 96, false>(c, q, B, G1, U1, [&](int i, int hf) __attribute__((always_inline)) { shadow(i, hf, 0, G0, U0); });
      correct(G1, U1);
      STAMPB(12 + 20 * layer);
      pf = ~c.tag;
      if (sopaque(m) >= 11) {
        tail(G1, U1);
        store_h(1, 0); store_h(1, 1);
        DRAIN();
        set_flag(c, fo + 64);
        wait_flags(c, fd, mk);
      }
      if (sopaque(m) < 11) {
        G0 = f32x16{}; U0 = f32x16{};
        pair_steps<false, 64, 96, 96, false>(c, q, B, G0, U0, [&](int i, int hf) __attribute__((always_inline)) { shadow(i, hf, 1, G1, U1); });
        correct(G0, U0);
        tail(G0, U0);
        store_h(2, 0); store_h(2, 1);
        STAMPB(13 + 20 * layer);
      }
      if (sopaque(m) >= 11) {
#pragma unroll
        for (int i = 0; i < 15; ++i) ldh(i);
      }
      sd = c.s;
      wide_steps<43, -3, 0>(c, d, B, aM, aO, kh, NoHook());
    }
    STAMPB(14 + 20 * layer);
    {
      unsigned pf2;
      auto hook = [&](int i, int hf) __attribute__((always_inline)) {
        if (hf == 0) {
          if (i == 7) { wait_vm<7>(); set_flag(c, fl2); }
          if (i == 13) pf2 = poll_issue(c, fd, 0x7ffull << 32);
          return;
        }
        if (i == 1) { poll_done<5>(c, pf, fd, mk << 16); if (layer) STAMPB(47); }
        if (i == 19 && layer) STAMPB(48);
        if (i == 31 && layer) STAMPB(50);
        if (i + 15 < 32) ldh(i + 15);
        if (i == 20) { poll_done<4>(c, pf2, fd, 0x7ffull << 32); if (layer) STAMPB(49); }
        if (i >= 20 && i <= 23) { ldh(2 * i - 8); ldh(2 * i - 7); }
        if (i >= 24 && i <= 26) ldh(i + 16);
        if (i == PKVAT) permit(c, sd + 11 + PKV);
        if (i == 36) {
          const v4u r0 = vld<v4u>(L_KVS + w * 2048 + lane * 16), r1 = vld<v4u>(L_KVS + w * 2048 + 1024 + lane * 16);
          R[0] = r0.x; R[1] = r0.y; R[2] = r0.z; R[3] = r0.w; R[4] = r1.x; R[5] = r1.y; R[6] = r1.z; R[7] = r1.w;
        }
      };
      wide_steps<43, 0, 43>(c, d, B, aM, aO, kh, hook);
      STAMPB(17 + 20 * layer);
      FRESH();
      finish_wide(c, aM, aO, R, 2 * layer + 2);
    }
#pragma unroll
    for (int i = 0; i < 32; ++i) asm volatile("" : "=v"(B[i]));
    asm volatile("" : "=v"(aK), "=v"(aV), "=v"(la), "=v"(mf), "=v"(ck), "=v"(cv));
    asm volatile("" : "=v"(ar[0]), "=v"(ar[1]), "=v"(ar[2]), "=v"(ar[3]));
    if (layer == 0) {
      publish_x(c, rs, R, WS_X2 + (tbw * 32 + 2 * cb) * 1024, WS_ST2 + (tbw * 16 + cb) * 256, WS_FX2 + tbw * 256 + cb * 4);
      STAMPB(18);
      FRESH();
      aK = f32x16{}; aV = f32x16{};
      Pipe q;
      const unsigned s0 = c.s;
      pair_steps<true, -PD, 0, 32, false>(c, q, B, aK, aV, NoHook());
      wait_flags(c, WS_FX2 + tbq * 256, 0xffffull);
      STAMPB(20);
      Stats sv;
      const int xo = WS_X2 + tbq * 32768;
#pragma unroll
      for (int i = 0; i < AH; ++i) B[i] = ld16(rs, lane * 16 + (i & 3) * 1024, xo + (((i >> 2) + ROT(m)) & 7) * 4096);
      SCHED();
      auto hook = [&](int i, int hf) __attribute__((always_inline)) {
        const int n = i + AH;
        stats_hook(c, sv, 2 * i + hf, rs, WS_ST2 + tbq * 4096, la, mf);
        if (hf == 0) {
          if (i == 25) vst<float>(L_SA + w * 128 + t32 * 4, la);
          if (i == 26) { ar[0] = vld<f32x4>(L_SA + w * 128 + 4 * h * 4); ar[1] = vld<f32x4>(L_SA + w * 128 + (8 + 4 * h) * 4); }
          if (i == 27) { ar[2] = vld<f32x4>(L_SA + w * 128 + (16 + 4 * h) * 4); ar[3] = vld<f32x4>(L_SA + w * 128 + (24 + 4 * h) * 4); }
          return;
        }
        if (n < 32) B[n] = ld16(rs, lane * 16 + (n & 3) * 1024, xo + (((n >> 2) + ROT(m)) & 7) * 4096);
        if (n == 32) permit(c, s0 + KVP2);
        cs_hook<26, true>(c, i, 0, ck, cv);
      };
      pair_steps<true, 0, 32, 32, false>(c, q, B, aK, aV, hook);
    }
  }
  {
    layer = 0;
    FRESH();
    GAS char* o = (GAS char*)P.out + (size_t)(tbw * 32 + t32) * 1024 + cb * 64 + h * 8;
#pragma unroll
    for (int g = 0; g < 4; ++g) *(GAS v2u*)(o + g * 16) = v2u{R[2 * g], R[2 * g + 1]};
  }
  STAMPB(38);
  STAMP_END();
  STAMPV(42, (unsigned long long)(BUDGET - c.budget));
  if (b == 0 && w == 0 && lane == 0) *(GAS unsigned*)(P.ws + WS_EPOCH) = c.tag;
}
}

extern "C" void kernel_launch(void* const* d_in, const int* in_sizes, int n_in, void* d_out, int out_size, void* d_ws,
                              size_t ws_size, hipStream_t stream) {
  Params P;
  P.x = (const char*)d_in[0]; P.wqkv = (const char*)d_in[1]; P.wo = (const char*)d_in[2];
  P.wg = (const char*)d_in[4]; P.wu = (const char*)d_in[5]; P.wd = (const char*)d_in[6];
  P.out = (char*)d_out; P.ws = (char*)d_ws;
  (void)hipFuncSetAttribute((const void*)fused_fwd, hipFuncAttributeMaxDynamicSharedMemorySize, LDS_BYTES);
  fused_fwd<<<256, 512, LDS_BYTES, stream>>>(P);
}
```

```cpp
#include <hip/hip_runtime.h>
#include <stdint.h>

#define GAS __attribute__((address_space(1)))
#define LAS __attribute__((address_space(3)))
#define DI __device__ __forceinline__
typedef unsigned v4u __attribute__((ext_vector_type(4)));
typedef unsigned v2u __attribute__((ext_vector_type(2)));
typedef float f32x16 __attribute__((ext_vector_type(16)));
typedef float f32x4 __attribute__((ext_vector_type(4)));
typedef float f32x2 __attribute__((ext_vector_type(2)));
typedef __bf16 bf16x8 __attribute__((ext_vector_type(8)));
typedef __bf16 bf16x2 __attribute__((ext_vector_type(2)));
typedef short s16x4 __attribute__((ext_vector_type(4)));

#define MAPMODE 1
#define LDEPTH 8
#define LPRIO 0
#define LSLEEP 1
#define AW 8
#define PKV 8
#define THIN 2
#define PD 2
#define PKVAT 16
#define THINB 18
#define KVP2 14
#define PACEB 10
#define XUP 2
#define RPP 0
#define FS 7
#define PI 13
#define PC 20
#define AH 8

#define ROT(m) (((m) + 2 * p) & 7)
#define GM(m) ((((m) & 7) << 1) | ((m) >> 3))
static_assert(AH == 8, "");
namespace {
constexpr int KB = 1024;
constexpr int WS_EPOCH = 0;
constexpr int WS_FKV = 4096;
constexpr int WS_FO = WS_FKV + 32 * KB;
constexpr int WS_FX1 = WS_FO + 32 * KB;
constexpr int WS_FH = WS_FX1 + 32 * KB;
constexpr int WS_FX2 = WS_FH + 32 * KB;
constexpr int WS_ST1 = WS_FX2 + 16 * KB;
constexpr int WS_ST2 = WS_ST1 + 512 * KB;
constexpr int KVW = 1152;
constexpr int WS_KVP = WS_ST2 + 256 * KB;
constexpr int WS_CSP = WS_KVP + 2 * 16 * 16 * 2 * KVW;
constexpr int WS_O = WS_CSP + 16 * 2 * 16 * 768;
constexpr int WS_X1 = WS_O + 4096 * KB;
constexpr int WS_X2 = WS_X1 + 4096 * KB;
constexpr int WS_H = WS_X2 + 2048 * KB;
constexpr int WS_RQ = WS_H + 2 * 64 * 86 * KB;
constexpr int WS_RG = WS_RQ + 1536 * KB;
constexpr int WS_RU = WS_RG + 43 * 32 * KB;
constexpr int WS_DBG = WS_RU + 43 * 32 * KB;
constexpr int WS_END = WS_DBG + 256 * 8 * 64 * 8;
constexpr int NSLOT = 8;
constexpr int L_RING = 0;
constexpr int L_SCR = NSLOT * 16 * KB;
constexpr int L_KVS = L_SCR + 16 * KB;
constexpr int L_CSF = L_KVS + 8 * KB;
constexpr int L_SA = L_CSF + 5120;
constexpr int L_KVR = L_SA + 512;
constexpr int L_ZR = L_KVR + 1024;
constexpr int L_ZS = L_ZR + 64;
constexpr int L_DUMMY = L_ZS + 512;
constexpr int L_RDY = L_DUMMY + 448;
constexpr int L_PL = L_RDY + 16;
constexpr int L_CSC = L_RDY + 32;
constexpr int L_FCNT = L_CSC + 32;
constexpr int L_F1 = L_FCNT + 16;
constexpr int L_F2 = L_F1 + 16;
constexpr int L_F3 = L_F2 + 16;
constexpr int L_RPD = L_F3 + 16;
constexpr int L_END = L_RPD + 16;
static_assert(L_END <= 160 * KB, "LDS");
constexpr int LDS_BYTES = (L_END + 15) / 16 * 16;
constexpr unsigned CODE_BYTES = 38912;
constexpr unsigned BUDGET = 1u << 18;

struct Params {
  const char* x; const char* wqkv; const char* wo; const char* wg; const char* wu; const char* wd;
  char* out; char* ws;
};

extern __shared__ __attribute__((aligned(16))) char smem[];
template <class T> DI LAS T* lp(unsigned off) { return (LAS T*)((LAS char*)smem + off); }
template <class T> DI T vld(unsigned off) { return *(volatile LAS T*)((LAS char*)smem + off); }
template <class T> DI void vst(unsigned off, T v) { *(volatile LAS T*)((LAS char*)smem + off) = v; }
DI unsigned rfl(unsigned v) { return __builtin_amdgcn_readfirstlane(v); }
DI const char* uni(const char* p) { const unsigned long long v = (unsigned long long)p; return (const char*)(((unsigned long long)rfl((unsigned)(v >> 32)) << 32) | rfl((unsigned)v)); }
#define SCHED() __builtin_amdgcn_sched_barrier(0)
#define CFENCE() asm volatile("" ::: "memory")
#define DRAIN() asm volatile("s_waitcnt vmcnt(0)" ::: "memory")
#define PIN(x) asm volatile("" : "+v"(x))

#define STAMP(i) do {} while (0)
#define STAMPV(i, v) do {} while (0)
#define STAMPB(i) do {} while (0)
#define STAMP_END() do {} while (0)

DI bf16x8 ldw(unsigned a, int second) {
  s16x4 lo = __builtin_amdgcn_ds_read_tr16_b64_v4i16(lp<s16x4>(a));
  s16x4 hi = __builtin_amdgcn_ds_read_tr16_b64_v4i16(lp<s16x4>(a + second));
  return __builtin_bit_cast(bf16x8, __builtin_shufflevector(lo, hi, 0, 1, 2, 3, 4, 5, 6, 7));
}
DI f32x16 mfma(bf16x8 a, bf16x8 b, f32x16 c) { return __builtin_amdgcn_mfma_f32_32x32x16_bf16(a, b, c, 0, 0, 0); }
DI bf16x8 asb(v4u v) { return __builtin_bit_cast(bf16x8, v); }
DI unsigned pk2(float lo, float hi) { bf16x2 r = {(__bf16)lo, (__bf16)hi}; return __builtin_bit_cast(unsigned, r); }
DI unsigned pk2(f32x2 v) { return pk2(v.x, v.y); }
DI float dot2(unsigned a, unsigned b, float c) { return __builtin_amdgcn_fdot2_f32_bf16(__builtin_bit_cast(bf16x2, a), __builtin_bit_cast(bf16x2, b), c, false); }
DI float u2f(unsigned u) { return __builtin_bit_cast(float, u); }
DI unsigned f2u(float f) { return __builtin_bit_cast(unsigned, f); }
DI float sel(bool c, float a, float b) { asm volatile("" : "+v"(a), "+v"(b)); return c ? a : b; }
DI int opaque(int v) { asm volatile("" : "+v"(v)); return v; }
DI int sopaque(int v) { asm volatile("" : "+s"(v)); return v; }
DI float blo(unsigned u) { return u2f(u << 16); }
DI float bhi(unsigned u) { return u2f(u & 0xffff0000u); }
DI float sum_halves(float v) {
  auto r = __builtin_amdgcn_permlane32_swap(f2u(v), f2u(v), false, false);
  float o; asm("v_add_f32 %0, %1, %2" : "=v"(o) : "v"(r[0]), "v"(r[1])); return o;
}
template <int CTRL> DI float dpp_add(float v) { return v + u2f(__builtin_amdgcn_update_dpp(0u, f2u(v), CTRL, 0xf, 0xf, false)); }
DI float row_sum(float v) { v = dpp_add<0x128>(v); v = dpp_add<0x124>(v); v = dpp_add<0x122>(v); return dpp_add<0x121>(v); }
DI f32x2 pair(const f32x16& a, int j) { return f32x2{a[2 * j], a[2 * j + 1]}; }
DI unsigned phi2(f32x2 x, f32x2 a) {
  const f32x2 e2 = x * (a * 1.4426950408889634f);
  const f32x2 t = x * a + 1e-6f;
  const f32x2 e = {__builtin_amdgcn_fmed3f(__builtin_amdgcn_exp2f(e2.x), 0.f, 1.f), __builtin_amdgcn_fmed3f(__builtin_amdgcn_exp2f(e2.y), 0.f, 1.f)};
  const f32x2 mx = {fmaxf(t.x, 1e-6f), fmaxf(t.y, 1e-6f)};
  return pk2(mx + e);
}
constexpr unsigned ONES = 0x3f803f80u;
DI float fadd1(float a, float b) { float r; asm("v_add_f32 %0, %1, %2" : "=v"(r) : "v"(a), "v"(b)); return r; }
DI float fmul1(float a, float b) { float r; asm("v_mul_f32 %0, %1, %2" : "=v"(r) : "v"(a), "v"(b)); return r; }

typedef __amdgpu_buffer_rsrc_t rsrc_t;
DI v4u ld16(rsrc_t rs, int voff, int soff) { return __builtin_amdgcn_raw_buffer_load_b128(rs, voff, soff, 16); }
DI v2u ld8(rsrc_t rs, int voff, int soff) { return __builtin_amdgcn_raw_buffer_load_b64(rs, voff, soff, 16); }
DI void st16(rsrc_t rs, v4u v, int voff, int soff) {
  __builtin_amdgcn_raw_buffer_store_b128(v, rs, voff, soff, 16);
  asm volatile("s_nop 1" ::"v"(v) : "memory");
}
DI void st8(rsrc_t rs, v2u v, int voff, int soff) { __builtin_amdgcn_raw_buffer_store_b64(v, rs, voff, soff, 16); }

struct CW {
  unsigned s, pos;
  unsigned lane_tr;
  unsigned fa;
  unsigned tag, perm;
  int budget;
  int lane, w;
  char* ws;
};
DI bool spent(CW& c) { c.budget -= 1; return c.budget < 0; }
DI unsigned min4(v4u v) { return rfl(min(min(v.x, v.y), min(v.z, v.w))); }
DI void wait_count(CW& c, unsigned have, unsigned addr, unsigned need) {
  if (__builtin_expect(rfl(have) < need, 0)) {
    for (;;) { if (rfl(vld<unsigned>(addr)) >= need || spent(c)) break; }
  }
}
DI void wait_slot(CW& c, unsigned have, unsigned t) {
  if (__builtin_expect(have < t, 0)) {
    for (;;) { const v2u v = vld<v2u>(L_PL); if (rfl(min(v.x, v.y)) >= t || spent(c)) break; }
  }
}
DI void post(CW& c, int word, unsigned v) { vst<unsigned>(c.fa + (word - L_FCNT), v); }
DI void release(CW& c) { c.s += 1; c.pos = (c.pos + 1) & (NSLOT - 1); post(c, L_FCNT, min(c.s + NSLOT, c.perm)); }
DI void permit(CW& c, unsigned v) { c.perm = v; post(c, L_FCNT, min(c.s + NSLOT, v)); }
DI void set_flag(CW& c, int off) {
  const char* base = uni(c.ws + off);
  asm volatile("s_mov_b64 exec, 1\n\ts_nop 4\n\tglobal_atomic_swap %0, %1, %2\n\ts_mov_b64 exec, -1" ::"v"(0u), "v"(c.tag), "s"(base) : "memory");
}
DI void wait_flags(CW& c, int off, unsigned long long mask) {
  unsigned f, bud = rfl(c.budget);
  const unsigned voff = c.lane * 4;
  const char* base = uni(c.ws + off);
  asm volatile(
      "s_mov_b64 exec, %5\n\ts_nop 4\n\t"
      "1:\n\t"
      "global_atomic_or %0, %2, %6, %3 sc0\n\t"
      "s_waitcnt vmcnt(0)\n\t"
      "v_cmp_ne_u32 vcc, %4, %0\n\t"
      "s_cbranch_vccz 2f\n\t"
      "s_sub_i32 %1, %1, 8\n\t"
      "s_cmp_ge_i32 %1, 0\n\t"
      "s_cbranch_scc1 1b\n\t"
      "2:\n\t"
      "s_mov_b64 exec, -1\n\t"
      : "=&v"(f), "+s"(bud) : "v"(voff), "s"(base), "s"(c.tag), "s"(mask), "v"(0u) : "memory", "vcc", "scc");
  c.budget = rfl(bud);
}
DI unsigned poll_issue(CW& c, int off, unsigned long long mask) {
  unsigned f = c.tag;
  const unsigned voff = c.lane * 4;
  const char* base = uni(c.ws + off);
  asm volatile("s_mov_b64 exec, %3\n\tv_not_b32 %0, %0\n\ts_nop 4\n\tglobal_atomic_or %0, %1, %4, %2 sc0\n\ts_mov_b64 exec, -1"
               : "+v"(f) : "v"(voff), "s"(base), "s"(mask), "v"(0u) : "memory");
  return f;
}
template <int N> DI void poll_done(CW& c, unsigned f, int off, unsigned long long mask) {
  asm volatile("s_waitcnt vmcnt(%c1)" : "+v"(f) : "i"(N) : "memory");
  if (__builtin_expect(__builtin_amdgcn_ballot_w64(f != c.tag) != 0ull, 0)) wait_flags(c, off, mask);
}

template <int V> struct IC { static constexpr int value = V; DI constexpr operator int() const { return V; } };
template <int I, int E, class F> DI void static_for(F&& f) { if constexpr (I < E) { f(IC<I>{}); static_for<I + 1, E>(f); } }
struct Pipe { bf16x8 wa[PD + 1], wb[PD + 1]; v2u pl; unsigned ad, ad2, adn, have, rel; };
#define SPIN(x) asm volatile("" : "+s"(x))
template <bool Q0> DI void slot_first(CW& c, Pipe& q) {
  if (Q0) wait_count(c, 0u, L_RDY, 4u); else wait_slot(c, 0u, c.s + 1);
  q.adn = c.lane_tr + c.pos * 16384u;
}
template <int K, bool NX, int CK, bool Q0 = false> DI void book(CW& c, Pipe& q) {
  if constexpr (CK && K == 0) { if (Q0) q.pl.x = vld<unsigned>(L_RDY + (c.s + 1) * 4); else q.pl = vld<v2u>(L_PL); }
  if constexpr (NX && K == 2) { q.adn = c.lane_tr + ((c.pos + 1) & (NSLOT - 1)) * 16384u; PIN(q.adn); }
  if constexpr (CK && K == 3) { q.have = rfl(Q0 ? q.pl.x : min(q.pl.x, q.pl.y)); SPIN(q.have); }
  if constexpr (CK && K == 4) { if (Q0) wait_count(c, q.have, L_RDY + (c.s + 1) * 4, 4u); else wait_slot(c, q.have, c.s + CK); }
  if constexpr (K == 5) { c.s += 1; c.pos = (c.pos + 1) & (NSLOT - 1); q.rel = min(c.s + NSLOT, c.perm); PIN(q.rel); }
  if constexpr (K == 6) post(c, L_FCNT, q.rel);
}
template <bool SWAP, int I0, int I1, int NEND, bool MORE, bool FR = true, class HOOK>
DI void pair_steps(CW& c, Pipe& q, v4u (&B)[32], f32x16& acc0, f32x16& acc1, HOOK hook) {
  const unsigned keep = c.lane_tr;
  if (FR) c.lane_tr = c.lane * 16;
  static_for<I0, I1>([&](auto ic) __attribute__((always_inline)) {
    constexpr int i = ic.value, n = i + PD;
    constexpr bool F = n < NEND, NX = F && (MORE || n + 8 - n % 8 < NEND);
    constexpr int CK = !NX ? 0 : !FR ? 1 : (n / 8) % 2 ? 2 : 0;
    if constexpr (n == 0) slot_first<!FR>(c, q);
    if constexpr (i >= 0) acc0 = SWAP ? mfma(asb(B[i % 32]), q.wa[i % (PD + 1)], acc0) : mfma(q.wa[i % (PD + 1)], asb(B[i % 32]), acc0);
    SCHED();
    if constexpr (F && n % 8 == 0) q.ad = q.adn;
    if constexpr (F) q.wa[n % (PD + 1)] = FR ? asb(vld<v4u>(q.ad + (n % 8) * 1024)) : ldw(q.ad + (n % 8) * 1024, 512);
    if constexpr (F && n % 8 >= 2) book<n % 8 - 2, NX, CK, !FR>(c, q);
    hook(IC<(i < 0 ? i : i % 32)>{}, IC<0>{});
    SCHED();
    if constexpr (i >= 0) acc1 = SWAP ? mfma(asb(B[i % 32]), q.wb[i % (PD + 1)], acc1) : mfma(q.wb[i % (PD + 1)], asb(B[i % 32]), acc1);
    SCHED();
    if constexpr (F) q.wb[n % (PD + 1)] = FR ? asb(vld<v4u>(q.ad + 8192 + (n % 8) * 1024)) : ldw(q.ad + 8192 + (n % 8) * 1024, 512);
    if constexpr (F && n % 8 == 7) book<6, NX, CK>(c, q);
    hook(IC<(i < 0 ? i : i % 32)>{}, IC<1>{});
    SCHED();
  });
  c.lane_tr = keep;
}
template <bool SWAP, class HOOK>
DI void gemm_pair(CW& c, v4u (&B)[32], f32x16& acc0, f32x16& acc1, HOOK hook) {
  Pipe q;
  pair_steps<SWAP, -PD, 32, 32, false, false>(c, q, B, acc0, acc1, hook);
}
template <class HOOK>
DI void gemm_single(CW& c, v4u (&B)[32], f32x16& acc, HOOK hook) {
  bf16x8 wa[5];
  Pipe q;
  static_for<-4, 32>([&](auto ic) __attribute__((always_inline)) {
    constexpr int i = ic.value, n = i + 4;
    if constexpr (n == 0) { wait_slot(c, 0u, c.s); q.adn = c.lane_tr + c.pos * 16384u; }
    if constexpr (i >= 0) acc = mfma(wa[i % 5], asb(B[i]), acc);
    SCHED();
    if constexpr (n < 32) {
      if constexpr (n % 16 == 0) q.ad = q.adn;
      wa[n % 5] = ldw(q.ad + (n % 16) * 1024, 512);
      if constexpr (n % 16 == 6) book<0, (n < 16), (n < 16)>(c, q);
      if constexpr (n % 16 >= 10) book<n % 16 - 9, (n < 16), (n < 16)>(c, q);
    }
    hook(ic);
    SCHED();
  });
}
constexpr int TL = 6;
template <int PW> struct Wide { bf16x8 wa[PW + 1 + TL], wb[PW + 1]; Pipe q; unsigned dO, lt; };
template <int NK, int I0, int I1, int PW, class HOOK>
DI void wide_steps(CW& c, Wide<PW>& d, v4u (&B)[32], f32x16& accM, f32x16& accO, int kh, HOOK hook) {
  Pipe& q = d.q;
  const unsigned keep = c.lane_tr;
  if constexpr (I0 == -PW) { d.lt = c.lane_tr + kh * (8192u + 512u); d.dO = kh ? -512u : 512u; }
  c.lane_tr = d.lt;
  static_for<I0, I1>([&](auto ic) __attribute__((always_inline)) {
    constexpr int i = ic.value, n = i + PW;
    constexpr bool F = n < NK, NX = F && n + 4 - n % 4 < NK;
    constexpr int CK = !NX || (n / 4) % 2 == 0 ? 0 : n + 8 - n % 4 < NK ? 2 : 1;
    if constexpr (n == 0) slot_first<false>(c, q);
    if constexpr (i >= 0) accO = mfma(d.wb[i % (PW + 1)], asb(B[i % 16]), accO);
    SCHED();
    if constexpr (F && n % 4 == 0) { q.ad = q.adn; if constexpr (n + 3 == NK) q.ad -= kh * 2048u; q.ad2 = q.ad + d.dO; }
    if constexpr (F) d.wb[n % (PW + 1)] = ldw(q.ad2 + (n % 4) * 2048, 1024);
    if constexpr (F && n % 4 == 1) { book<2, NX, CK>(c, q); }
    if constexpr (F && n % 4 == 2) { book<3, NX, CK>(c, q); }
    if constexpr (F && (n % 4 == 3 || n == NK - 1)) { book<5, NX, CK>(c, q); }
    hook(ic, IC<0>{});
    SCHED();
    if constexpr (i >= 0 && i < NK - TL) accM = mfma(d.wa[i % (PW + 1 + TL)], asb(B[i % 16]), accM);
    SCHED();
    if constexpr (F) d.wa[n % (PW + 1 + TL)] = ldw(q.ad + (n % 4) * 2048, 1024);
    if constexpr (F && n % 4 == 0) book<0, NX, CK>(c, q);
    if constexpr (F && n % 4 == 2) book<4, NX, CK>(c, q);
    if constexpr (F && (n % 4 == 3 || n == NK - 1)) book<6, NX, CK>(c, q);
    hook(ic, IC<1>{});
    SCHED();
  });
  c.lane_tr = keep;
}
struct NoHook { template <class A, class B> DI void operator()(A, B) const {} };

DI void ln_factors(float sum, float sq, float& a, unsigned& mf, bool up) {
  float mean = sum * (1.0f / 512.0f);
  PIN(mean);
  const float var = fmaxf(sq * (1.0f / 512.0f) - mean * mean, 0.f);
  a = __builtin_amdgcn_rsqf(var + 1e-5f);
  const unsigned hi = pk2(-mean, -mean);
  const float lo = -mean - blo(hi);
  mf = up ? pk2(lo, lo) : hi;
}
struct Stats { v4u sv[4]; f32x4 t; f32x2 u[4]; float mean, var; };
constexpr int K0 = 34;
DI void stats_hook(CW& c, Stats& t, int k, rsrc_t rs, int soff, float& a, unsigned& mf) {
  if (k == 3 || k == 7 || k == 11 || k == 15) t.sv[k >> 2] = ld16(rs, c.lane * 16 + (k >> 2) * 1024, soff);
  const unsigned sc = L_SCR + (c.w ^ 2) * 4096;
  if (k >= K0 && k < K0 + 4) { const int e = k - K0; t.t[e] = fadd1(fadd1(u2f(t.sv[0][e]), u2f(t.sv[1][e])), fadd1(u2f(t.sv[2][e]), u2f(t.sv[3][e]))); PIN(t.t[e]); }
  if (k == K0 + 4) vst<f32x4>(sc + c.lane * 16, t.t);
  if (k == K0 + 5) { t.u[0] = vld<f32x2>(sc + (c.lane & 31) * 8); t.u[1] = vld<f32x2>(sc + 256 + (c.lane & 31) * 8); }
  if (k == K0 + 6) { t.u[2] = vld<f32x2>(sc + 512 + (c.lane & 31) * 8); t.u[3] = vld<f32x2>(sc + 768 + (c.lane & 31) * 8); }
  if (k == K0 + 10) { t.mean = fadd1(fadd1(t.u[0].x, t.u[1].x), fadd1(t.u[2].x, t.u[3].x)); PIN(t.mean); }
  if (k == K0 + 11) { t.var = fadd1(fadd1(t.u[0].y, t.u[1].y), fadd1(t.u[2].y, t.u[3].y)); PIN(t.var); }
  if (k == K0 + 12) { t.mean *= -1.0f / 512.0f; PIN(t.mean); t.var = t.var * (1.0f / 512.0f) + 1e-5f; PIN(t.var); }
  if (k == K0 + 13) { t.var = fmaxf(t.var - t.mean * t.mean, 1e-5f); PIN(t.var); a = __builtin_amdgcn_rsqf(t.var); PIN(a); }
  if (k == K0 + 14) { mf = pk2(t.mean, t.mean); t.mean -= blo(mf); PIN(mf); PIN(t.mean); }
  if (k == K0 + 15) { mf = c.lane >= 32 ? pk2(t.mean, t.mean) : mf; PIN(mf); }
}
DI f32x16 unpack(const unsigned (&R)[8]) {
  f32x16 a;
#pragma unroll
  for (int j = 0; j < 8; ++j) { a[2 * j] = blo(R[j]); a[2 * j + 1] = bhi(R[j]); }
  return a;
}
template <int NK, int PW>
DI void finish_wide(CW& c, Wide<PW>& d, v4u (&B)[32], f32x16& accM, const f32x16& accO, unsigned (&R)[8], unsigned seq) {
  const unsigned mine = L_SCR + c.w * 4096 + c.lane * 16, other = L_SCR + (c.w ^ 2) * 4096 + c.lane * 16;
  auto step = [&](int k) __attribute__((always_inline)) { accM = mfma(d.wa[(NK - TL + k) % (PW + 1 + TL)], asb(B[(NK - TL + k) % 16]), accM); SCHED(); };
  auto put = [&](int g) __attribute__((always_inline)) { vst<f32x4>(mine + g * 1024, f32x4{accO[4 * g], accO[4 * g + 1], accO[4 * g + 2], accO[4 * g + 3]}); };
  step(0); step(1);
  put(0); put(1); SCHED();
  step(2);
  put(2); put(3); post(c, L_F3, seq); SCHED();
  step(3); step(4);
  unsigned fl = vld<unsigned>(L_F3 + (c.w ^ 2) * 4);
  f32x4 v[4];
#pragma unroll
  for (int g = 0; g < 4; ++g) v[g] = vld<f32x4>(other + g * 1024);
  SCHED();
  step(5);
  if (rfl(fl) < seq) {
    for (;;) {
      fl = vld<unsigned>(L_F3 + (c.w ^ 2) * 4);
#pragma unroll
      for (int g = 0; g < 4; ++g) v[g] = vld<f32x4>(other + g * 1024);
      if (rfl(fl) >= seq || spent(c)) break;
    }
  }
#pragma unroll
  for (int g = 0; g < 4; ++g) {
    R[2 * g] = pk2(pair(accM, 2 * g) + f32x2{v[g].x, v[g].y});
    R[2 * g + 1] = pk2(pair(accM, 2 * g + 1) + f32x2{v[g].z, v[g].w});
  }
}
DI void publish_x(CW& c, rsrc_t rs, const unsigned (&R)[8], int xoff, int stoff) {
  st16(rs, v4u{R[0], R[1], R[2], R[3]}, c.lane * 16, xoff);
  st16(rs, v4u{R[4], R[5], R[6], R[7]}, c.lane * 16 + 1024, xoff);
  SCHED();
  float s0 = 0.f, q0 = 0.f, s1 = 0.f, q1 = 0.f;
#pragma unroll
  for (int i = 0; i < 4; ++i) { s0 = dot2(R[i], ONES, s0); q0 = dot2(R[i], R[i], q0); s1 = dot2(R[4 + i], ONES, s1); q1 = dot2(R[4 + i], R[4 + i], q1); }
  const float s = sum_halves(s0 + s1), q = sum_halves(q0 + q1);
  st8(rs, v2u{f2u(s), f2u(q)}, (c.lane & 31) * 8, stoff);
}

DI void dma4(const char* base, unsigned v0, unsigned v1, unsigned v2, unsigned v3, unsigned dst) {
  asm volatile("s_mov_b32 m0, %5\n\ts_nop 3\n\t"
               "global_load_lds_dwordx4 %0, %4\n\tglobal_load_lds_dwordx4 %1, %4 offset:1024\n\t"
               "global_load_lds_dwordx4 %2, %4 offset:2048\n\tglobal_load_lds_dwordx4 %3, %4 offset:3072"
               ::"v"(v0), "v"(v1), "v"(v2), "v"(v3), "s"(uni(base)), "s"(rfl(dst)) : "memory");
}
#define PACE "s_sleep 1\n\t"
DI void dma4p(const char* base, unsigned v0, unsigned v1, unsigned v2, unsigned v3, unsigned dst) {
  asm volatile("s_mov_b32 m0, %5\n\ts_nop 3\n\t"
               "global_load_lds_dwordx4 %0, %4\n\t" PACE "global_load_lds_dwordx4 %1, %4 offset:1024\n\t" PACE
               "global_load_lds_dwordx4 %2, %4 offset:2048\n\t" PACE "global_load_lds_dwordx4 %3, %4 offset:3072\n\t" PACE
               ::"v"(v0), "v"(v1), "v"(v2), "v"(v3), "s"(uni(base)), "s"(rfl(dst)) : "memory");
}
DI void dma4_sc1(const char* base, unsigned v, unsigned dst) {
  asm volatile("s_mov_b32 m0, %2\n\ts_nop 3\n\t"
               "global_load_lds_dwordx4 %0, %1 sc1\n\t" PACE "global_load_lds_dwordx4 %0, %1 offset:1024 sc1\n\t" PACE
               "global_load_lds_dwordx4 %0, %1 offset:2048 sc1\n\t" PACE "global_load_lds_dwordx4 %0, %1 offset:3072 sc1\n\t" PACE
               ::"v"(v), "s"(uni(base)), "s"(rfl(dst)) : "memory");
}
DI void dma4_sc1f(const char* base, unsigned v, unsigned dst) {
  asm volatile("s_mov_b32 m0, %2\n\ts_nop 3\n\t"
               "global_load_lds_dwordx4 %0, %1 sc1\n\tglobal_load_lds_dwordx4 %0, %1 offset:1024 sc1\n\t"
               "global_load_lds_dwordx4 %0, %1 offset:2048 sc1\n\tglobal_load_lds_dwordx4 %0, %1 offset:3072 sc1"
               ::"v"(v), "s"(uni(base)), "s"(rfl(dst)) : "memory");
}
template <int N> DI void wait_vm() { asm volatile("s_waitcnt vmcnt(%c0)" ::"i"(N) : "memory"); }
DI unsigned tr_lane(int lane) { return (4 * (lane >> 5) + ((lane & 15) >> 2)) * 64 + ((lane >> 4) & 1) * 32 + (lane & 3) * 8; }
DI int fresh_lane() { int l; asm volatile("v_mbcnt_lo_u32_b32 %0, -1, 0\n\tv_mbcnt_hi_u32_b32 %0, -1, %0" : "=v"(l)); return l; }
#define LSTAMP(v, s) do {} while (0)
DI void lds_add(unsigned pa, unsigned off, unsigned n) { CFENCE(); (void)__hip_atomic_fetch_add(lp<unsigned>(pa + off), n, __ATOMIC_RELAXED, __HIP_MEMORY_SCOPE_WORKGROUP); CFENCE(); }
DI void loader(const Params& P, int p, int m, int lw) {
  const int lane = fresh_lane();
  const unsigned smem_base = (unsigned)(uintptr_t)(LAS char*)smem;
  const unsigned nr = GM(m) < 11 ? 3 : 2, NS = 21 + 4 * nr, NT = 2 * NS;
  const unsigned v_lin = lane * 16, v_wide = ((lane & 31) >> 2) * 1024 + (lane >> 5) * 64 + (lane & 3) * 16, v_raw = (lane >> 2) * 3072 + (lane & 3) * 16;
  const unsigned pa = lane == 0 ? L_RDY : L_DUMMY + lane * 4;
  const unsigned pp = pa + (L_PL - L_RDY) + lw * 4;
  __builtin_amdgcn_s_setprio(LPRIO);
  unsigned limit = 0;
  int budget = BUDGET;
  bool pend = false;
  auto entry = [&](unsigned s) __attribute__((always_inline)) -> unsigned long long {
    const bool l2 = s >= NS;
    const unsigned q = lane & 3, i = l2 ? s - NS : s, g = i - 10u, d = g - 4u * nr, r4 = 4u * ROT(m);
    const unsigned blk = i < 4u ? q >> 1 : 2u, ks0 = ((i < 4u ? 8u * i + (q & 1) * 4u : 16u * (i - 4u) + q * 4u) + r4) & 31u;
    if (i < 6u && l2) return (unsigned long long)P.ws + WS_RQ + ((m * 3 + blk) * 32u + ks0) * 1024u + 1u;
    if (i < 6u) return (unsigned long long)P.wqkv + m * 64 + ks0 * (16u * 3072u);
    if (i < 10u) return (unsigned long long)P.wo + ((i - 6u) * 128u + q * 32u) * 1024u + (m & 7) * 128 + 2u;
    if (g < 4u * nr) return (unsigned long long)P.ws + ((q >> 1) ? WS_RU : WS_RG) + ((16u * (g >> 2) + GM(m)) * 32u + ((8u * (g & 3u) + (q & 1) * 4u + r4) & 31u)) * 1024u + 1u;
    return (unsigned long long)P.wd + min((d < 8u ? (d & 4u) | ((d + ROT(m)) & 3u) : d) * 128u + q * 32u, 1344u) * 1024u + (m & 7) * 128 + 2u;
  };
  unsigned long long tab = entry(4u + lw + 2u * (lane >> 2));
  const unsigned long long tab2 = entry(36u + lw + 2u * (lane >> 2));
  for (unsigned s = 4 + lw, t = 0; s < NT; s += 2, t = (t + 4) & 63u) {
    const unsigned i = s >= NS ? s - NS : s;
    if (s >= 36u) tab = tab2;
    const char* src[4];
#pragma unroll
    for (int q = 0; q < 4; ++q)
      src[q] = (const char*)(((unsigned long long)__builtin_amdgcn_readlane((unsigned)(tab >> 32), t + q) << 32) | (__builtin_amdgcn_readlane((unsigned)tab, t + q) & ~3u));
    const unsigned kind = __builtin_amdgcn_readlane((unsigned)tab, t) & 3u;
    if (pend && i < THINB) { wait_vm<0>(); vst<unsigned>(pp, s - 1); LSTAMP(tlan, s - 2); pend = false; }
    while (s >= limit) {
      limit = min4(vld<v4u>(L_FCNT));
      if (s < limit) break;
      if (pend) { wait_vm<0>(); vst<unsigned>(pp, s - 1); LSTAMP(tlan, s - 2); pend = false; continue; }
      __builtin_amdgcn_s_sleep(LSLEEP);
      if (--budget < 0) break;
    }
    LSTAMP(tiss, s);
    const unsigned dst = smem_base + L_RING + (s & (NSLOT - 1)) * 16384u;
    const unsigned v0 = kind == 2u ? v_wide : v_raw, st = kind == 2u ? 7168u : 48128u;
    if (i >= PACEB && i < 18u && s >= 6u) {
      if (kind == 1u) { dma4_sc1(src[0], v_lin, dst); dma4_sc1(src[1], v_lin, dst + 4096); dma4_sc1(src[2], v_lin, dst + 8192); dma4_sc1(src[3], v_lin, dst + 12288); }
      else {
#pragma unroll
        for (int q = 0; q < 4; ++q) dma4p(src[q], v0, v0 + st, v0 + 2 * st, v0 + 3 * st, dst + q * 4096);
      }
    } else {
      if (kind == 1u) { dma4_sc1f(src[0], v_lin, dst); dma4_sc1f(src[1], v_lin, dst + 4096); dma4_sc1f(src[2], v_lin, dst + 8192); dma4_sc1f(src[3], v_lin, dst + 12288); }
      else {
#pragma unroll
        for (int q = 0; q < 4; ++q) dma4(src[q], v0, v0 + st, v0 + 2 * st, v0 + 3 * st, dst + q * 4096);
      }
    }
    if (pend) { wait_vm<16>(); vst<unsigned>(pp, s - 1); LSTAMP(tlan, s - 2); }
    LSTAMP(tis2, s);
    pend = true;
    if (s < 6u) {
      wait_vm<0>(); vst<unsigned>(pp, s + 1); LSTAMP(tlan, s); pend = false;
      __builtin_amdgcn_s_setprio(3);
      const unsigned a = tr_lane(lane) + s * 16384u;
      float c0 = 0.f, c1 = 0.f;
#pragma unroll
      for (int n = 0; n < 16; ++n) {
        const v4u f = __builtin_bit_cast(v4u, ldw(a + n * 1024, 512));
        c0 = dot2(f.x, ONES, c0); c1 = dot2(f.y, ONES, c1); c0 = dot2(f.z, ONES, c0); c1 = dot2(f.w, ONES, c1);
      }
      const float t = sum_halves(c0 + c1);
      vst<unsigned>(L_CSF + 2 * 512 + (lane & 31) * 16 + lw * 4, pk2(t, t - blo(pk2(t, t))));
      lds_add(pa, L_CSC - L_RDY + 4, 1);
      __builtin_amdgcn_s_setprio(LPRIO);
    }
    if (s + 2 >= NT) { wait_vm<0>(); vst<unsigned>(pp, s + 1); }
  }
}
DI void server(const Params& P, int p, int m, int sw) {
  const int lane = fresh_lane();
  unsigned epoch;
  asm volatile("s_load_dword %0, %1, 0x0" : "=s"(epoch) : "s"(P.ws) : "memory");
  const unsigned pa = lane == 0 ? L_RDY : L_DUMMY + lane * 4;
  const rsrc_t rs = __builtin_amdgcn_make_buffer_rsrc((void*)P.ws, 0, WS_END, 0x00020000);
  CW c;
  c.lane = lane; c.ws = P.ws; c.budget = BUDGET;
  asm volatile("s_waitcnt lgkmcnt(0)" : "+s"(epoch) :: "memory");
  c.tag = epoch + 1u;
  const int co = WS_CSP + (m * 2 + sw) * 16 * 768;
  STAMP(7);
  DRAIN();
  STAMP(10);
  lds_add(pa, L_RPD - L_RDY, 1);
  {
    {
      const unsigned b = blockIdx.x;
      const char* tb = sw ? P.wo : P.wd;
      const unsigned to = sw ? (b * 16u + (lane & 15)) * 128u : (b * 43u + min(lane, 42)) * 128u;
      asm volatile("s_mov_b32 m0, %2\n\ts_nop 3\n\tglobal_load_lds_dword %0, %1" :: "v"(to), "s"(uni(tb)), "s"((unsigned)(uintptr_t)(LAS char*)smem + L_DUMMY) : "memory");
    }
    while (rfl(vld<unsigned>(L_RPD)) < 2u && !spent(c)) __builtin_amdgcn_s_sleep(1);
  }
  for (unsigned layer = 0; layer < 2; ++layer) {
    __builtin_amdgcn_s_setprio(3);
    f32x4 tk[4], tz[4];
    for (;;) {
      const v4u fl = vld<v4u>(L_F1);
      if (min4(fl) > layer || spent(c)) break;
      __builtin_amdgcn_s_sleep(1);
    }
    STAMP(11 + 4 * layer);
#pragma unroll
    for (int u = 0; u < 4; ++u) { tk[u] = vld<f32x4>(L_KVS + u * 2048 + sw * 1024 + lane * 16); tz[u] = vld<f32x4>(L_ZS + u * 128 + (lane & 3) * 32 + sw * 16); }
    const int go = WS_KVP + ((layer * 16 + m) * 2 + sw) * 16 * KVW, fo = WS_FKV + ((layer * 16 + m) * 2 + sw) * 256;
    v4u sk, sz;
#pragma unroll
    for (int e = 0; e < 4; ++e) { sk[e] = f2u(fadd1(fadd1(tk[0][e], tk[1][e]), fadd1(tk[2][e], tk[3][e]))); sz[e] = f2u(fadd1(fadd1(tz[0][e], tz[1][e]), fadd1(tz[2][e], tz[3][e]))); }
    st16(rs, sk, lane * 16, go + p * KVW);
    st16(rs, sz, 1024 + (lane & 7) * 16, go + p * KVW);
    DRAIN();
    set_flag(c, fo + p * 4);
    STAMP(12 + 4 * layer);
    wait_flags(c, fo, 0xffffull);
    STAMP(13 + 4 * layer);
    const v4u zl = ld16(rs, 1024 + (lane & 15) * KVW + (lane >> 4) * 16, go);
    v4u g[16];
#pragma unroll
    for (int i = 0; i < 16; ++i) g[i] = ld16(rs, lane * 16, go + i * KVW);
    SCHED();
    const float z0 = row_sum(u2f(zl.x)), z1 = row_sum(u2f(zl.y)), z2 = row_sum(u2f(zl.z)), z3 = row_sum(u2f(zl.w));
    if ((lane & 15) == 0) vst<v2u>(L_ZR + (lane >> 4) * 16 + sw * 8, v2u{pk2(z0, z1), pk2(z2, z3)});
    f32x4 r = __builtin_bit_cast(f32x4, g[0]);
#pragma unroll
    for (int i = 1; i < 16; ++i) r += __builtin_bit_cast(f32x4, g[i]);
    vst<v2u>(L_KVR + lane * 16 + sw * 8, v2u{pk2(r.x, r.y), pk2(r.z, r.w)});
    lds_add(pa, L_F2 - L_RDY, 2);
    STAMP(14 + 4 * layer);
    __builtin_amdgcn_s_setprio(0);
    if (layer == 0) {
      v4u t[16];
#pragma unroll
      for (int i = 0; i < 16; ++i) t[i] = ld16(rs, min(lane, 47) * 16, co + i * 768);
      SCHED();
      f32x4 v = __builtin_bit_cast(f32x4, t[0]);
#pragma unroll
      for (int i = 1; i < 16; ++i) v += __builtin_bit_cast(f32x4, t[i]);
      if (lane < 48) {
#pragma unroll
        for (int e = 0; e < 4; ++e) vst<unsigned>(L_CSF + 2048 + (lane * 4 + e) * 16 + sw * 4, pk2(v[e], v[e] - blo(pk2(v[e], v[e]))));
      }
      lds_add(pa, L_CSC - L_RDY + 8, 2); lds_add(pa, L_CSC - L_RDY + 12, 2); lds_add(pa, L_CSC - L_RDY + 16, 2);
    }
  }
}
template <int AT, bool TWO> DI void cs_hook(CW& c, int i, unsigned k, v4u& a, v4u& b) {
  if (i == AT) a.x = vld<unsigned>(L_CSC + k * 4);
  if (i == AT + 3) {
    wait_count(c, a.x, L_CSC + k * 4, TWO ? 4u : 2u);
    a = vld<v4u>(L_CSF + k * 1024 + (c.lane & 31) * 16);
    if (TWO) b = vld<v4u>(L_CSF + k * 1024 + 512 + (c.lane & 31) * 16);
  }
}

__global__ void __launch_bounds__(512, 2) fused_fwd(Params P) {
  const int tid = threadIdx.x; int lane = tid & 63; const int wv = __builtin_amdgcn_readfirstlane(tid >> 6);
  const int b = blockIdx.x;
  {
    const unsigned co = CODE_BYTES - 256u - min(((b >> 3) * 8 + wv) * 256u, CODE_BYTES - 256u) + lane * 4;
    unsigned long long pc;
    asm volatile("s_mov_b32 m0, %2\n\ts_getpc_b64 %0\n\tglobal_load_lds_dword %1, %0" : "=&s"(pc) : "v"(co), "s"((unsigned)(uintptr_t)(LAS char*)smem + L_DUMMY) : "memory");
  }
  int p, m;
  if (MAPMODE == 0) { p = (b & 7) * 2 + (b >> 7); m = (b >> 3) & 15; }
  else if (MAPMODE == 1) { const int x = b & 7, i = b >> 3; p = (x >> 1) * 4 + (i >> 3); m = (x & 1) * 8 + (i & 7); }
  else { const int x = b & 7, i = b >> 3; p = i >> 1; m = x * 2 + (i & 1); }
  if (tid < (L_END - L_RDY) / 4) vst<unsigned>(L_RDY + tid * 4, tid == 4 || tid == 5 ? tid - 1u : 0u);
  if (tid < 320) vst<v4u>(L_CSF + tid * 16, v4u{0u, 0u, 0u, 0u});
  asm volatile("s_waitcnt lgkmcnt(0)\n\ts_barrier" ::: "memory");
  if (wv >= 4) __builtin_amdgcn_s_setprio(3);
  STAMP(21);
  const unsigned smem_base = (unsigned)(uintptr_t)(LAS char*)smem;
  int w = wv & 3;
  unsigned xv[4];
  const char* xbase = (wv < 4 ? P.x : P.wqkv) + (unsigned)sopaque(wv < 4 ? (4 * p + w) * 32768 : ((w >> 1) + 1) * 1024 + m * 64);
  auto xbuf = [&](int q) -> unsigned { const int j = q % 5; return (j == 0 ? L_SCR : j == 1 ? 7 * 16384 : j == 2 ? 6 * 16384 : j == 3 ? 4 * 16384 : 5 * 16384) + w * 4096; };
  {
#pragma unroll
    for (int k = 0; k < 4; ++k) {
      const unsigned a = (8 * k + (lane >> 3)) * 1024 + (((lane & 7) ^ ((4 * k + (lane >> 4)) & 7)) * 16) - k * 1024;
      const unsigned c = (lane >> 2) * 3072 + (lane & 3) * 16 + k * 48128;
      xv[k] = wv < 4 ? a : c;
    }
    const unsigned cw = wv < 4, stride = sopaque(cw ? 128 : 64 * 3072), r0 = ROT(m) + (cw ? 0 : (w & 1));
    dma4(xbase + (r0 & 7) * stride, xv[0], xv[1], xv[2], xv[3], smem_base + (cw ? xbuf(0) : w * 4096));
    if (!cw) {
      const unsigned pa = smem_base + (lane == 0 ? L_RDY : L_DUMMY + lane * 4), a = tr_lane(lane) + w * 4096u;
      float c0 = 0.f, c1 = 0.f;
      auto cs = [&](int j) __attribute__((always_inline)) {
#pragma unroll
        for (int n = 0; n < 4; ++n) {
          const v4u f = __builtin_bit_cast(v4u, ldw(a + j * 16384 + n * 1024, 512));
          c0 = dot2(f.x, ONES, c0); c1 = dot2(f.y, ONES, c1); c0 = dot2(f.z, ONES, c0); c1 = dot2(f.w, ONES, c1);
        }
        PIN(c0); PIN(c1);
      };
      STAMP(2);
      asm volatile("s_waitcnt vmcnt(0)\n\tds_add_u32 %0, %1" ::"v"(pa), "v"(1u) : "memory");
      STAMP(3);
      dma4(xbase + ((2 + r0) & 7) * stride, xv[0], xv[1], xv[2], xv[3], smem_base + 1 * 16384 + w * 4096);
      cs(0);
      asm volatile("s_waitcnt vmcnt(0)\n\tds_add_u32 %0, %1 offset:4" ::"v"(pa), "v"(1u) : "memory");
      STAMP(4);
      dma4(xbase + ((4 + r0) & 7) * stride, xv[0], xv[1], xv[2], xv[3], smem_base + 2 * 16384 + w * 4096);
      cs(1);
      asm volatile("s_waitcnt vmcnt(0)\n\tds_add_u32 %0, %1 offset:8" ::"v"(pa), "v"(1u) : "memory");
      STAMP(5);
      dma4(xbase + ((6 + r0) & 7) * stride, xv[0], xv[1], xv[2], xv[3], smem_base + 3 * 16384 + w * 4096);
      cs(2);
      asm volatile("s_waitcnt vmcnt(0)\n\tds_add_u32 %0, %1 offset:12" ::"v"(pa), "v"(1u) : "memory");
      STAMP(6);
      const int ks = 2 * p + (w & 1), r2 = GM(m) < 11 ? 2 : 1;
      v4u rp[9];
      if (w >= 2) {
        __builtin_amdgcn_s_setprio(RPP);
        const GAS char* sq = (const GAS char*)P.wqkv + m * 64 + ks * (16 * 3072) + xv[0];
        const unsigned vg = GM(m) * 64 + ks * (16 * 2752) + (lane >> 2) * 2752 + (lane & 3) * 16;
        const GAS char* sg = (const GAS char*)P.wg + vg; const GAS char* su = (const GAS char*)P.wu + vg;
        rp[0] = *(const GAS v4u*)(sg); rp[1] = *(const GAS v4u*)(su); rp[2] = *(const GAS v4u*)(sg + 1024); rp[3] = *(const GAS v4u*)(su + 1024);
        rp[4] = *(const GAS v4u*)(sg + r2 * 1024); rp[5] = *(const GAS v4u*)(su + r2 * 1024);
        rp[6] = *(const GAS v4u*)(sq + 1024); rp[7] = *(const GAS v4u*)(sq + 2048); rp[8] = *(const GAS v4u*)(sq);
        SCHED();
        __builtin_amdgcn_s_setprio(3);
      }
      cs(3);
      const float t = sum_halves(c0 + c1);
      vst<unsigned>(L_CSF + (w >> 1) * 512 + (lane & 31) * 16 + (w & 1) * 4, pk2(t, t - blo(pk2(t, t))));
      asm volatile("ds_add_u32 %0, %1 offset:%c2" ::"v"(pa), "v"(1u), "i"(L_CSC - L_RDY) : "memory");
      if (sopaque(w) >= 2) {
        const rsrc_t rs = __builtin_amdgcn_make_buffer_rsrc((void*)P.ws, 0, WS_END, 0x00020000);
        const int dg = (GM(m) * 32 + ks) * 1024, dq = WS_RQ + (m * 96 + ks) * 1024;
        const unsigned SCX = L_CSF + 3072 + (w & 1) * 1024;
        v4u f[8];
#pragma unroll
        for (int u = 0; u < 8; ++u) {
          vst<v4u>(SCX + lane * 16, rp[u]);
          CFENCE();
          f[u] = __builtin_bit_cast(v4u, ldw(SCX + tr_lane(lane), 512));
          CFENCE();
        }
        vst<v4u>(SCX + lane * 16, v4u{0u, 0u, 0u, 0u});
        st16(rs, f[0], lane * 16, WS_RG + dg); st16(rs, f[1], lane * 16, WS_RU + dg);
        st16(rs, f[2], lane * 16, WS_RG + dg + 16 * 32768); st16(rs, f[3], lane * 16, WS_RU + dg + 16 * 32768);
        st16(rs, f[4], lane * 16, WS_RG + dg + r2 * (16 * 32768)); st16(rs, f[5], lane * 16, WS_RU + dg + r2 * (16 * 32768));
        st16(rs, f[6], lane * 16, dq); st16(rs, f[7], lane * 16, dq + 32768); st16(rs, rp[8], lane * 16, dq + 65536);
        STAMP(8);
        float cs6[6];
#pragma unroll
        for (int u = 0; u < 6; ++u) cs6[u] = dot2(f[u].x, ONES, dot2(f[u].y, ONES, dot2(f[u].z, ONES, dot2(f[u].w, ONES, 0.f))));
#pragma unroll
        for (int u = 0; u < 3; ++u) {
          const auto r = __builtin_amdgcn_permlane32_swap(f2u(cs6[2 * u]), f2u(cs6[2 * u + 1]), false, false);
          __builtin_amdgcn_raw_buffer_store_b32(f2u(u2f(r[0]) + u2f(r[1])), rs, lane * 4 + u * 256, WS_CSP + ((m * 2 + (w & 1)) * 16 + p) * 768, 16);
        }
        STAMP(9);
      }
    }
  }
  STAMPV(0, t00);
  STAMP(61);
  if (sopaque(wv) >= 6) {
    server(P, p, m, wv - 6);
    STAMP(1); STAMP_END(); return; }
  if (sopaque(wv) >= 4) { loader(P, p, m, wv - 4); STAMP(1); STAMP_END(); return; }
  __builtin_amdgcn_s_setprio(2);

  unsigned epoch;
  asm volatile("s_load_dword %0, %1, 0x0" : "=s"(epoch) : "s"(P.ws) : "memory");
  v4u B[32];
  auto xdma = [&](int q) __attribute__((always_inline)) { dma4(xbase + ((q + ROT(m)) & 7) * 128, xv[0], xv[1], xv[2], xv[3], smem_base + xbuf(q)); };
  xdma(1);
  const rsrc_t rs = __builtin_amdgcn_make_buffer_rsrc((void*)P.ws, 0, WS_END, 0x00020000);
  asm volatile("s_waitcnt lgkmcnt(0)" : "+s"(epoch) :: "memory");
  STAMP(19);
  CW c;
  c.s = 0; c.pos = 0; c.perm = 4; c.lane_tr = tr_lane(lane); c.fa = lane == 0 ? L_FCNT + w * 4 : L_DUMMY + lane * 4;
  c.budget = BUDGET; c.lane = lane; c.w = w; c.ws = P.ws; c.tag = epoch + 1u;
  int h, t32, tbq, tbw, kh, cb;
#define FRESH() do { lane = opaque(lane); h = lane >> 5; t32 = lane & 31; c.lane = lane; p = sopaque(p); m = sopaque(m); w = sopaque(w); c.w = w; \
    tbq = layer * 64 + 4 * p + w; kh = w >> 1; tbw = layer * 64 + 4 * p + 2 * (m >> 3) + (w & 1); cb = 2 * (m & 7) + kh; } while (0)
  unsigned R[8];

  int layer = 0;
    f32x16 aK = {}, aV = {};
    float la; unsigned mf;
    f32x4 ar[4];
    v4u ck, cv;
    FRESH();
    auto rows_ab = [&]() __attribute__((always_inline)) {
      vst<float>(L_SA + w * 128 + t32 * 4, la);
#pragma unroll
      for (int g = 0; g < 4; ++g) ar[g] = vld<f32x4>(L_SA + w * 128 + (8 * g + 4 * h) * 4);
    };
    {
      float s1 = 0.f, s2 = 0.f, s3 = 0.f, s4 = 0.f;
      const unsigned xl = t32 * 128 + 8 * h, xf = (t32 >> 1) & 7;
      auto hook = [&](int i, int hf) __attribute__((always_inline)) {
        const int n = i + 2;
        if (n >= 0 && n < 32 && hf == 0) {
          const int q = n / 4, j = n % 4;
          if (j == 0) {
            if (q == 0) { STAMPB(39); wait_vm<4 * (XUP - 1)>(); STAMPB(40); }
            if (q == 1 || q == 2 || q == 3) wait_vm<12>();
            if (q == 4) wait_vm<12>();
            if (q == 5) wait_vm<8>();
            if (q == 6) wait_vm<4>();
            if (q == 7) wait_vm<0>();
          }
          const v2u lo = vld<v2u>(xbuf(q) + xl + (((2 * j) ^ xf) * 16)), hi = vld<v2u>(xbuf(q) + xl + (((2 * j + 1) ^ xf) * 16));
          B[n] = v4u{lo.x, lo.y, hi.x, hi.y};
        }
        if (hf == 1) {
          if (i == -1) xdma(2);
          if (i == 0) xdma(3);
          if (i == 1) xdma(4);
          if (i == 3 || i == 7 || i == 11) xdma(5 + i / 4);
          if (i >= 0 && i % 4 == 3) {
#pragma unroll
            for (int u = i - 3; u <= i; ++u) {
              const v4u f = B[u];
              s1 = dot2(f.x, ONES, s1); s2 = dot2(f.x, f.x, s2); s3 = dot2(f.y, ONES, s3); s4 = dot2(f.y, f.y, s4);
              s1 = dot2(f.z, ONES, s1); s2 = dot2(f.z, f.z, s2); s3 = dot2(f.w, ONES, s3); s4 = dot2(f.w, f.w, s4);
            }
            PIN(s1); PIN(s2); PIN(s3); PIN(s4);
          }
          cs_hook<26, true>(c, i, 0, ck, cv);
          if (n == 15 || n == 19) permit(c, n / 4 + 2);
          if (i == 30) permit(c, 10);
        }
      };
      gemm_pair<true>(c, B, aK, aV, hook);
      ln_factors(sum_halves(s1 + s3), sum_halves(s2 + s4), la, mf, h != 0);
      rows_ab();
    }
  for (; layer < 2; ++layer) {
    const bf16x8 mfr = asb(v4u{mf, mf, mf, mf});
    aK = mfma(mfr, asb(ck), aK);
    aV = mfma(mfr, asb(cv), aV);
    STAMPB(2 + 20 * layer);
    FRESH();
    {
      unsigned pkk[8], pkv[8];
#pragma unroll
      for (int j = 0; j < 8; ++j) {
        const f32x2 a = (j & 1) ? f32x2{ar[j >> 1].z, ar[j >> 1].w} : f32x2{ar[j >> 1].x, ar[j >> 1].y};
        pkk[j] = phi2(pair(aK, j), a);
        pkv[j] = pk2(pair(aV, j) * a);
      }
      const bf16x8 k0 = asb(v4u{pkk[0], pkk[1], pkk[2], pkk[3]}), k1 = asb(v4u{pkk[4], pkk[5], pkk[6], pkk[7]});
      const bf16x8 v0 = asb(v4u{pkv[0], pkv[1], pkv[2], pkv[3]}), v1 = asb(v4u{pkv[4], pkv[5], pkv[6], pkv[7]});
      const bf16x8 one = asb(v4u{ONES, ONES, ONES, ONES});
      f32x16 kv = {}, zz = {};
      kv = mfma(k0, v0, kv); zz = mfma(k0, one, zz); kv = mfma(k1, v1, kv); zz = mfma(k1, one, zz);
      const bool hd = (lane & 16) != 0;
      f32x4 q0, q1, z0, z1;
#pragma unroll
      for (int j = 0; j < 4; ++j) { q0[j] = sel(hd, kv[8 + j], kv[j]); q1[j] = sel(hd, kv[12 + j], kv[4 + j]); }
      vst<f32x4>(L_KVS + w * 2048 + lane * 16, q0);
      vst<f32x4>(L_KVS + w * 2048 + 1024 + lane * 16, q1);
#pragma unroll
      for (int j = 0; j < 4; ++j) { z0[j] = sel(hd, zz[8 + j], zz[j]); z1[j] = sel(hd, zz[12 + j], zz[4 + j]); }
      vst<f32x4>(L_ZS + w * 128 + (lane >> 4) * 32, z0);
      vst<f32x4>(L_ZS + w * 128 + (lane >> 4) * 32 + 16, z1);
      post(c, L_F1, layer + 1);
    }
    STAMPB(3 + 20 * layer);
    unsigned pq[8];
    {
      f32x16 aQ = {};
      v4u c0;
      auto hook = [&](int i) __attribute__((always_inline)) {
        if (i == 20) {
          if (layer == 0) {
            const GAS char* xr = (const GAS char*)P.x + (size_t)(tbw * 32 + t32) * 1024 + cb * 64 + h * 8;
#pragma unroll
            for (int g = 0; g < 4; ++g) { const v2u v = *(const GAS v2u*)(xr + g * 16); R[2 * g] = v.x; R[2 * g + 1] = v.y; }
          }
        }
        cs_hook<24, false>(c, i, 1, c0, c0);
      };
      gemm_single(c, B, aQ, hook);
      aQ = mfma(asb(c0), mfr, aQ);
#pragma unroll
      for (int j = 0; j < 8; ++j) { pq[j] = phi2(pair(aQ, j), f32x2{la, la}); PIN(pq[j]); }
    }
    STAMPB(4 + 20 * layer);
    {
      FRESH();
      f32x16 aM = {}, aO = {};
      Wide<16> d;
      const unsigned s0 = c.s;
      wide_steps<16, -16, 0>(c, d, B, aM, aO, kh, NoHook());
      STAMPB(54);
      v4u fk, fz;
      for (;;) {
        const unsigned fl = vld<unsigned>(L_F2);
        fk = vld<v4u>(L_KVR + lane * 16); fz = vld<v4u>(L_ZR + (lane >> 4) * 16);
        if (rfl(fl) >= 4u * layer + 4u || spent(c)) break;
      }
      STAMPB(5 + 20 * layer);
      permit(c, s0 + 8);
      const bool hd = (lane & 16) != 0;
      const v4u zero = {0u, 0u, 0u, 0u};
      const bf16x8 p0 = asb(v4u{pq[0], pq[1], pq[2], pq[3]}), p1 = asb(v4u{pq[4], pq[5], pq[6], pq[7]});
      f32x16 num = {}, den = {};
      den = mfma(asb(hd ? zero : fz), p0, den); den = mfma(asb(hd ? fz : zero), p1, den);
      num = mfma(asb(hd ? zero : fk), p0, num); num = mfma(asb(hd ? fk : zero), p1, num);
      const float i0 = __builtin_amdgcn_rcpf(den[0] + 1e-6f), i1 = __builtin_amdgcn_rcpf(den[8] + 1e-6f);
      const int os = WS_O + (tbq * 32 + 2 * m) * 1024;
      st16(rs, v4u{pk2(pair(num, 0) * i0), pk2(pair(num, 1) * i0), pk2(pair(num, 2) * i0), pk2(pair(num, 3) * i0)}, lane * 16, os);
      st16(rs, v4u{pk2(pair(num, 4) * i1), pk2(pair(num, 5) * i1), pk2(pair(num, 6) * i1), pk2(pair(num, 7) * i1)}, lane * 16 + 1024, os);
      aM = unpack(R); PIN(aM);
      int oo = WS_O + (tbw * 32 + 4 * kh) * 1024, xs = WS_X1 + (tbw * 32 + 2 * cb) * 1024, ss = WS_ST1 + (tbw * 16 + cb) * 256;
      SPIN(oo); SPIN(xs); SPIN(ss);
      DRAIN();
      set_flag(c, WS_FO + tbq * 256 + m * 4);
      STAMPB(6 + 20 * layer);
      wait_flags(c, WS_FO + tbw * 256, kh ? 0xccccull : 0x3333ull);
      STAMPB(7 + 20 * layer);
#pragma unroll
      for (int i = 0; i < AW; ++i) B[i] = ld16(rs, lane * 16 + (i & 3) * 1024, oo + (i >> 2) * 8192);
      SCHED();
      auto hook = [&](int i, int hf) __attribute__((always_inline)) {
        const int n = i + AW;
        if (hf == 0) return;
        if (n < 16) B[n] = ld16(rs, lane * 16 + (n & 3) * 1024, oo + (n >> 2) * 8192);
        if (n == 16) permit(c, s0 + 12);
      };
      wide_steps<16, 0, 16>(c, d, B, aM, aO, kh, hook);
      STAMPB(8 + 20 * layer);
      finish_wide<16>(c, d, B, aM, aO, R, 2 * layer + 1);
      publish_x(c, rs, R, xs, ss);
      vst<v4u>(L_KVS + w * 2048 + lane * 16, v4u{R[0], R[1], R[2], R[3]});
      vst<v4u>(L_KVS + w * 2048 + 1024 + lane * 16, v4u{R[4], R[5], R[6], R[7]});
    }
    unsigned pf;
    f32x16 aM = {}, aO = {};
    Wide<4> d;
    unsigned sd;
    int fd, hd0, fl2, xs2, ss2; unsigned long long mk;
    auto ldh = [&](int n) __attribute__((always_inline)) {
      if (n < 32) B[n % 16] = ld16(rs, lane * 16 + (n & 3) * 1024, hd0 + (((n >> 2) & 4) | (((n >> 2) + ROT(m)) & 3)) * 8192);
      else if (n < 40) B[n % 16] = ld16(rs, lane * 16 + (n & 3) * 1024, hd0 + (n >> 2) * 8192);
      else B[n % 16] = ld16(rs, lane * 16 + (n & 3) * 1024, hd0 + 81920 - kh * 1024);
    };
    {
      FRESH();
      f32x16 G0 = {}, U0 = {}, G1 = {}, U1 = {};
      Pipe q;
      const unsigned s0 = c.s;
      pair_steps<false, -PD, 0, 64, false>(c, q, B, G0, U0, NoHook());
      const int xo = WS_X1 + tbq * 32768;
      int xa = xo + (ROT(m) & 7) * 4096, xb = xo + ((ROT(m) + 1) & 7) * 4096;
      SPIN(xa); SPIN(xb);
      DRAIN();
      set_flag(c, WS_FX1 + tbw * 256 + cb * 4);
      STAMPB(9 + 20 * layer);
      wait_flags(c, WS_FX1 + tbq * 256, 0xffffull);
      STAMPB(10 + 20 * layer);
      Stats sv;
#pragma unroll
      for (int i = 0; i < 8; ++i) B[i] = ld16(rs, lane * 16 + (i & 3) * 1024, i < 4 ? xa : xb);
      SCHED();
      float a2; unsigned gm;
      v4u cg, cu;
      unsigned hp[8];
      const int ho = WS_H + (tbq * 86 + 2 * GM(m)) * 1024, fo = WS_FH + tbq * 256 + GM(m) * 4;
      float hv;
      fd = WS_FH + tbw * 256; mk = kh ? 0xccccull : 0x3333ull; hd0 = WS_H + (tbw * 86 + 4 * kh) * 1024;
      fl2 = fo + 128;
      auto piece = [&](int e, const f32x16& G, const f32x16& U) __attribute__((always_inline)) {
        float g;
        if (e & 1) asm volatile("v_max_f32 %1, 0, %2\n\tv_mul_f32 %0, %3, %4\n\tv_mul_f32 %1, %1, %0\n\tv_cvt_pk_bf16_f32 %0, %5, %1" : "=&v"(hp[e >> 1]), "=&v"(g) : "v"(G[e]), "v"(U[e]), "v"(a2), "v"(hv));
        else asm volatile("v_max_f32 %1, 0, %2\n\tv_mul_f32 %0, %3, %4\n\tv_mul_f32 %0, %1, %0" : "=&v"(hv), "=&v"(g) : "v"(G[e]), "v"(U[e]), "v"(a2));
      };
      auto tail = [&](const f32x16& G, const f32x16& U) __attribute__((always_inline)) {
#pragma unroll
        for (int j = 0; j < 8; ++j) {
          const f32x2 g = {fmaxf(G[2 * j], 0.f), fmaxf(G[2 * j + 1], 0.f)};
          hp[j] = pk2(g * (pair(U, j) * a2));
        }
      };
      auto store_h = [&](int r, int half) __attribute__((always_inline)) { st16(rs, v4u{hp[4 * half], hp[4 * half + 1], hp[4 * half + 2], hp[4 * half + 3]}, lane * 16 + half * 1024, ho + r * 32768); };
      auto correct = [&](f32x16& G, f32x16& U) __attribute__((always_inline)) {
        const bf16x8 f = asb(v4u{gm, gm, gm, gm});
        G = mfma(asb(cg), f, G); U = mfma(asb(cu), f, U);
      };
      auto shadow = [&](int i, int hf, int r, const f32x16& G, const f32x16& U) __attribute__((always_inline)) {
        constexpr int at[16] = {3, 5, 7, 9, 12, 13, 14, 15, 17, 19, 21, 23, 25, 28, 29, 30};
#pragma unroll
        for (int e = 0; e < 16; ++e) if (2 * i + hf == at[e]) piece(e, G, U);
        if (2 * i + hf == 31) store_h(r, 0);
        if (2 * i + hf == 33) store_h(r, 1);
        if (hf == 0 && i == 25) { if (r == 0) DRAIN(); else wait_vm<8>(); set_flag(c, fo + r * 64); }
        if (hf == 1) cs_hook<25, true>(c, i, 3 + r, cg, cu);
        if (r == 1) {
          if (hf == 0 && i == 8) pf = poll_issue(c, fd, mk);
          if (hf == 0 && i == 17) { poll_done<2>(c, pf, fd, mk); if (layer) STAMPB(51); }
          if (hf == 1 && i >= 17) ldh(i - 17);
          if (hf == 0 && i == 30) pf = poll_issue(c, fd, mk << 16);
        }
      };
      pair_steps<false, 0, 32, 64, false>(c, q, B, G0, U0, [&](int i, int hf) __attribute__((always_inline)) {
        const int n = i + AH;
        stats_hook(c, sv, 2 * i + hf, rs, WS_ST1 + tbq * 4096, a2, gm);
        if (hf == 0) return;
        if (n < 32) B[n] = ld16(rs, lane * 16 + (n & 3) * 1024, xo + (((n >> 2) + ROT(m)) & 7) * 4096);
        if (n == 32) permit(c, s0 + (GM(m) < 11 ? 23 : 19));
        if (i == 26) { a2 = fmul1(a2, a2); PIN(a2); }
        cs_hook<25, true>(c, i, 2, cg, cu);
      });
      correct(G0, U0);
      STAMPB(11 + 20 * layer);
      pair_steps<false, 32, 64, 96, false>(c, q, B, G1, U1, [&](int i, int hf) __attribute__((always_inline)) { shadow(i, hf, 0, G0, U0); });
      correct(G1, U1);
      STAMPB(12 + 20 * layer);
      pf = ~c.tag;
      if (sopaque(GM(m)) >= 11) {
        tail(G1, U1);
        store_h(1, 0); store_h(1, 1);
        DRAIN();
        set_flag(c, fo + 64);
        wait_flags(c, fd, mk);
      }
      if (sopaque(GM(m)) < 11) {
        G0 = f32x16{}; U0 = f32x16{};
        pair_steps<false, 64, 96, 96, false>(c, q, B, G0, U0, [&](int i, int hf) __attribute__((always_inline)) { shadow(i, hf, 1, G1, U1); });
        correct(G0, U0);
        tail(G0, U0);
        store_h(2, 0); store_h(2, 1);
        STAMPB(13 + 20 * layer);
      }
      if (sopaque(GM(m)) >= 11) {
#pragma unroll
        for (int i = 0; i < 15; ++i) ldh(i);
      }
      sd = c.s;
      const v4u r0 = vld<v4u>(L_KVS + w * 2048 + lane * 16), r1 = vld<v4u>(L_KVS + w * 2048 + 1024 + lane * 16);
      wide_steps<43, -4, 0>(c, d, B, aM, aO, kh, NoHook());
      R[0] = r0.x; R[1] = r0.y; R[2] = r0.z; R[3] = r0.w; R[4] = r1.x; R[5] = r1.y; R[6] = r1.z; R[7] = r1.w;
      aM = unpack(R);
    }
    STAMPB(14 + 20 * layer);
    {
      unsigned pf2;
      auto hook = [&](int i, int hf) __attribute__((always_inline)) {
        if (hf == 0) {
          if (i == FS) { wait_vm<FS>(); set_flag(c, fl2); }
          if (i == PI) pf2 = poll_issue(c, fd, 0x7ffull << 32);
          return;
        }
        if (i == 1) { poll_done<5>(c, pf, fd, mk << 16); if (layer) STAMPB(47); }
        if (i == 19 && layer) STAMPB(48);
        if (i == 31 && layer) STAMPB(50);
        if (i + 15 < 32) ldh(i + 15);
        if (i == PC) { poll_done<(PI < 17 ? 17 - PI : 0)>(c, pf2, fd, 0x7ffull << 32); if (layer) STAMPB(49); }
        if (PC == 20) {
          if (i >= 20 && i <= 23) { ldh(2 * i - 8); ldh(2 * i - 7); }
          if (i >= 24 && i <= 26) ldh(i + 16);
        } else {
          if (i >= PC && i < PC + 5) { ldh(32 + 2 * (i - PC)); ldh(33 + 2 * (i - PC)); }
          if (i == PC + 5) ldh(42);
        }
        if (i == PKVAT) permit(c, sd + 11 + PKV);
        if (i == 34) { xs2 = WS_X2 + (tbw * 32 + 2 * cb) * 1024; ss2 = WS_ST2 + (tbw * 16 + cb) * 256; SPIN(xs2); SPIN(ss2); }
      };
      wide_steps<43, 0, 43>(c, d, B, aM, aO, kh, hook);
      STAMPB(17 + 20 * layer);
      finish_wide<43>(c, d, B, aM, aO, R, 2 * layer + 2);
    }
#pragma unroll
    for (int i = 0; i < 32; ++i) asm volatile("" : "=v"(B[i]));
    asm volatile("" : "=v"(aK), "=v"(aV), "=v"(la), "=v"(mf), "=v"(ck), "=v"(cv));
    asm volatile("" : "=v"(ar[0]), "=v"(ar[1]), "=v"(ar[2]), "=v"(ar[3]));
    if (layer == 0) {
      publish_x(c, rs, R, xs2, ss2);
      FRESH();
      aK = f32x16{}; aV = f32x16{};
      Pipe q;
      const unsigned s0 = c.s;
      pair_steps<true, -PD, 0, 32, false>(c, q, B, aK, aV, NoHook());
      const int xo = WS_X2 + tbq * 32768;
      int xa = xo + (ROT(m) & 7) * 4096, xb = xo + ((ROT(m) + 1) & 7) * 4096;
      SPIN(xa); SPIN(xb);
      DRAIN();
      set_flag(c, WS_FX2 + tbw * 256 + cb * 4);
      STAMPB(18);
      wait_flags(c, WS_FX2 + tbq * 256, 0xffffull);
      STAMPB(20);
      Stats sv;
#pragma unroll
      for (int i = 0; i < 8; ++i) B[i] = ld16(rs, lane * 16 + (i & 3) * 1024, i < 4 ? xa : xb);
      SCHED();
      auto hook = [&](int i, int hf) __attribute__((always_inline)) {
        const int n = i + AH;
        stats_hook(c, sv, 2 * i + hf, rs, WS_ST2 + tbq * 4096, la, mf);
        if (hf == 0) {
          if (i == 25) vst<float>(L_SA + w * 128 + t32 * 4, la);
          if (i == 26) { ar[0] = vld<f32x4>(L_SA + w * 128 + 4 * h * 4); ar[1] = vld<f32x4>(L_SA + w * 128 + (8 + 4 * h) * 4); }
          if (i == 27) { ar[2] = vld<f32x4>(L_SA + w * 128 + (16 + 4 * h) * 4); ar[3] = vld<f32x4>(L_SA + w * 128 + (24 + 4 * h) * 4); }
          return;
        }
        if (n < 32) B[n] = ld16(rs, lane * 16 + (n & 3) * 1024, xo + (((n >> 2) + ROT(m)) & 7) * 4096);
        if (n == 32) permit(c, s0 + KVP2);
        cs_hook<26, true>(c, i, 0, ck, cv);
      };
      pair_steps<true, 0, 32, 32, false>(c, q, B, aK, aV, hook);
    }
  }
  {
    layer = 0;
    FRESH();
    GAS char* o = (GAS char*)P.out + (size_t)(tbw * 32 + t32) * 1024 + cb * 64 + h * 8;
#pragma unroll
    for (int g = 0; g < 4; ++g) *(GAS v2u*)(o + g * 16) = v2u{R[2 * g], R[2 * g + 1]};
  }
  STAMPB(38);
  STAMP_END();
  STAMPV(42, (unsigned long long)(BUDGET - c.budget));
  if (b == 0 && w == 0 && lane == 0) *(GAS unsigned*)(P.ws + WS_EPOCH) = c.tag;
}
}

extern "C" void kernel_launch(void* const* d_in, const int* in_sizes, int n_in, void* d_out, int out_size, void* d_ws,
                              size_t ws_size, hipStream_t stream) {
  Params P;
  P.x = (const char*)d_in[0]; P.wqkv = (const char*)d_in[1]; P.wo = (const char*)d_in[2];
  P.wg = (const char*)d_in[4]; P.wu = (const char*)d_in[5]; P.wd = (const char*)d_in[6];
  P.out = (char*)d_out; P.ws = (char*)d_ws + ((ws_size / 2) & ~(size_t)0x1fffff);
  (void)hipFuncSetAttribute((const void*)fused_fwd, hipFuncAttributeMaxDynamicSharedMemorySize, LDS_BYTES);
  fused_fwd<<<256, 512, LDS_BYTES, stream>>>(P);
}
```

```cpp
#include <hip/hip_runtime.h>
#include <stdint.h>

#define GAS __attribute__((address_space(1)))
#define LAS __attribute__((address_space(3)))
#define DI __device__ __forceinline__
typedef unsigned v4u __attribute__((ext_vector_type(4)));
typedef unsigned v2u __attribute__((ext_vector_type(2)));
typedef float f32x16 __attribute__((ext_vector_type(16)));
typedef float f32x4 __attribute__((ext_vector_type(4)));
typedef float f32x2 __attribute__((ext_vector_type(2)));
typedef __bf16 bf16x8 __attribute__((ext_vector_type(8)));
typedef __bf16 bf16x2 __attribute__((ext_vector_type(2)));
typedef short s16x4 __attribute__((ext_vector_type(4)));

#define MAPMODE 1
#define LDEPTH 8
#define LPRIO 0
#define LSLEEP 1
#define AW 8
#define PKV 8
#define THIN 2
#define PD 2
#define PKVAT 16
#define THINB 18
#define KVP2 14
#define PACEB 10
#define XUP 2
#define RPP 0
#define FS 7
#define PI 13
#define PC 20
#define VCL 400
#define AH 8

#define ROT(m) (((m) + 2 * p) & 7)
#define GM(m) ((((m) & 7) << 1) | ((m) >> 3))
static_assert(AH == 8, "");
namespace {
constexpr int KB = 1024;
constexpr int WS_EPOCH = 0;
constexpr int WS_VOTE = 1024;
constexpr unsigned WS_PROBE = 192 * KB;
constexpr int WS_FKV = 16 * KB;
constexpr int WS_FO = WS_FKV + 32 * KB;
constexpr int WS_FX1 = WS_FO + 32 * KB;
constexpr int WS_FH = WS_FX1 + 32 * KB;
constexpr int WS_FX2 = WS_FH + 32 * KB;
constexpr int WS_ST1 = 256 * KB;
constexpr int WS_ST2 = WS_ST1 + 512 * KB;
constexpr int KVW = 1152;
constexpr int WS_KVP = WS_ST2 + 256 * KB;
constexpr int WS_CSP = WS_KVP + 2 * 16 * 16 * 2 * KVW;
constexpr int WS_O = 4096 * KB;
constexpr int WS_X1 = WS_O + 4096 * KB;
constexpr int WS_X2 = WS_X1 + 4096 * KB;
constexpr int WS_H = WS_X2 + 2048 * KB;
constexpr int WS_RQ = WS_H + 16384 * KB;
constexpr int WS_RG = WS_RQ + 1536 * KB;
constexpr int WS_RU = WS_RG + 43 * 32 * KB;
constexpr int WS_DBG = WS_RU + 43 * 32 * KB;
constexpr int WS_END = WS_DBG + 256 * 8 * 64 * 8;
constexpr int NSLOT = 8;
constexpr int L_RING = 0;
constexpr int L_SCR = NSLOT * 16 * KB;
constexpr int L_KVS = L_SCR + 16 * KB;
constexpr int L_CSF = L_KVS + 8 * KB;
constexpr int L_SA = L_CSF + 5120;
constexpr int L_KVR = L_SA + 512;
constexpr int L_ZR = L_KVR + 1024;
constexpr int L_ZS = L_ZR + 64;
constexpr int L_DUMMY = L_ZS + 512;
constexpr int L_RDY = L_DUMMY + 448;
constexpr int L_PL = L_RDY + 16;
constexpr int L_CSC = L_RDY + 32;
constexpr int L_FCNT = L_CSC + 32;
constexpr int L_F1 = L_FCNT + 16;
constexpr int L_F2 = L_F1 + 16;
constexpr int L_F3 = L_F2 + 16;
constexpr int L_RPD = L_F3 + 16;
constexpr int L_CB = L_RPD + 4;
constexpr int L_END = L_RPD + 16;
static_assert(L_END <= 160 * KB, "LDS");
constexpr int LDS_BYTES = (L_END + 15) / 16 * 16;
constexpr unsigned CODE_BYTES = 42496;
constexpr unsigned BUDGET = 1u << 18;

struct Params {
  const char* x; const char* wqkv; const char* wo; const char* wg; const char* wu; const char* wd;
  char* out; char* ws;
};

extern __shared__ __attribute__((aligned(16))) char smem[];
template <class T> DI LAS T* lp(unsigned off) { return (LAS T*)((LAS char*)smem + off); }
template <class T> DI T vld(unsigned off) { return *(volatile LAS T*)((LAS char*)smem + off); }
template <class T> DI void vst(unsigned off, T v) { *(volatile LAS T*)((LAS char*)smem + off) = v; }
DI unsigned rfl(unsigned v) { return __builtin_amdgcn_readfirstlane(v); }
DI const char* uni(const char* p) { const unsigned long long v = (unsigned long long)p; return (const char*)(((unsigned long long)rfl((unsigned)(v >> 32)) << 32) | rfl((unsigned)v)); }
#define SCHED() __builtin_amdgcn_sched_barrier(0)
#define CFENCE() asm volatile("" ::: "memory")
#define DRAIN() asm volatile("s_waitcnt vmcnt(0)" ::: "memory")
#define PIN(x) asm volatile("" : "+v"(x))

#define STAMP(i) do {} while (0)
#define STAMPV(i, v) do {} while (0)
#define STAMPB(i) do {} while (0)
#define STAMP_END() do {} while (0)

DI bf16x8 ldw(unsigned a, int second) {
  s16x4 lo = __builtin_amdgcn_ds_read_tr16_b64_v4i16(lp<s16x4>(a));
  s16x4 hi = __builtin_amdgcn_ds_read_tr16_b64_v4i16(lp<s16x4>(a + second));
  return __builtin_bit_cast(bf16x8, __builtin_shufflevector(lo, hi, 0, 1, 2, 3, 4, 5, 6, 7));
}
DI f32x16 mfma(bf16x8 a, bf16x8 b, f32x16 c) { return __builtin_amdgcn_mfma_f32_32x32x16_bf16(a, b, c, 0, 0, 0); }
DI bf16x8 asb(v4u v) { return __builtin_bit_cast(bf16x8, v); }
DI unsigned pk2(float lo, float hi) { bf16x2 r = {(__bf16)lo, (__bf16)hi}; return __builtin_bit_cast(unsigned, r); }
DI unsigned pk2(f32x2 v) { return pk2(v.x, v.y); }
DI float dot2(unsigned a, unsigned b, float c) { return __builtin_amdgcn_fdot2_f32_bf16(__builtin_bit_cast(bf16x2, a), __builtin_bit_cast(bf16x2, b), c, false); }
DI float u2f(unsigned u) { return __builtin_bit_cast(float, u); }
DI unsigned f2u(float f) { return __builtin_bit_cast(unsigned, f); }
DI float sel(bool c, float a, float b) { asm volatile("" : "+v"(a), "+v"(b)); return c ? a : b; }
DI int opaque(int v) { asm volatile("" : "+v"(v)); return v; }
DI int sopaque(int v) { asm volatile("" : "+s"(v)); return v; }
DI float blo(unsigned u) { return u2f(u << 16); }
DI float bhi(unsigned u) { return u2f(u & 0xffff0000u); }
DI float sum_halves(float v) {
  auto r = __builtin_amdgcn_permlane32_swap(f2u(v), f2u(v), false, false);
  float o; asm("v_add_f32 %0, %1, %2" : "=v"(o) : "v"(r[0]), "v"(r[1])); return o;
}
template <int CTRL> DI float dpp_add(float v) { return v + u2f(__builtin_amdgcn_update_dpp(0u, f2u(v), CTRL, 0xf, 0xf, false)); }
DI float row_sum(float v) { v = dpp_add<0x128>(v); v = dpp_add<0x124>(v); v = dpp_add<0x122>(v); return dpp_add<0x121>(v); }
DI f32x2 pair(const f32x16& a, int j) { return f32x2{a[2 * j], a[2 * j + 1]}; }
DI unsigned phi2(f32x2 x, f32x2 a) {
  const f32x2 e2 = x * (a * 1.4426950408889634f);
  const f32x2 t = x * a + 1e-6f;
  const f32x2 e = {__builtin_amdgcn_fmed3f(__builtin_amdgcn_exp2f(e2.x), 0.f, 1.f), __builtin_amdgcn_fmed3f(__builtin_amdgcn_exp2f(e2.y), 0.f, 1.f)};
  const f32x2 mx = {fmaxf(t.x, 1e-6f), fmaxf(t.y, 1e-6f)};
  return pk2(mx + e);
}
constexpr unsigned ONES = 0x3f803f80u;
DI float fadd1(float a, float b) { float r; asm("v_add_f32 %0, %1, %2" : "=v"(r) : "v"(a), "v"(b)); return r; }
DI float fmul1(float a, float b) { float r; asm("v_mul_f32 %0, %1, %2" : "=v"(r) : "v"(a), "v"(b)); return r; }

typedef __amdgpu_buffer_rsrc_t rsrc_t;
DI v4u ld16(rsrc_t rs, int voff, int soff) { return __builtin_amdgcn_raw_buffer_load_b128(rs, voff, soff, 16); }
DI v2u ld8(rsrc_t rs, int voff, int soff) { return __builtin_amdgcn_raw_buffer_load_b64(rs, voff, soff, 16); }
DI void st16(rsrc_t rs, v4u v, int voff, int soff) {
  __builtin_amdgcn_raw_buffer_store_b128(v, rs, voff, soff, 16);
  asm volatile("s_nop 1" ::"v"(v) : "memory");
}
DI void st8(rsrc_t rs, v2u v, int voff, int soff) { __builtin_amdgcn_raw_buffer_store_b64(v, rs, voff, soff, 16); }

DI int near(int off, int sd) { return off + (((sd ^ (off >> 21)) & 1) << 13); }
DI int tbi(int t) { return (t >> 6) * 32 + (t & 31); }
DI int a_act(int region, int t, int sd) { return near(region + tbi(t) * 65536, sd); }
DI int a_hid(int t, int sd) { return near(WS_H + tbi(t) * 262144, sd); }
DI int a_flag(int region, int t, int sd) { return near(region + (t >> 6) * 16384 + (t & 31) * 256, sd); }
DI int a_stat(int region, int t, int sd) { return near(region + (tbi(t) >> 1) * 16384 + (t & 1) * 4096, sd); }
DI int fr(int f) { return (f >> 3) * 16384 + (f & 7) * 1024; }
struct CW {
  unsigned s, pos;
  unsigned lane_tr;
  unsigned fa;
  unsigned tag, perm;
  int budget;
  int lane, w;
  char* ws;
};
DI bool spent(CW& c) { c.budget -= 1; return c.budget < 0; }
DI unsigned min4(v4u v) { return rfl(min(min(v.x, v.y), min(v.z, v.w))); }
DI void wait_count(CW& c, unsigned have, unsigned addr, unsigned need) {
  if (__builtin_expect(rfl(have) < need, 0)) {
    for (;;) { if (rfl(vld<unsigned>(addr)) >= need || spent(c)) break; }
  }
}
DI void wait_slot(CW& c, unsigned have, unsigned t) {
  if (__builtin_expect(have < t, 0)) {
    for (;;) { const v2u v = vld<v2u>(L_PL); if (rfl(min(v.x, v.y)) >= t || spent(c)) break; }
  }
}
DI void post(CW& c, int word, unsigned v) { vst<unsigned>(c.fa + (word - L_FCNT), v); }
DI void release(CW& c) { c.s += 1; c.pos = (c.pos + 1) & (NSLOT - 1); post(c, L_FCNT, min(c.s + NSLOT, c.perm)); }
DI void permit(CW& c, unsigned v) { c.perm = v; post(c, L_FCNT, min(c.s + NSLOT, v)); }
DI void set_flag(CW& c, int off) {
  const char* base = uni(c.ws + off);
  asm volatile("s_mov_b64 exec, 1\n\ts_nop 4\n\tglobal_atomic_swap %0, %1, %2\n\ts_mov_b64 exec, -1" ::"v"(0u), "v"(c.tag), "s"(base) : "memory");
}
DI void wait_flags(CW& c, int off, unsigned long long mask) {
  unsigned f, bud = rfl(c.budget);
  const unsigned voff = c.lane * 4;
  const char* base = uni(c.ws + off);
  asm volatile(
      "s_mov_b64 exec, %5\n\ts_nop 4\n\t"
      "1:\n\t"
      "global_atomic_or %0, %2, %6, %3 sc0\n\t"
      "s_waitcnt vmcnt(0)\n\t"
      "v_cmp_ne_u32 vcc, %4, %0\n\t"
      "s_cbranch_vccz 2f\n\t"
      "s_sub_i32 %1, %1, 8\n\t"
      "s_cmp_ge_i32 %1, 0\n\t"
      "s_cbranch_scc1 1b\n\t"
      "2:\n\t"
      "s_mov_b64 exec, -1\n\t"
      : "=&v"(f), "+s"(bud) : "v"(voff), "s"(base), "s"(c.tag), "s"(mask), "v"(0u) : "memory", "vcc", "scc");
  c.budget = rfl(bud);
}
DI unsigned poll_issue(CW& c, int off, unsigned long long mask) {
  unsigned f = c.tag;
  const unsigned voff = c.lane * 4;
  const char* base = uni(c.ws + off);
  asm volatile("s_mov_b64 exec, %3\n\tv_not_b32 %0, %0\n\ts_nop 4\n\tglobal_atomic_or %0, %1, %4, %2 sc0\n\ts_mov_b64 exec, -1"
               : "+v"(f) : "v"(voff), "s"(base), "s"(mask), "v"(0u) : "memory");
  return f;
}
template <int N> DI void poll_done(CW& c, unsigned f, int off, unsigned long long mask) {
  asm volatile("s_waitcnt vmcnt(%c1)" : "+v"(f) : "i"(N) : "memory");
  if (__builtin_expect(__builtin_amdgcn_ballot_w64(f != c.tag) != 0ull, 0)) wait_flags(c, off, mask);
}

template <int V> struct IC { static constexpr int value = V; DI constexpr operator int() const { return V; } };
template <int I, int E, class F> DI void static_for(F&& f) { if constexpr (I < E) { f(IC<I>{}); static_for<I + 1, E>(f); } }
struct Pipe { bf16x8 wa[PD + 1], wb[PD + 1]; v2u pl; unsigned ad, ad2, adn, have, rel; };
#define SPIN(x) asm volatile("" : "+s"(x))
template <bool Q0> DI void slot_first(CW& c, Pipe& q) {
  if (Q0) wait_count(c, 0u, L_RDY, 4u); else wait_slot(c, 0u, c.s + 1);
  q.adn = c.lane_tr + c.pos * 16384u;
}
template <int K, bool NX, int CK, bool Q0 = false> DI void book(CW& c, Pipe& q) {
  if constexpr (CK && K == 0) { if (Q0) q.pl.x = vld<unsigned>(L_RDY + (c.s + 1) * 4); else q.pl = vld<v2u>(L_PL); }
  if constexpr (NX && K == 2) { q.adn = c.lane_tr + ((c.pos + 1) & (NSLOT - 1)) * 16384u; PIN(q.adn); }
  if constexpr (CK && K == 3) { q.have = rfl(Q0 ? q.pl.x : min(q.pl.x, q.pl.y)); SPIN(q.have); }
  if constexpr (CK && K == 4) { if (Q0) wait_count(c, q.have, L_RDY + (c.s + 1) * 4, 4u); else wait_slot(c, q.have, c.s + CK); }
  if constexpr (K == 5) { c.s += 1; c.pos = (c.pos + 1) & (NSLOT - 1); q.rel = min(c.s + NSLOT, c.perm); PIN(q.rel); }
  if constexpr (K == 6) post(c, L_FCNT, q.rel);
}
template <bool SWAP, int I0, int I1, int NEND, bool MORE, bool FR = true, class HOOK>
DI void pair_steps(CW& c, Pipe& q, v4u (&B)[32], f32x16& acc0, f32x16& acc1, HOOK hook) {
  const unsigned keep = c.lane_tr;
  if (FR) c.lane_tr = c.lane * 16;
  static_for<I0, I1>([&](auto ic) __attribute__((always_inline)) {
    constexpr int i = ic.value, n = i + PD;
    constexpr bool F = n < NEND, NX = F && (MORE || n + 8 - n % 8 < NEND);
    constexpr int CK = !NX ? 0 : !FR ? 1 : (n / 8) % 2 ? 2 : 0;
    if constexpr (n == 0) slot_first<!FR>(c, q);
    if constexpr (i >= 0) acc0 = SWAP ? mfma(asb(B[i % 32]), q.wa[i % (PD + 1)], acc0) : mfma(q.wa[i % (PD + 1)], asb(B[i % 32]), acc0);
    SCHED();
    if constexpr (F && n % 8 == 0) q.ad = q.adn;
    if constexpr (F) q.wa[n % (PD + 1)] = FR ? asb(vld<v4u>(q.ad + (n % 8) * 1024)) : ldw(q.ad + (n % 8) * 1024, 512);
    if constexpr (F && n % 8 >= 2) book<n % 8 - 2, NX, CK, !FR>(c, q);
    hook(IC<(i < 0 ? i : i % 32)>{}, IC<0>{});
    SCHED();
    if constexpr (i >= 0) acc1 = SWAP ? mfma(asb(B[i % 32]), q.wb[i % (PD + 1)], acc1) : mfma(q.wb[i % (PD + 1)], asb(B[i % 32]), acc1);
    SCHED();
    if constexpr (F) q.wb[n % (PD + 1)] = FR ? asb(vld<v4u>(q.ad + 8192 + (n % 8) * 1024)) : ldw(q.ad + 8192 + (n % 8) * 1024, 512);
    if constexpr (F && n % 8 == 7) book<6, NX, CK>(c, q);
    hook(IC<(i < 0 ? i : i % 32)>{}, IC<1>{});
    SCHED();
  });
  c.lane_tr = keep;
}
template <bool SWAP, class HOOK>
DI void gemm_pair(CW& c, v4u (&B)[32], f32x16& acc0, f32x16& acc1, HOOK hook) {
  Pipe q;
  pair_steps<SWAP, -PD, 32, 32, false, false>(c, q, B, acc0, acc1, hook);
}
template <class HOOK>
DI void gemm_single(CW& c, v4u (&B)[32], f32x16& acc, HOOK hook) {
  bf16x8 wa[5];
  Pipe q;
  static_for<-4, 32>([&](auto ic) __attribute__((always_inline)) {
    constexpr int i = ic.value, n = i + 4;
    if constexpr (n == 0) { wait_slot(c, 0u, c.s); q.adn = c.lane_tr + c.pos * 16384u; }
    if constexpr (i >= 0) acc = mfma(wa[i % 5], asb(B[i]), acc);
    SCHED();
    if constexpr (n < 32) {
      if constexpr (n % 16 == 0) q.ad = q.adn;
      wa[n % 5] = ldw(q.ad + (n % 16) * 1024, 512);
      if constexpr (n % 16 == 6) book<0, (n < 16), (n < 16)>(c, q);
      if constexpr (n % 16 >= 10) book<n % 16 - 9, (n < 16), (n < 16)>(c, q);
    }
    hook(ic);
    SCHED();
  });
}
constexpr int TL = 6;
template <int PW> struct Wide { bf16x8 wa[PW + 1 + TL], wb[PW + 1]; Pipe q; unsigned dO, lt; };
template <int NK, int I0, int I1, int PW, class HOOK>
DI void wide_steps(CW& c, Wide<PW>& d, v4u (&B)[32], f32x16& accM, f32x16& accO, int kh, HOOK hook) {
  Pipe& q = d.q;
  const unsigned keep = c.lane_tr;
  if constexpr (I0 == -PW) { d.lt = c.lane_tr + kh * (8192u + 512u); d.dO = kh ? -512u : 512u; }
  c.lane_tr = d.lt;
  static_for<I0, I1>([&](auto ic) __attribute__((always_inline)) {
    constexpr int i = ic.value, n = i + PW;
    constexpr bool F = n < NK, NX = F && n + 4 - n % 4 < NK;
    constexpr int CK = !NX || (n / 4) % 2 == 0 ? 0 : n + 8 - n % 4 < NK ? 2 : 1;
    if constexpr (n == 0) slot_first<false>(c, q);
    if constexpr (i >= 0) accO = mfma(d.wb[i % (PW + 1)], asb(B[i % 16]), accO);
    SCHED();
    if constexpr (F && n % 4 == 0) { q.ad = q.adn; if constexpr (n + 3 == NK) q.ad -= kh * 2048u; q.ad2 = q.ad + d.dO; }
    if constexpr (F) d.wb[n % (PW + 1)] = ldw(q.ad2 + (n % 4) * 2048, 1024);
    if constexpr (F && n % 4 == 1) { book<2, NX, CK>(c, q); }
    if constexpr (F && n % 4 == 2) { book<3, NX, CK>(c, q); }
    if constexpr (F && (n % 4 == 3 || n == NK - 1)) { book<5, NX, CK>(c, q); }
    hook(ic, IC<0>{});
    SCHED();
    if constexpr (i >= 0 && i < NK - TL) accM = mfma(d.wa[i % (PW + 1 + TL)], asb(B[i % 16]), accM);
    SCHED();
    if constexpr (F) d.wa[n % (PW + 1 + TL)] = ldw(q.ad + (n % 4) * 2048, 1024);
    if constexpr (F && n % 4 == 0) book<0, NX, CK>(c, q);
    if constexpr (F && n % 4 == 2) book<4, NX, CK>(c, q);
    if constexpr (F && (n % 4 == 3 || n == NK - 1)) book<6, NX, CK>(c, q);
    hook(ic, IC<1>{});
    SCHED();
  });
  c.lane_tr = keep;
}
struct NoHook { template <class A, class B> DI void operator()(A, B) const {} };

DI void ln_factors(float sum, float sq, float& a, unsigned& mf, bool up) {
  float mean = sum * (1.0f / 512.0f);
  PIN(mean);
  const float var = fmaxf(sq * (1.0f / 512.0f) - mean * mean, 0.f);
  a = __builtin_amdgcn_rsqf(var + 1e-5f);
  const unsigned hi = pk2(-mean, -mean);
  const float lo = -mean - blo(hi);
  mf = up ? pk2(lo, lo) : hi;
}
struct Stats { v4u sv[4]; f32x4 t; f32x2 u[4]; float mean, var; };
constexpr int K0 = 34;
DI void stats_hook(CW& c, Stats& t, int k, rsrc_t rs, int soff, float& a, unsigned& mf) {
  if (k == 3 || k == 7 || k == 11 || k == 15) t.sv[k >> 2] = ld16(rs, c.lane * 16 + (k >> 2) * 1024, soff);
  const unsigned sc = L_SCR + (c.w ^ 2) * 4096;
  if (k >= K0 && k < K0 + 4) { const int e = k - K0; t.t[e] = fadd1(fadd1(u2f(t.sv[0][e]), u2f(t.sv[1][e])), fadd1(u2f(t.sv[2][e]), u2f(t.sv[3][e]))); PIN(t.t[e]); }
  if (k == K0 + 4) vst<f32x4>(sc + c.lane * 16, t.t);
  if (k == K0 + 5) { t.u[0] = vld<f32x2>(sc + (c.lane & 31) * 8); t.u[1] = vld<f32x2>(sc + 256 + (c.lane & 31) * 8); }
  if (k == K0 + 6) { t.u[2] = vld<f32x2>(sc + 512 + (c.lane & 31) * 8); t.u[3] = vld<f32x2>(sc + 768 + (c.lane & 31) * 8); }
  if (k == K0 + 10) { t.mean = fadd1(fadd1(t.u[0].x, t.u[1].x), fadd1(t.u[2].x, t.u[3].x)); PIN(t.mean); }
  if (k == K0 + 11) { t.var = fadd1(fadd1(t.u[0].y, t.u[1].y), fadd1(t.u[2].y, t.u[3].y)); PIN(t.var); }
  if (k == K0 + 12) { t.mean *= -1.0f / 512.0f; PIN(t.mean); t.var = t.var * (1.0f / 512.0f) + 1e-5f; PIN(t.var); }
  if (k == K0 + 13) { t.var = fmaxf(t.var - t.mean * t.mean, 1e-5f); PIN(t.var); a = __builtin_amdgcn_rsqf(t.var); PIN(a); }
  if (k == K0 + 14) { mf = pk2(t.mean, t.mean); t.mean -= blo(mf); PIN(mf); PIN(t.mean); }
  if (k == K0 + 15) { mf = c.lane >= 32 ? pk2(t.mean, t.mean) : mf; PIN(mf); }
}
DI f32x16 unpack(const unsigned (&R)[8]) {
  f32x16 a;
#pragma unroll
  for (int j = 0; j < 8; ++j) { a[2 * j] = blo(R[j]); a[2 * j + 1] = bhi(R[j]); }
  return a;
}
template <int NK, int PW>
DI void finish_wide(CW& c, Wide<PW>& d, v4u (&B)[32], f32x16& accM, const f32x16& accO, unsigned (&R)[8], unsigned seq) {
  const unsigned mine = L_SCR + c.w * 4096 + c.lane * 16, other = L_SCR + (c.w ^ 2) * 4096 + c.lane * 16;
  auto step = [&](int k) __attribute__((always_inline)) { accM = mfma(d.wa[(NK - TL + k) % (PW + 1 + TL)], asb(B[(NK - TL + k) % 16]), accM); SCHED(); };
  auto put = [&](int g) __attribute__((always_inline)) { vst<f32x4>(mine + g * 1024, f32x4{accO[4 * g], accO[4 * g + 1], accO[4 * g + 2], accO[4 * g + 3]}); };
  step(0); step(1);
  put(0); put(1); SCHED();
  step(2);
  put(2); put(3); post(c, L_F3, seq); SCHED();
  step(3); step(4);
  unsigned fl = vld<unsigned>(L_F3 + (c.w ^ 2) * 4);
  f32x4 v[4];
#pragma unroll
  for (int g = 0; g < 4; ++g) v[g] = vld<f32x4>(other + g * 1024);
  SCHED();
  step(5);
  if (rfl(fl) < seq) {
    for (;;) {
      fl = vld<unsigned>(L_F3 + (c.w ^ 2) * 4);
#pragma unroll
      for (int g = 0; g < 4; ++g) v[g] = vld<f32x4>(other + g * 1024);
      if (rfl(fl) >= seq || spent(c)) break;
    }
  }
#pragma unroll
  for (int g = 0; g < 4; ++g) {
    R[2 * g] = pk2(pair(accM, 2 * g) + f32x2{v[g].x, v[g].y});
    R[2 * g + 1] = pk2(pair(accM, 2 * g + 1) + f32x2{v[g].z, v[g].w});
  }
}
DI void publish_x(CW& c, rsrc_t rs, const unsigned (&R)[8], int xoff, int stoff) {
  st16(rs, v4u{R[0], R[1], R[2], R[3]}, c.lane * 16, xoff);
  st16(rs, v4u{R[4], R[5], R[6], R[7]}, c.lane * 16 + 1024, xoff);
  SCHED();
  float s0 = 0.f, q0 = 0.f, s1 = 0.f, q1 = 0.f;
#pragma unroll
  for (int i = 0; i < 4; ++i) { s0 = dot2(R[i], ONES, s0); q0 = dot2(R[i], R[i], q0); s1 = dot2(R[4 + i], ONES, s1); q1 = dot2(R[4 + i], R[4 + i], q1); }
  const float s = sum_halves(s0 + s1), q = sum_halves(q0 + q1);
  st8(rs, v2u{f2u(s), f2u(q)}, (c.lane & 31) * 8, stoff);
}

DI void dma4(const char* base, unsigned v0, unsigned v1, unsigned v2, unsigned v3, unsigned dst) {
  asm volatile("s_mov_b32 m0, %5\n\ts_nop 3\n\t"
               "global_load_lds_dwordx4 %0, %4\n\tglobal_load_lds_dwordx4 %1, %4 offset:1024\n\t"
               "global_load_lds_dwordx4 %2, %4 offset:2048\n\tglobal_load_lds_dwordx4 %3, %4 offset:3072"
               ::"v"(v0), "v"(v1), "v"(v2), "v"(v3), "s"(uni(base)), "s"(rfl(dst)) : "memory");
}
#define PACE "s_sleep 1\n\t"
DI void dma4p(const char* base, unsigned v0, unsigned v1, unsigned v2, unsigned v3, unsigned dst) {
  asm volatile("s_mov_b32 m0, %5\n\ts_nop 3\n\t"
               "global_load_lds_dwordx4 %0, %4\n\t" PACE "global_load_lds_dwordx4 %1, %4 offset:1024\n\t" PACE
               "global_load_lds_dwordx4 %2, %4 offset:2048\n\t" PACE "global_load_lds_dwordx4 %3, %4 offset:3072\n\t" PACE
               ::"v"(v0), "v"(v1), "v"(v2), "v"(v3), "s"(uni(base)), "s"(rfl(dst)) : "memory");
}
DI void dma4_sc1(const char* base, unsigned v, unsigned dst) {
  asm volatile("s_mov_b32 m0, %2\n\ts_nop 3\n\t"
               "global_load_lds_dwordx4 %0, %1 sc1\n\t" PACE "global_load_lds_dwordx4 %0, %1 offset:1024 sc1\n\t" PACE
               "global_load_lds_dwordx4 %0, %1 offset:2048 sc1\n\t" PACE "global_load_lds_dwordx4 %0, %1 offset:3072 sc1\n\t" PACE
               ::"v"(v), "s"(uni(base)), "s"(rfl(dst)) : "memory");
}
DI void dma4_sc1f(const char* base, unsigned v, unsigned dst) {
  asm volatile("s_mov_b32 m0, %2\n\ts_nop 3\n\t"
               "global_load_lds_dwordx4 %0, %1 sc1\n\tglobal_load_lds_dwordx4 %0, %1 offset:1024 sc1\n\t"
               "global_load_lds_dwordx4 %0, %1 offset:2048 sc1\n\tglobal_load_lds_dwordx4 %0, %1 offset:3072 sc1"
               ::"v"(v), "s"(uni(base)), "s"(rfl(dst)) : "memory");
}
template <int N> DI void wait_vm() { asm volatile("s_waitcnt vmcnt(%c0)" ::"i"(N) : "memory"); }
DI unsigned tr_lane(int lane) { return (4 * (lane >> 5) + ((lane & 15) >> 2)) * 64 + ((lane >> 4) & 1) * 32 + (lane & 3) * 8; }
DI int fresh_lane() { int l; asm volatile("v_mbcnt_lo_u32_b32 %0, -1, 0\n\tv_mbcnt_hi_u32_b32 %0, -1, %0" : "=v"(l)); return l; }
#define LSTAMP(v, s) do {} while (0)
DI void lds_add(unsigned pa, unsigned off, unsigned n) { CFENCE(); (void)__hip_atomic_fetch_add(lp<unsigned>(pa + off), n, __ATOMIC_RELAXED, __HIP_MEMORY_SCOPE_WORKGROUP); CFENCE(); }
DI void loader(const Params& P, int p, int m, int lw) {
  const int lane = fresh_lane();
  const unsigned smem_base = (unsigned)(uintptr_t)(LAS char*)smem;
  const unsigned nr = GM(m) < 11 ? 3 : 2, NS = 21 + 4 * nr, NT = 2 * NS;
  const unsigned v_lin = lane * 16, v_wide = ((lane & 31) >> 2) * 1024 + (lane >> 5) * 64 + (lane & 3) * 16, v_raw = (lane >> 2) * 3072 + (lane & 3) * 16;
  const unsigned pa = lane == 0 ? L_RDY : L_DUMMY + lane * 4;
  const unsigned pp = pa + (L_PL - L_RDY) + lw * 4;
  if (blockIdx.x < 16) {
    const unsigned vi = blockIdx.x * 2 + lw, first = lw * 8192u, poff = WS_PROBE + vi * 128u;
    unsigned long long t0, t1, t2; unsigned j0, j1, epoch;
    asm volatile("s_load_dword %5, %8, 0x0\n\ts_load_dword %3, %6, 0x1000\n\ts_waitcnt lgkmcnt(0)\n\t"
                 "s_memtime %0\n\ts_waitcnt lgkmcnt(0)\n\ts_load_dword %3, %6, 0x0\n\ts_waitcnt lgkmcnt(0)\n\ts_memtime %1\n\ts_waitcnt lgkmcnt(0)\n\t"
                 "s_load_dword %4, %7, 0x0\n\ts_waitcnt lgkmcnt(0)\n\ts_memtime %2\n\ts_waitcnt lgkmcnt(0)"
                 : "=&s"(t0), "=&s"(t1), "=&s"(t2), "=&s"(j0), "=&s"(j1), "=&s"(epoch)
                 : "s"(uni(P.ws + poff + first)), "s"(uni(P.ws + poff + (first ^ 8192u))), "s"(uni(P.ws)) : "memory");
    int d = min(max((int)((unsigned)(t1 - t0) - (unsigned)(t2 - t1)), -VCL), VCL);
    if ((lw ^ (p >> 3) ^ (poff >> 21)) & 1) d = -d;
    st8(__builtin_amdgcn_make_buffer_rsrc((void*)P.ws, 0, WS_END, 0x00020000), v2u{(unsigned)d, epoch + 1u}, 0, WS_VOTE + vi * 8);
  }
  __builtin_amdgcn_s_setprio(LPRIO);
  unsigned limit = 0;
  int budget = BUDGET;
  bool pend = false;
  auto entry = [&](unsigned s) __attribute__((always_inline)) -> unsigned long long {
    const bool l2 = s >= NS;
    const unsigned q = lane & 3, i = l2 ? s - NS : s, g = i - 10u, d = g - 4u * nr, r4 = 4u * ROT(m);
    const unsigned blk = i < 4u ? q >> 1 : 2u, ks0 = ((i < 4u ? 8u * i + (q & 1) * 4u : 16u * (i - 4u) + q * 4u) + r4) & 31u;
    if (i < 6u && l2) return (unsigned long long)P.ws + WS_RQ + ((m * 3 + blk) * 32u + ks0) * 1024u + 1u;
    if (i < 6u) return (unsigned long long)P.wqkv + m * 64 + ks0 * (16u * 3072u);
    if (i < 10u) return (unsigned long long)P.wo + ((i - 6u) * 128u + q * 32u) * 1024u + (m & 7) * 128 + 2u;
    if (g < 4u * nr) return (unsigned long long)P.ws + ((q >> 1) ? WS_RU : WS_RG) + ((16u * (g >> 2) + GM(m)) * 32u + ((8u * (g & 3u) + (q & 1) * 4u + r4) & 31u)) * 1024u + 1u;
    return (unsigned long long)P.wd + min((d < 8u ? (d & 4u) | ((d + ROT(m)) & 3u) : d) * 128u + q * 32u, 1344u) * 1024u + (m & 7) * 128 + 2u;
  };
  unsigned long long tab = entry(4u + lw + 2u * (lane >> 2));
  const unsigned long long tab2 = entry(36u + lw + 2u * (lane >> 2));
  for (unsigned s = 4 + lw, t = 0; s < NT; s += 2, t = (t + 4) & 63u) {
    const unsigned i = s >= NS ? s - NS : s;
    if (s >= 36u) tab = tab2;
    const char* src[4];
#pragma unroll
    for (int q = 0; q < 4; ++q)
      src[q] = (const char*)(((unsigned long long)__builtin_amdgcn_readlane((unsigned)(tab >> 32), t + q) << 32) | (__builtin_amdgcn_readlane((unsigned)tab, t + q) & ~3u));
    const unsigned kind = __builtin_amdgcn_readlane((unsigned)tab, t) & 3u;
    if (pend && i < THINB) { wait_vm<0>(); vst<unsigned>(pp, s - 1); LSTAMP(tlan, s - 2); pend = false; }
    while (s >= limit) {
      limit = min4(vld<v4u>(L_FCNT));
      if (s < limit) break;
      if (pend) { wait_vm<0>(); vst<unsigned>(pp, s - 1); LSTAMP(tlan, s - 2); pend = false; continue; }
      __builtin_amdgcn_s_sleep(LSLEEP);
      if (--budget < 0) break;
    }
    LSTAMP(tiss, s);
    const unsigned dst = smem_base + L_RING + (s & (NSLOT - 1)) * 16384u;
    const unsigned v0 = kind == 2u ? v_wide : v_raw, st = kind == 2u ? 7168u : 48128u;
    if (i >= PACEB && i < 18u && s >= 6u) {
      if (kind == 1u) { dma4_sc1(src[0], v_lin, dst); dma4_sc1(src[1], v_lin, dst + 4096); dma4_sc1(src[2], v_lin, dst + 8192); dma4_sc1(src[3], v_lin, dst + 12288); }
      else {
#pragma unroll
        for (int q = 0; q < 4; ++q) dma4p(src[q], v0, v0 + st, v0 + 2 * st, v0 + 3 * st, dst + q * 4096);
      }
    } else {
      if (kind == 1u) { dma4_sc1f(src[0], v_lin, dst); dma4_sc1f(src[1], v_lin, dst + 4096); dma4_sc1f(src[2], v_lin, dst + 8192); dma4_sc1f(src[3], v_lin, dst + 12288); }
      else {
#pragma unroll
        for (int q = 0; q < 4; ++q) dma4(src[q], v0, v0 + st, v0 + 2 * st, v0 + 3 * st, dst + q * 4096);
      }
    }
    if (pend) { wait_vm<16>(); vst<unsigned>(pp, s - 1); LSTAMP(tlan, s - 2); }
    LSTAMP(tis2, s);
    pend = true;
    if (s < 6u) {
      wait_vm<0>(); vst<unsigned>(pp, s + 1); LSTAMP(tlan, s); pend = false;
      __builtin_amdgcn_s_setprio(3);
      const unsigned a = tr_lane(lane) + s * 16384u;
      float c0 = 0.f, c1 = 0.f;
#pragma unroll
      for (int n = 0; n < 16; ++n) {
        const v4u f = __builtin_bit_cast(v4u, ldw(a + n * 1024, 512));
        c0 = dot2(f.x, ONES, c0); c1 = dot2(f.y, ONES, c1); c0 = dot2(f.z, ONES, c0); c1 = dot2(f.w, ONES, c1);
      }
      const float t = sum_halves(c0 + c1);
      vst<unsigned>(L_CSF + 2 * 512 + (lane & 31) * 16 + lw * 4, pk2(t, t - blo(pk2(t, t))));
      lds_add(pa, L_CSC - L_RDY + 4, 1);
      __builtin_amdgcn_s_setprio(LPRIO);
    }
    if (s + 2 >= NT) { wait_vm<0>(); vst<unsigned>(pp, s + 1); }
  }
}
DI void server(const Params& P, int p, int m, int sw) {
  const int lane = fresh_lane();
  unsigned epoch;
  asm volatile("s_load_dword %0, %1, 0x0" : "=s"(epoch) : "s"(P.ws) : "memory");
  const unsigned pa = lane == 0 ? L_RDY : L_DUMMY + lane * 4;
  const rsrc_t rs = __builtin_amdgcn_make_buffer_rsrc((void*)P.ws, 0, WS_END, 0x00020000);
  CW c;
  c.lane = lane; c.ws = P.ws; c.budget = BUDGET;
  asm volatile("s_waitcnt lgkmcnt(0)" : "+s"(epoch) :: "memory");
  c.tag = epoch + 1u;
  const int co = WS_CSP + (m * 2 + sw) * 16 * 768;
  STAMP(7);
  DRAIN();
  STAMP(10);
  lds_add(pa, L_RPD - L_RDY, 1);
  {
    const unsigned idx = (blockIdx.x * 2 + sw) * 4 + (lane & 3), line = (idx / 3u) % 576u;
    asm volatile("s_mov_b64 exec, 0xf\n\ts_nop 4\n\tglobal_atomic_or %0, %1, %2\n\ts_mov_b64 exec, -1" ::"v"(WS_FKV + line * 256u + (idx % 3u) * 64u), "v"(0u), "s"(uni(P.ws)) : "memory");
  }
  {
    {
      const unsigned b = blockIdx.x;
      const char* tb = sw ? P.wo : P.wd;
      const unsigned to = sw ? (b * 16u + (lane & 15)) * 128u : (b * 43u + min(lane, 42)) * 128u;
      asm volatile("s_mov_b32 m0, %2\n\ts_nop 3\n\tglobal_load_lds_dword %0, %1" :: "v"(to), "s"(uni(tb)), "s"((unsigned)(uintptr_t)(LAS char*)smem + L_DUMMY) : "memory");
    }
    while (rfl(vld<unsigned>(L_RPD)) < 2u && !spent(c)) __builtin_amdgcn_s_sleep(1);
  }
  for (unsigned layer = 0; layer < 2; ++layer) {
    __builtin_amdgcn_s_setprio(3);
    f32x4 tk[4], tz[4];
    for (;;) {
      const v4u fl = vld<v4u>(L_F1);
      if (min4(fl) > layer || spent(c)) break;
      __builtin_amdgcn_s_sleep(1);
    }
    STAMP(11 + 4 * layer);
#pragma unroll
    for (int u = 0; u < 4; ++u) { tk[u] = vld<f32x4>(L_KVS + u * 2048 + sw * 1024 + lane * 16); tz[u] = vld<f32x4>(L_ZS + u * 128 + (lane & 3) * 32 + sw * 16); }
    const int go = WS_KVP + ((layer * 16 + m) * 2 + sw) * 16 * KVW, fo = WS_FKV + ((layer * 16 + m) * 2 + sw) * 256;
    v4u sk, sz;
#pragma unroll
    for (int e = 0; e < 4; ++e) { sk[e] = f2u(fadd1(fadd1(tk[0][e], tk[1][e]), fadd1(tk[2][e], tk[3][e]))); sz[e] = f2u(fadd1(fadd1(tz[0][e], tz[1][e]), fadd1(tz[2][e], tz[3][e]))); }
    st16(rs, sk, lane * 16, go + p * KVW);
    st16(rs, sz, 1024 + (lane & 7) * 16, go + p * KVW);
    DRAIN();
    set_flag(c, fo + p * 4);
    STAMP(12 + 4 * layer);
    wait_flags(c, fo, 0xffffull);
    STAMP(13 + 4 * layer);
    v4u vt = ld16(rs, (lane & 15) * 16, WS_VOTE);
    const v4u zl = ld16(rs, 1024 + (lane & 15) * KVW + (lane >> 4) * 16, go);
    v4u g[16];
#pragma unroll
    for (int i = 0; i < 16; ++i) g[i] = ld16(rs, lane * 16, go + i * KVW);
    SCHED();
    const float z0 = row_sum(u2f(zl.x)), z1 = row_sum(u2f(zl.y)), z2 = row_sum(u2f(zl.z)), z3 = row_sum(u2f(zl.w));
    if ((lane & 15) == 0) vst<v2u>(L_ZR + (lane >> 4) * 16 + sw * 8, v2u{pk2(z0, z1), pk2(z2, z3)});
    f32x4 r = __builtin_bit_cast(f32x4, g[0]);
#pragma unroll
    for (int i = 1; i < 16; ++i) r += __builtin_bit_cast(f32x4, g[i]);
    vst<v2u>(L_KVR + lane * 16 + sw * 8, v2u{pk2(r.x, r.y), pk2(r.z, r.w)});
    while (__builtin_amdgcn_ballot_w64(vt.y != c.tag || vt.w != c.tag) != 0ull && !spent(c)) vt = ld16(rs, (lane & 15) * 16, WS_VOTE);
    vst<unsigned>(L_CB, row_sum((float)((int)vt.x + (int)vt.z)) > 0.f ? 1u : 0u);
    lds_add(pa, L_F2 - L_RDY, 2);
    STAMP(14 + 4 * layer);
    __builtin_amdgcn_s_setprio(0);
    if (layer == 0) {
      v4u t[16];
#pragma unroll
      for (int i = 0; i < 16; ++i) t[i] = ld16(rs, min(lane, 47) * 16, co + i * 768);
      SCHED();
      f32x4 v = __builtin_bit_cast(f32x4, t[0]);
#pragma unroll
      for (int i = 1; i < 16; ++i) v += __builtin_bit_cast(f32x4, t[i]);
      if (lane < 48) {
#pragma unroll
        for (int e = 0; e < 4; ++e) vst<unsigned>(L_CSF + 2048 + (lane * 4 + e) * 16 + sw * 4, pk2(v[e], v[e] - blo(pk2(v[e], v[e]))));
      }
      lds_add(pa, L_CSC - L_RDY + 8, 2); lds_add(pa, L_CSC - L_RDY + 12, 2); lds_add(pa, L_CSC - L_RDY + 16, 2);
    }
  }
}
template <int AT, bool TWO> DI void cs_hook(CW& c, int i, unsigned k, v4u& a, v4u& b) {
  if (i == AT) a.x = vld<unsigned>(L_CSC + k * 4);
  if (i == AT + 3) {
    wait_count(c, a.x, L_CSC + k * 4, TWO ? 4u : 2u);
    a = vld<v4u>(L_CSF + k * 1024 + (c.lane & 31) * 16);
    if (TWO) b = vld<v4u>(L_CSF + k * 1024 + 512 + (c.lane & 31) * 16);
  }
}

__global__ void __launch_bounds__(512, 2) fused_fwd(Params P) {
  const int tid = threadIdx.x; int lane = tid & 63; const int wv = __builtin_amdgcn_readfirstlane(tid >> 6);
  const int b = blockIdx.x;
  {
    const unsigned co = CODE_BYTES - 256u - min(((b >> 3) * 8 + wv) * 256u, CODE_BYTES - 256u) + lane * 4;
    unsigned long long pc;
    asm volatile("s_mov_b32 m0, %2\n\ts_getpc_b64 %0\n\tglobal_load_lds_dword %1, %0" : "=&s"(pc) : "v"(co), "s"((unsigned)(uintptr_t)(LAS char*)smem + L_DUMMY) : "memory");
  }
  int p, m;
  if (MAPMODE == 0) { p = (b & 7) * 2 + (b >> 7); m = (b >> 3) & 15; }
  else if (MAPMODE == 1) { const int x = b & 7, i = b >> 3; p = (x >> 1) * 4 + (i >> 3); m = (x & 1) * 8 + (i & 7); }
  else { const int x = b & 7, i = b >> 3; p = i >> 1; m = x * 2 + (i & 1); }
  if (tid < (L_END - L_RDY) / 4) vst<unsigned>(L_RDY + tid * 4, tid == 4 || tid == 5 ? tid - 1u : 0u);
  if (tid < 320) vst<v4u>(L_CSF + tid * 16, v4u{0u, 0u, 0u, 0u});
  asm volatile("s_waitcnt lgkmcnt(0)\n\ts_barrier" ::: "memory");
  if (wv >= 4) __builtin_amdgcn_s_setprio(3);
  STAMP(21);
  if (b < 16 && (wv & 6) == 4) {
    const unsigned po = WS_PROBE + (b * 2 + (wv & 1)) * 128u + (lane & 1) * 64 + (lane >> 1) * 4096;
    asm volatile("s_mov_b64 exec, 0xff\n\ts_nop 4\n\tglobal_atomic_or %0, %1, %2\n\ts_mov_b64 exec, -1" ::"v"(po), "v"(0u), "s"(uni(P.ws)) : "memory");
  }
  const unsigned smem_base = (unsigned)(uintptr_t)(LAS char*)smem;
  int w = wv & 3;
  unsigned xv[4];
  const char* xbase = (wv < 4 ? P.x : P.wqkv) + (unsigned)sopaque(wv < 4 ? (4 * p + w) * 32768 : ((w >> 1) + 1) * 1024 + m * 64);
  auto xbuf = [&](int q) -> unsigned { const int j = q % 5; return (j == 0 ? L_SCR : j == 1 ? 7 * 16384 : j == 2 ? 6 * 16384 : j == 3 ? 4 * 16384 : 5 * 16384) + w * 4096; };
  {
#pragma unroll
    for (int k = 0; k < 4; ++k) {
      const unsigned a = (8 * k + (lane >> 3)) * 1024 + (((lane & 7) ^ ((4 * k + (lane >> 4)) & 7)) * 16) - k * 1024;
      const unsigned c = (lane >> 2) * 3072 + (lane & 3) * 16 + k * 48128;
      xv[k] = wv < 4 ? a : c;
    }
    const unsigned cw = wv < 4, stride = sopaque(cw ? 128 : 64 * 3072), r0 = ROT(m) + (cw ? 0 : (w & 1));
    dma4(xbase + (r0 & 7) * stride, xv[0], xv[1], xv[2], xv[3], smem_base + (cw ? xbuf(0) : w * 4096));
    if (!cw) {
      const unsigned pa = smem_base + (lane == 0 ? L_RDY : L_DUMMY + lane * 4), a = tr_lane(lane) + w * 4096u;
      float c0 = 0.f, c1 = 0.f;
      auto cs = [&](int j) __attribute__((always_inline)) {
#pragma unroll
        for (int n = 0; n < 4; ++n) {
          const v4u f = __builtin_bit_cast(v4u, ldw(a + j * 16384 + n * 1024, 512));
          c0 = dot2(f.x, ONES, c0); c1 = dot2(f.y, ONES, c1); c0 = dot2(f.z, ONES, c0); c1 = dot2(f.w, ONES, c1);
        }
        PIN(c0); PIN(c1);
      };
      STAMP(2);
      asm volatile("s_waitcnt vmcnt(0)\n\tds_add_u32 %0, %1" ::"v"(pa), "v"(1u) : "memory");
      STAMP(3);
      dma4(xbase + ((2 + r0) & 7) * stride, xv[0], xv[1], xv[2], xv[3], smem_base + 1 * 16384 + w * 4096);
      cs(0);
      asm volatile("s_waitcnt vmcnt(0)\n\tds_add_u32 %0, %1 offset:4" ::"v"(pa), "v"(1u) : "memory");
      STAMP(4);
      dma4(xbase + ((4 + r0) & 7) * stride, xv[0], xv[1], xv[2], xv[3], smem_base + 2 * 16384 + w * 4096);
      cs(1);
      asm volatile("s_waitcnt vmcnt(0)\n\tds_add_u32 %0, %1 offset:8" ::"v"(pa), "v"(1u) : "memory");
      STAMP(5);
      dma4(xbase + ((6 + r0) & 7) * stride, xv[0], xv[1], xv[2], xv[3], smem_base + 3 * 16384 + w * 4096);
      cs(2);
      asm volatile("s_waitcnt vmcnt(0)\n\tds_add_u32 %0, %1 offset:12" ::"v"(pa), "v"(1u) : "memory");
      STAMP(6);
      const int ks = 2 * p + (w & 1), r2 = GM(m) < 11 ? 2 : 1;
      v4u rp[9];
      if (w >= 2) {
        __builtin_amdgcn_s_setprio(RPP);
        const GAS char* sq = (const GAS char*)P.wqkv + m * 64 + ks * (16 * 3072) + xv[0];
        const unsigned vg = GM(m) * 64 + ks * (16 * 2752) + (lane >> 2) * 2752 + (lane & 3) * 16;
        const GAS char* sg = (const GAS char*)P.wg + vg; const GAS char* su = (const GAS char*)P.wu + vg;
        rp[0] = *(const GAS v4u*)(sg); rp[1] = *(const GAS v4u*)(su); rp[2] = *(const GAS v4u*)(sg + 1024); rp[3] = *(const GAS v4u*)(su + 1024);
        rp[4] = *(const GAS v4u*)(sg + r2 * 1024); rp[5] = *(const GAS v4u*)(su + r2 * 1024);
        rp[6] = *(const GAS v4u*)(sq + 1024); rp[7] = *(const GAS v4u*)(sq + 2048); rp[8] = *(const GAS v4u*)(sq);
        SCHED();
        __builtin_amdgcn_s_setprio(3);
      }
      cs(3);
      const float t = sum_halves(c0 + c1);
      vst<unsigned>(L_CSF + (w >> 1) * 512 + (lane & 31) * 16 + (w & 1) * 4, pk2(t, t - blo(pk2(t, t))));
      asm volatile("ds_add_u32 %0, %1 offset:%c2" ::"v"(pa), "v"(1u), "i"(L_CSC - L_RDY) : "memory");
      if (sopaque(w) >= 2) {
        const rsrc_t rs = __builtin_amdgcn_make_buffer_rsrc((void*)P.ws, 0, WS_END, 0x00020000);
        const int dg = (GM(m) * 32 + ks) * 1024, dq = WS_RQ + (m * 96 + ks) * 1024;
        const unsigned SCX = L_CSF + 3072 + (w & 1) * 1024;
        v4u f[8];
#pragma unroll
        for (int u = 0; u < 8; ++u) {
          vst<v4u>(SCX + lane * 16, rp[u]);
          CFENCE();
          f[u] = __builtin_bit_cast(v4u, ldw(SCX + tr_lane(lane), 512));
          CFENCE();
        }
        vst<v4u>(SCX + lane * 16, v4u{0u, 0u, 0u, 0u});
        st16(rs, f[0], lane * 16, WS_RG + dg); st16(rs, f[1], lane * 16, WS_RU + dg);
        st16(rs, f[2], lane * 16, WS_RG + dg + 16 * 32768); st16(rs, f[3], lane * 16, WS_RU + dg + 16 * 32768);
        st16(rs, f[4], lane * 16, WS_RG + dg + r2 * (16 * 32768)); st16(rs, f[5], lane * 16, WS_RU + dg + r2 * (16 * 32768));
        st16(rs, f[6], lane * 16, dq); st16(rs, f[7], lane * 16, dq + 32768); st16(rs, rp[8], lane * 16, dq + 65536);
        STAMP(8);
        float cs6[6];
#pragma unroll
        for (int u = 0; u < 6; ++u) cs6[u] = dot2(f[u].x, ONES, dot2(f[u].y, ONES, dot2(f[u].z, ONES, dot2(f[u].w, ONES, 0.f))));
#pragma unroll
        for (int u = 0; u < 3; ++u) {
          const auto r = __builtin_amdgcn_permlane32_swap(f2u(cs6[2 * u]), f2u(cs6[2 * u + 1]), false, false);
          __builtin_amdgcn_raw_buffer_store_b32(f2u(u2f(r[0]) + u2f(r[1])), rs, lane * 4 + u * 256, WS_CSP + ((m * 2 + (w & 1)) * 16 + p) * 768, 16);
        }
        STAMP(9);
      }
    }
  }
  STAMPV(0, t00);
  STAMP(61);
  if (sopaque(wv) >= 6) {
    server(P, p, m, wv - 6);
    STAMP(1); STAMP_END(); return; }
  if (sopaque(wv) >= 4) { loader(P, p, m, wv - 4); STAMP(1); STAMP_END(); return; }
  __builtin_amdgcn_s_setprio(2);

  unsigned epoch;
  asm volatile("s_load_dword %0, %1, 0x0" : "=s"(epoch) : "s"(P.ws) : "memory");
  v4u B[32];
  auto xdma = [&](int q) __attribute__((always_inline)) { dma4(xbase + ((q + ROT(m)) & 7) * 128, xv[0], xv[1], xv[2], xv[3], smem_base + xbuf(q)); };
  xdma(1);
  const rsrc_t rs = __builtin_amdgcn_make_buffer_rsrc((void*)P.ws, 0, WS_END, 0x00020000);
  asm volatile("s_waitcnt lgkmcnt(0)" : "+s"(epoch) :: "memory");
  STAMP(19);
  CW c;
  c.s = 0; c.pos = 0; c.perm = 4; c.lane_tr = tr_lane(lane); c.fa = lane == 0 ? L_FCNT + w * 4 : L_DUMMY + lane * 4;
  c.budget = BUDGET; c.lane = lane; c.w = w; c.ws = P.ws; c.tag = epoch + 1u;
  int h, t32, tbq, tbw, kh, cb;
  int sdn = 0;
#define FRESH() do { lane = opaque(lane); h = lane >> 5; t32 = lane & 31; c.lane = lane; p = sopaque(p); m = sopaque(m); w = sopaque(w); c.w = w; \
    tbq = layer * 64 + 4 * p + w; kh = w >> 1; tbw = layer * 64 + 4 * p + 2 * (m >> 3) + (w & 1); cb = 2 * (m & 7) + kh; } while (0)
  unsigned R[8];

  int layer = 0;
    f32x16 aK = {}, aV = {};
    float la; unsigned mf;
    f32x4 ar[4];
    v4u ck, cv;
    FRESH();
    auto rows_ab = [&]() __attribute__((always_inline)) {
      vst<float>(L_SA + w * 128 + t32 * 4, la);
#pragma unroll
      for (int g = 0; g < 4; ++g) ar[g] = vld<f32x4>(L_SA + w * 128 + (8 * g + 4 * h) * 4);
    };
    {
      float s1 = 0.f, s2 = 0.f, s3 = 0.f, s4 = 0.f;
      const unsigned xl = t32 * 128 + 8 * h, xf = (t32 >> 1) & 7;
      auto hook = [&](int i, int hf) __attribute__((always_inline)) {
        const int n = i + 2;
        if (n >= 0 && n < 32 && hf == 0) {
          const int q = n / 4, j = n % 4;
          if (j == 0) {
            if (q == 0) { STAMPB(39); wait_vm<4 * (XUP - 1)>(); STAMPB(40); }
            if (q == 1 || q == 2 || q == 3) wait_vm<12>();
            if (q == 4) wait_vm<12>();
            if (q == 5) wait_vm<8>();
            if (q == 6) wait_vm<4>();
            if (q == 7) wait_vm<0>();
          }
          const v2u lo = vld<v2u>(xbuf(q) + xl + (((2 * j) ^ xf) * 16)), hi = vld<v2u>(xbuf(q) + xl + (((2 * j + 1) ^ xf) * 16));
          B[n] = v4u{lo.x, lo.y, hi.x, hi.y};
        }
        if (hf == 1) {
          if (i == -1) xdma(2);
          if (i == 0) xdma(3);
          if (i == 1) xdma(4);
          if (i == 3 || i == 7 || i == 11) xdma(5 + i / 4);
          if (i >= 0 && i % 4 == 3) {
#pragma unroll
            for (int u = i - 3; u <= i; ++u) {
              const v4u f = B[u];
              s1 = dot2(f.x, ONES, s1); s2 = dot2(f.x, f.x, s2); s3 = dot2(f.y, ONES, s3); s4 = dot2(f.y, f.y, s4);
              s1 = dot2(f.z, ONES, s1); s2 = dot2(f.z, f.z, s2); s3 = dot2(f.w, ONES, s3); s4 = dot2(f.w, f.w, s4);
            }
            PIN(s1); PIN(s2); PIN(s3); PIN(s4);
          }
          cs_hook<26, true>(c, i, 0, ck, cv);
          if (n == 15 || n == 19) permit(c, n / 4 + 2);
          if (i == 30) permit(c, 10);
        }
      };
      gemm_pair<true>(c, B, aK, aV, hook);
      ln_factors(sum_halves(s1 + s3), sum_halves(s2 + s4), la, mf, h != 0);
      rows_ab();
    }
  for (; layer < 2; ++layer) {
    const bf16x8 mfr = asb(v4u{mf, mf, mf, mf});
    aK = mfma(mfr, asb(ck), aK);
    aV = mfma(mfr, asb(cv), aV);
    STAMPB(2 + 20 * layer);
    FRESH();
    {
      unsigned pkk[8], pkv[8];
#pragma unroll
      for (int j = 0; j < 8; ++j) {
        const f32x2 a = (j & 1) ? f32x2{ar[j >> 1].z, ar[j >> 1].w} : f32x2{ar[j >> 1].x, ar[j >> 1].y};
        pkk[j] = phi2(pair(aK, j), a);
        pkv[j] = pk2(pair(aV, j) * a);
      }
      const bf16x8 k0 = asb(v4u{pkk[0], pkk[1], pkk[2], pkk[3]}), k1 = asb(v4u{pkk[4], pkk[5], pkk[6], pkk[7]});
      const bf16x8 v0 = asb(v4u{pkv[0], pkv[1], pkv[2], pkv[3]}), v1 = asb(v4u{pkv[4], pkv[5], pkv[6], pkv[7]});
      const bf16x8 one = asb(v4u{ONES, ONES, ONES, ONES});
      f32x16 kv = {}, zz = {};
      kv = mfma(k0, v0, kv); zz = mfma(k0, one, zz); kv = mfma(k1, v1, kv); zz = mfma(k1, one, zz);
      const bool hd = (lane & 16) != 0;
      f32x4 q0, q1, z0, z1;
#pragma unroll
      for (int j = 0; j < 4; ++j) { q0[j] = sel(hd, kv[8 + j], kv[j]); q1[j] = sel(hd, kv[12 + j], kv[4 + j]); }
      vst<f32x4>(L_KVS + w * 2048 + lane * 16, q0);
      vst<f32x4>(L_KVS + w * 2048 + 1024 + lane * 16, q1);
#pragma unroll
      for (int j = 0; j < 4; ++j) { z0[j] = sel(hd, zz[8 + j], zz[j]); z1[j] = sel(hd, zz[12 + j], zz[4 + j]); }
      vst<f32x4>(L_ZS + w * 128 + (lane >> 4) * 32, z0);
      vst<f32x4>(L_ZS + w * 128 + (lane >> 4) * 32 + 16, z1);
      post(c, L_F1, layer + 1);
    }
    STAMPB(3 + 20 * layer);
    unsigned pq[8];
    {
      f32x16 aQ = {};
      v4u c0;
      auto hook = [&](int i) __attribute__((always_inline)) {
        if (i == 20) {
          if (layer == 0) {
            const GAS char* xr = (const GAS char*)P.x + (size_t)(tbw * 32 + t32) * 1024 + cb * 64 + h * 8;
#pragma unroll
            for (int g = 0; g < 4; ++g) { const v2u v = *(const GAS v2u*)(xr + g * 16); R[2 * g] = v.x; R[2 * g + 1] = v.y; }
          }
        }
        cs_hook<24, false>(c, i, 1, c0, c0);
      };
      gemm_single(c, B, aQ, hook);
      aQ = mfma(asb(c0), mfr, aQ);
#pragma unroll
      for (int j = 0; j < 8; ++j) { pq[j] = phi2(pair(aQ, j), f32x2{la, la}); PIN(pq[j]); }
    }
    STAMPB(4 + 20 * layer);
    {
      FRESH();
      f32x16 aM = {}, aO = {};
      Wide<16> d;
      const unsigned s0 = c.s;
      wide_steps<16, -16, 0>(c, d, B, aM, aO, kh, NoHook());
      STAMPB(54);
      v4u fk, fz; unsigned cbv;
      for (;;) {
        const unsigned fl = vld<unsigned>(L_F2);
        fk = vld<v4u>(L_KVR + lane * 16); fz = vld<v4u>(L_ZR + (lane >> 4) * 16); cbv = vld<unsigned>(L_CB);
        if (rfl(fl) >= 4u * layer + 4u || spent(c)) break;
      }
      STAMPB(5 + 20 * layer);
      sdn = (p >> 3) ^ rfl(cbv);
      permit(c, s0 + 8);
      const bool hd = (lane & 16) != 0;
      const v4u zero = {0u, 0u, 0u, 0u};
      const bf16x8 p0 = asb(v4u{pq[0], pq[1], pq[2], pq[3]}), p1 = asb(v4u{pq[4], pq[5], pq[6], pq[7]});
      f32x16 num = {}, den = {};
      den = mfma(asb(hd ? zero : fz), p0, den); den = mfma(asb(hd ? fz : zero), p1, den);
      num = mfma(asb(hd ? zero : fk), p0, num); num = mfma(asb(hd ? fk : zero), p1, num);
      const float i0 = __builtin_amdgcn_rcpf(den[0] + 1e-6f), i1 = __builtin_amdgcn_rcpf(den[8] + 1e-6f);
      const int os = a_act(WS_O, tbq, sdn) + fr(2 * m);
      st16(rs, v4u{pk2(pair(num, 0) * i0), pk2(pair(num, 1) * i0), pk2(pair(num, 2) * i0), pk2(pair(num, 3) * i0)}, lane * 16, os);
      st16(rs, v4u{pk2(pair(num, 4) * i1), pk2(pair(num, 5) * i1), pk2(pair(num, 6) * i1), pk2(pair(num, 7) * i1)}, lane * 16 + 1024, os);
      aM = unpack(R); PIN(aM);
      int oo = a_act(WS_O, tbw, sdn) + kh * 4096, xs = a_act(WS_X1, tbw, sdn) + fr(2 * cb), ss = a_stat(WS_ST1, tbw, sdn) + cb * 256;
      SPIN(oo); SPIN(xs); SPIN(ss);
      DRAIN();
      set_flag(c, a_flag(WS_FO, tbq, sdn) + m * 4);
      STAMPB(6 + 20 * layer);
      wait_flags(c, a_flag(WS_FO, tbw, sdn), kh ? 0xccccull : 0x3333ull);
      STAMPB(7 + 20 * layer);
#pragma unroll
      for (int i = 0; i < AW; ++i) B[i] = ld16(rs, lane * 16 + (i & 3) * 1024, oo + (i >> 2) * 16384);
      SCHED();
      auto hook = [&](int i, int hf) __attribute__((always_inline)) {
        const int n = i + AW;
        if (hf == 0) return;
        if (n < 16) B[n] = ld16(rs, lane * 16 + (n & 3) * 1024, oo + (n >> 2) * 16384);
        if (n == 16) permit(c, s0 + 12);
      };
      wide_steps<16, 0, 16>(c, d, B, aM, aO, kh, hook);
      STAMPB(8 + 20 * layer);
      finish_wide<16>(c, d, B, aM, aO, R, 2 * layer + 1);
      publish_x(c, rs, R, xs, ss);
      vst<v4u>(L_KVS + w * 2048 + lane * 16, v4u{R[0], R[1], R[2], R[3]});
      vst<v4u>(L_KVS + w * 2048 + 1024 + lane * 16, v4u{R[4], R[5], R[6], R[7]});
    }
    unsigned pf;
    f32x16 aM = {}, aO = {};
    Wide<4> d;
    unsigned sd;
    int fd, hd0, fl2, xs2, ss2; unsigned long long mk;
    auto ldh = [&](int n) __attribute__((always_inline)) {
      if (n < 32) B[n % 16] = ld16(rs, lane * 16 + (n & 3) * 1024, hd0 + (((n >> 2) & 4) | (((n >> 2) + ROT(m)) & 3)) * 16384);
      else if (n < 40) B[n % 16] = ld16(rs, lane * 16 + (n & 3) * 1024, hd0 + (n >> 2) * 16384);
      else B[n % 16] = ld16(rs, lane * 16 + (n & 3) * 1024, hd0 + 163840 - kh * 1024);
    };
    {
      FRESH();
      f32x16 G0 = {}, U0 = {}, G1 = {}, U1 = {};
      Pipe q;
      const unsigned s0 = c.s;
      pair_steps<false, -PD, 0, 64, false>(c, q, B, G0, U0, NoHook());
      const int xo = a_act(WS_X1, tbq, sdn);
      int xa = xo + fr(4 * ROT(m)), xb = xo + fr(4 * ((ROT(m) + 1) & 7));
      SPIN(xa); SPIN(xb);
      DRAIN();
      set_flag(c, a_flag(WS_FX1, tbw, sdn) + cb * 4);
      STAMPB(9 + 20 * layer);
      wait_flags(c, a_flag(WS_FX1, tbq, sdn), 0xffffull);
      STAMPB(10 + 20 * layer);
      Stats sv;
#pragma unroll
      for (int i = 0; i < 8; ++i) B[i] = ld16(rs, lane * 16 + (i & 3) * 1024, i < 4 ? xa : xb);
      SCHED();
      float a2; unsigned gm;
      v4u cg, cu;
      unsigned hp[8];
      const int ho = a_hid(tbq, sdn) + fr(2 * GM(m)), fo = a_flag(WS_FH, tbq, sdn) + GM(m) * 4;
      float hv;
      fd = a_flag(WS_FH, tbw, sdn); mk = kh ? 0xccccull : 0x3333ull; hd0 = a_hid(tbw, sdn) + kh * 4096;
      fl2 = fo + 128;
      auto piece = [&](int e, const f32x16& G, const f32x16& U) __attribute__((always_inline)) {
        float g;
        if (e & 1) asm volatile("v_max_f32 %1, 0, %2\n\tv_mul_f32 %0, %3, %4\n\tv_mul_f32 %1, %1, %0\n\tv_cvt_pk_bf16_f32 %0, %5, %1" : "=&v"(hp[e >> 1]), "=&v"(g) : "v"(G[e]), "v"(U[e]), "v"(a2), "v"(hv));
        else asm volatile("v_max_f32 %1, 0, %2\n\tv_mul_f32 %0, %3, %4\n\tv_mul_f32 %0, %1, %0" : "=&v"(hv), "=&v"(g) : "v"(G[e]), "v"(U[e]), "v"(a2));
      };
      auto tail = [&](const f32x16& G, const f32x16& U) __attribute__((always_inline)) {
#pragma unroll
        for (int j = 0; j < 8; ++j) {
          const f32x2 g = {fmaxf(G[2 * j], 0.f), fmaxf(G[2 * j + 1], 0.f)};
          hp[j] = pk2(g * (pair(U, j) * a2));
        }
      };
      auto store_h = [&](int r, int half) __attribute__((always_inline)) { st16(rs, v4u{hp[4 * half], hp[4 * half + 1], hp[4 * half + 2], hp[4 * half + 3]}, lane * 16 + half * 1024, ho + r * 65536); };
      auto correct = [&](f32x16& G, f32x16& U) __attribute__((always_inline)) {
        const bf16x8 f = asb(v4u{gm, gm, gm, gm});
        G = mfma(asb(cg), f, G); U = mfma(asb(cu), f, U);
      };
      auto shadow = [&](int i, int hf, int r, const f32x16& G, const f32x16& U) __attribute__((always_inline)) {
        constexpr int at[16] = {3, 5, 7, 9, 12, 13, 14, 15, 17, 19, 21, 23, 25, 28, 29, 30};
#pragma unroll
        for (int e = 0; e < 16; ++e) if (2 * i + hf == at[e]) piece(e, G, U);
        if (2 * i + hf == 31) store_h(r, 0);
        if (2 * i + hf == 33) store_h(r, 1);
        if (hf == 0 && i == 25) { if (r == 0) DRAIN(); else wait_vm<8>(); set_flag(c, fo + r * 64); }
        if (hf == 1) cs_hook<25, true>(c, i, 3 + r, cg, cu);
        if (r == 1) {
          if (hf == 0 && i == 8) pf = poll_issue(c, fd, mk);
          if (hf == 0 && i == 17) { poll_done<2>(c, pf, fd, mk); if (layer) STAMPB(51); }
          if (hf == 1 && i >= 17) ldh(i - 17);
          if (hf == 0 && i == 30) pf = poll_issue(c, fd, mk << 16);
        }
      };
      pair_steps<false, 0, 32, 64, false>(c, q, B, G0, U0, [&](int i, int hf) __attribute__((always_inline)) {
        const int n = i + AH;
        stats_hook(c, sv, 2 * i + hf, rs, a_stat(WS_ST1, tbq, sdn), a2, gm);
        if (hf == 0) return;
        if (n < 32) B[n] = ld16(rs, lane * 16 + (n & 3) * 1024, xo + fr(4 * (((n >> 2) + ROT(m)) & 7)));
        if (n == 32) permit(c, s0 + (GM(m) < 11 ? 23 : 19));
        if (i == 26) { a2 = fmul1(a2, a2); PIN(a2); }
        cs_hook<25, true>(c, i, 2, cg, cu);
      });
      correct(G0, U0);
      STAMPB(11 + 20 * layer);
      pair_steps<false, 32, 64, 96, false>(c, q, B, G1, U1, [&](int i, int hf) __attribute__((always_inline)) { shadow(i, hf, 0, G0, U0); });
      correct(G1, U1);
      STAMPB(12 + 20 * layer);
      pf = ~c.tag;
      if (sopaque(GM(m)) >= 11) {
        tail(G1, U1);
        store_h(1, 0); store_h(1, 1);
        DRAIN();
        set_flag(c, fo + 64);
        wait_flags(c, fd, mk);
      }
      if (sopaque(GM(m)) < 11) {
        G0 = f32x16{}; U0 = f32x16{};
        pair_steps<false, 64, 96, 96, false>(c, q, B, G0, U0, [&](int i, int hf) __attribute__((always_inline)) { shadow(i, hf, 1, G1, U1); });
        correct(G0, U0);
        tail(G0, U0);
        store_h(2, 0); store_h(2, 1);
        STAMPB(13 + 20 * layer);
      }
      if (sopaque(GM(m)) >= 11) {
#pragma unroll
        for (int i = 0; i < 15; ++i) ldh(i);
      }
      sd = c.s;
      const v4u r0 = vld<v4u>(L_KVS + w * 2048 + lane * 16), r1 = vld<v4u>(L_KVS + w * 2048 + 1024 + lane * 16);
      wide_steps<43, -4, 0>(c, d, B, aM, aO, kh, NoHook());
      R[0] = r0.x; R[1] = r0.y; R[2] = r0.z; R[3] = r0.w; R[4] = r1.x; R[5] = r1.y; R[6] = r1.z; R[7] = r1.w;
      aM = unpack(R);
    }
    STAMPB(14 + 20 * layer);
    {
      unsigned pf2;
      auto hook = [&](int i, int hf) __attribute__((always_inline)) {
        if (hf == 0) {
          if (i == FS) { wait_vm<FS>(); set_flag(c, fl2); }
          if (i == PI) pf2 = poll_issue(c, fd, 0x7ffull << 32);
          return;
        }
        if (i == 1) { poll_done<5>(c, pf, fd, mk << 16); if (layer) STAMPB(47); }
        if (i == 19 && layer) STAMPB(48);
        if (i == 31 && layer) STAMPB(50);
        if (i + 15 < 32) ldh(i + 15);
        if (i == PC) { poll_done<(PI < 17 ? 17 - PI : 0)>(c, pf2, fd, 0x7ffull << 32); if (layer) STAMPB(49); }
        if (PC == 20) {
          if (i >= 20 && i <= 23) { ldh(2 * i - 8); ldh(2 * i - 7); }
          if (i >= 24 && i <= 26) ldh(i + 16);
        } else {
          if (i >= PC && i < PC + 5) { ldh(32 + 2 * (i - PC)); ldh(33 + 2 * (i - PC)); }
          if (i == PC + 5) ldh(42);
        }
        if (i == PKVAT) permit(c, sd + 11 + PKV);
        if (i == 34) { xs2 = a_act(WS_X2, tbw, sdn) + fr(2 * cb); ss2 = a_stat(WS_ST2, tbw, sdn) + cb * 256; SPIN(xs2); SPIN(ss2); }
      };
      wide_steps<43, 0, 43>(c, d, B, aM, aO, kh, hook);
      STAMPB(17 + 20 * layer);
      finish_wide<43>(c, d, B, aM, aO, R, 2 * layer + 2);
    }
#pragma unroll
    for (int i = 0; i < 32; ++i) asm volatile("" : "=v"(B[i]));
    asm volatile("" : "=v"(aK), "=v"(aV), "=v"(la), "=v"(mf), "=v"(ck), "=v"(cv));
    asm volatile("" : "=v"(ar[0]), "=v"(ar[1]), "=v"(ar[2]), "=v"(ar[3]));
    if (layer == 0) {
      publish_x(c, rs, R, xs2, ss2);
      FRESH();
      aK = f32x16{}; aV = f32x16{};
      Pipe q;
      const unsigned s0 = c.s;
      pair_steps<true, -PD, 0, 32, false>(c, q, B, aK, aV, NoHook());
      const int xo = a_act(WS_X2, tbq, sdn);
      int xa = xo + fr(4 * ROT(m)), xb = xo + fr(4 * ((ROT(m) + 1) & 7));
      SPIN(xa); SPIN(xb);
      DRAIN();
      set_flag(c, a_flag(WS_FX2, tbw, sdn) + cb * 4);
      STAMPB(18);
      wait_flags(c, a_flag(WS_FX2, tbq, sdn), 0xffffull);
      STAMPB(20);
      Stats sv;
#pragma unroll
      for (int i = 0; i < 8; ++i) B[i] = ld16(rs, lane * 16 + (i & 3) * 1024, i < 4 ? xa : xb);
      SCHED();
      auto hook = [&](int i, int hf) __attribute__((always_inline)) {
        const int n = i + AH;
        stats_hook(c, sv, 2 * i + hf, rs, a_stat(WS_ST2, tbq, sdn), la, mf);
        if (hf == 0) {
          if (i == 25) vst<float>(L_SA + w * 128 + t32 * 4, la);
          if (i == 26) { ar[0] = vld<f32x4>(L_SA + w * 128 + 4 * h * 4); ar[1] = vld<f32x4>(L_SA + w * 128 + (8 + 4 * h) * 4); }
          if (i == 27) { ar[2] = vld<f32x4>(L_SA + w * 128 + (16 + 4 * h) * 4); ar[3] = vld<f32x4>(L_SA + w * 128 + (24 + 4 * h) * 4); }
          return;
        }
        if (n < 32) B[n] = ld16(rs, lane * 16 + (n & 3) * 1024, xo + fr(4 * (((n >> 2) + ROT(m)) & 7)));
        if (n == 32) permit(c, s0 + KVP2);
        cs_hook<26, true>(c, i, 0, ck, cv);
      };
      pair_steps<true, 0, 32, 32, false>(c, q, B, aK, aV, hook);
    }
  }
  {
    layer = 0;
    FRESH();
    GAS char* o = (GAS char*)P.out + (size_t)(tbw * 32 + t32) * 1024 + cb * 64 + h * 8;
#pragma unroll
    for (int g = 0; g < 4; ++g) *(GAS v2u*)(o + g * 16) = v2u{R[2 * g], R[2 * g + 1]};
  }
  STAMPB(38);
  STAMP_END();
  STAMPV(42, (unsigned long long)(BUDGET - c.budget));
  if (b == 0 && w == 0 && lane == 0) *(GAS unsigned*)(P.ws + WS_EPOCH) = c.tag;
}
}

extern "C" void kernel_launch(void* const* d_in, const int* in_sizes, int n_in, void* d_out, int out_size, void* d_ws,
                              size_t ws_size, hipStream_t stream) {
  Params P;
  P.x = (const char*)d_in[0]; P.wqkv = (const char*)d_in[1]; P.wo = (const char*)d_in[2];
  P.wg = (const char*)d_in[4]; P.wu = (const char*)d_in[5]; P.wd = (const char*)d_in[6];
  P.out = (char*)d_out; P.ws = (char*)(((uintptr_t)d_ws + 0x1fffff) & ~(uintptr_t)0x1fffff);
  (void)hipFuncSetAttribute((const void*)fused_fwd, hipFuncAttributeMaxDynamicSharedMemorySize, LDS_BYTES);
  fused_fwd<<<256, 512, LDS_BYTES, stream>>>(P);
}
```

```cpp
#include <hip/hip_runtime.h>
#include <stdint.h>

#define GAS __attribute__((address_space(1)))
#define LAS __attribute__((address_space(3)))
#define DI __device__ __forceinline__
typedef unsigned v4u __attribute__((ext_vector_type(4)));
typedef unsigned v2u __attribute__((ext_vector_type(2)));
typedef float f32x16 __attribute__((ext_vector_type(16)));
typedef float f32x4 __attribute__((ext_vector_type(4)));
typedef float f32x2 __attribute__((ext_vector_type(2)));
typedef __bf16 bf16x8 __attribute__((ext_vector_type(8)));
typedef __bf16 bf16x2 __attribute__((ext_vector_type(2)));
typedef short s16x4 __attribute__((ext_vector_type(4)));

#define MAPMODE 1
#define LDEPTH 8
#define LPRIO 0
#define LSLEEP 1
#define AW 8
#define PKV 8
#define THIN 2
#define PD 2
#define PKVAT 16
#define THINB 18
#define KVP2 14
#define PACEB 10
#define XUP 2
#define RPP 0
#define FS 7
#define PI 13
#define PC 20
#define VCL 400
#define GPN 32
#define KPN 32
#define WPN 16
#define FPN 8
#define OAUX 0
#define AH 8

#define RM 1
#define RP 2
#define ROT(m) ((RM * (m) + RP * p) & 7)
#define GM(m) ((((m) & 7) << 1) | ((m) >> 3))
static_assert(AH == 8, "");
namespace {
constexpr int KB = 1024;
constexpr int WS_EPOCH = 0;
constexpr int WS_VOTE = 1024;
constexpr unsigned WS_PROBE = 192 * KB;
constexpr int WS_FKV = 16 * KB;
constexpr int WS_FO = WS_FKV + 32 * KB;
constexpr int WS_FX1 = WS_FO + 32 * KB;
constexpr int WS_FH = WS_FX1 + 32 * KB;
constexpr int WS_FX2 = WS_FH + 32 * KB;
constexpr int WS_ST1 = 256 * KB;
constexpr int WS_ST2 = WS_ST1 + 512 * KB;
constexpr int KVW = 1152;
constexpr int WS_KVP = WS_ST2 + 256 * KB;
constexpr int WS_CSP = WS_KVP + 2 * 16 * 16 * 2 * KVW;
constexpr int WS_O = 4096 * KB;
constexpr int WS_X1 = WS_O + 4096 * KB;
constexpr int WS_X2 = WS_X1 + 4096 * KB;
constexpr int WS_H = WS_X2 + 2048 * KB;
constexpr int WS_RQ = WS_H + 16384 * KB;
constexpr int WS_RG = WS_RQ + 1536 * KB;
constexpr int WS_RU = WS_RG + 43 * 32 * KB;
constexpr int WS_DBG = WS_RU + 43 * 32 * KB;
constexpr int WS_END = WS_DBG + 256 * 8 * 64 * 8;
constexpr int NSLOT = 8;
constexpr int L_RING = 0;
constexpr int L_SCR = NSLOT * 16 * KB;
constexpr int L_KVS = L_SCR + 16 * KB;
constexpr int L_CSF = L_KVS + 8 * KB;
constexpr int L_SA = L_CSF + 5120;
constexpr int L_KVR = L_SA + 512;
constexpr int L_ZR = L_KVR + 1024;
constexpr int L_ZS = L_ZR + 64;
constexpr int L_DUMMY = L_ZS + 512;
constexpr int L_RDY = L_DUMMY + 448;
constexpr int L_PL = L_RDY + 16;
constexpr int L_CSC = L_RDY + 32;
constexpr int L_FCNT = L_CSC + 32;
constexpr int L_F1 = L_FCNT + 16;
constexpr int L_F2 = L_F1 + 16;
constexpr int L_F3 = L_F2 + 16;
constexpr int L_RPD = L_F3 + 16;
constexpr int L_CB = L_RPD + 4;
constexpr int L_END = L_RPD + 16;
static_assert(L_END <= 160 * KB, "LDS");
constexpr int LDS_BYTES = (L_END + 15) / 16 * 16;
constexpr unsigned CODE_BYTES = 41728;
constexpr unsigned BUDGET = 1u << 18;

struct Params {
  const char* x; const char* wqkv; const char* wo; const char* wg; const char* wu; const char* wd;
  char* out; char* ws;
};

extern __shared__ __attribute__((aligned(16))) char smem[];
template <class T> DI LAS T* lp(unsigned off) { return (LAS T*)((LAS char*)smem + off); }
template <class T> DI T vld(unsigned off) { return *(volatile LAS T*)((LAS char*)smem + off); }
template <class T> DI void vst(unsigned off, T v) { *(volatile LAS T*)((LAS char*)smem + off) = v; }
DI unsigned rfl(unsigned v) { return __builtin_amdgcn_readfirstlane(v); }
DI const char* uni(const char* p) { const unsigned long long v = (unsigned long long)p; return (const char*)(((unsigned long long)rfl((unsigned)(v >> 32)) << 32) | rfl((unsigned)v)); }
#define SCHED() __builtin_amdgcn_sched_barrier(0)
#define CFENCE() asm volatile("" ::: "memory")
#define DRAIN() asm volatile("s_waitcnt vmcnt(0)" ::: "memory")
#define PIN(x) asm volatile("" : "+v"(x))

#define STAMP(i) do {} while (0)
#define STAMPV(i, v) do {} while (0)
#define STAMPB(i) do {} while (0)
#define STAMP_END() do {} while (0)

DI bf16x8 ldw(unsigned a, int second) {
  s16x4 lo = __builtin_amdgcn_ds_read_tr16_b64_v4i16(lp<s16x4>(a));
  s16x4 hi = __builtin_amdgcn_ds_read_tr16_b64_v4i16(lp<s16x4>(a + second));
  return __builtin_bit_cast(bf16x8, __builtin_shufflevector(lo, hi, 0, 1, 2, 3, 4, 5, 6, 7));
}
DI f32x16 mfma(bf16x8 a, bf16x8 b, f32x16 c) { return __builtin_amdgcn_mfma_f32_32x32x16_bf16(a, b, c, 0, 0, 0); }
DI bf16x8 asb(v4u v) { return __builtin_bit_cast(bf16x8, v); }
DI unsigned pk2(float lo, float hi) { bf16x2 r = {(__bf16)lo, (__bf16)hi}; return __builtin_bit_cast(unsigned, r); }
DI unsigned pk2(f32x2 v) { return pk2(v.x, v.y); }
DI float dot2(unsigned a, unsigned b, float c) { return __builtin_amdgcn_fdot2_f32_bf16(__builtin_bit_cast(bf16x2, a), __builtin_bit_cast(bf16x2, b), c, false); }
DI float u2f(unsigned u) { return __builtin_bit_cast(float, u); }
DI unsigned f2u(float f) { return __builtin_bit_cast(unsigned, f); }
DI float sel(bool c, float a, float b) { asm volatile("" : "+v"(a), "+v"(b)); return c ? a : b; }
DI int opaque(int v) { asm volatile("" : "+v"(v)); return v; }
DI int sopaque(int v) { asm volatile("" : "+s"(v)); return v; }
DI float blo(unsigned u) { return u2f(u << 16); }
DI float bhi(unsigned u) { return u2f(u & 0xffff0000u); }
DI float sum_halves(float v) {
  auto r = __builtin_amdgcn_permlane32_swap(f2u(v), f2u(v), false, false);
  float o; asm("v_add_f32 %0, %1, %2" : "=v"(o) : "v"(r[0]), "v"(r[1])); return o;
}
template <int CTRL> DI float dpp_add(float v) { return v + u2f(__builtin_amdgcn_update_dpp(0u, f2u(v), CTRL, 0xf, 0xf, false)); }
DI float row_sum(float v) { v = dpp_add<0x128>(v); v = dpp_add<0x124>(v); v = dpp_add<0x122>(v); return dpp_add<0x121>(v); }
DI f32x2 pair(const f32x16& a, int j) { return f32x2{a[2 * j], a[2 * j + 1]}; }
DI unsigned phi2(f32x2 x, f32x2 a) {
  const f32x2 e2 = x * (a * 1.4426950408889634f);
  const f32x2 t = x * a + 1e-6f;
  const f32x2 e = {__builtin_amdgcn_fmed3f(__builtin_amdgcn_exp2f(e2.x), 0.f, 1.f), __builtin_amdgcn_fmed3f(__builtin_amdgcn_exp2f(e2.y), 0.f, 1.f)};
  const f32x2 mx = {fmaxf(t.x, 1e-6f), fmaxf(t.y, 1e-6f)};
  return pk2(mx + e);
}
constexpr unsigned ONES = 0x3f803f80u;
DI v2u split3(float t) {
  const float r1 = t - blo(pk2(t, t)), r2 = r1 - blo(pk2(r1, r1));
  return v2u{pk2(t, r1), pk2(r2, 0.f)};
}
DI float fadd1(float a, float b) { float r; asm("v_add_f32 %0, %1, %2" : "=v"(r) : "v"(a), "v"(b)); return r; }
DI float fmul1(float a, float b) { float r; asm("v_mul_f32 %0, %1, %2" : "=v"(r) : "v"(a), "v"(b)); return r; }

typedef __amdgpu_buffer_rsrc_t rsrc_t;
DI v4u ld16(rsrc_t rs, int voff, int soff) { return __builtin_amdgcn_raw_buffer_load_b128(rs, voff, soff, 16); }
DI v2u ld8(rsrc_t rs, int voff, int soff) { return __builtin_amdgcn_raw_buffer_load_b64(rs, voff, soff, 16); }
DI void st16(rsrc_t rs, v4u v, int voff, int soff) {
  __builtin_amdgcn_raw_buffer_store_b128(v, rs, voff, soff, 16);
  asm volatile("s_nop 1" ::"v"(v) : "memory");
}
DI void st8(rsrc_t rs, v2u v, int voff, int soff) { __builtin_amdgcn_raw_buffer_store_b64(v, rs, voff, soff, 16); }

DI int near(int off, int sd) { return off + (((sd ^ (off >> 21)) & 1) << 13); }
DI int tbi(int t) { return (t >> 6) * 32 + (t & 31); }
DI int a_act(int region, int t, int sd) { return near(region + tbi(t) * 65536, sd); }
DI int a_hid(int t, int sd) { return near(WS_H + tbi(t) * 262144, sd); }
DI int a_flag(int region, int t, int sd) { return near(region + (t >> 6) * 16384 + (t & 31) * 256, sd); }
DI int a_stat(int region, int t, int sd) { return near(region + (tbi(t) >> 1) * 16384 + (t & 1) * 4096, sd); }
DI int fr(int f) { return (f >> 3) * 16384 + (f & 7) * 1024; }
#define AX(r, t) a_act(r, t, sdn)
#define FX(f) fr(f)
#define AHH(t) a_hid(t, sdn)
#define FH(f) fr(f)
constexpr int HS = 16384;
struct CW {
  unsigned s, pos;
  unsigned lane_tr;
  unsigned fa;
  unsigned tag, perm;
  int budget;
  int lane, w;
  char* ws;
};
DI bool spent(CW& c) { c.budget -= 1; return c.budget < 0; }
DI unsigned min4(v4u v) { return rfl(min(min(v.x, v.y), min(v.z, v.w))); }
DI void wait_count(CW& c, unsigned have, unsigned addr, unsigned need) {
  if (__builtin_expect(rfl(have) < need, 0)) {
    for (;;) { if (rfl(vld<unsigned>(addr)) >= need || spent(c)) break; }
  }
}
DI void wait_slot(CW& c, unsigned have, unsigned t) {
  if (__builtin_expect(have < t, 0)) {
    for (;;) { const v2u v = vld<v2u>(L_PL); if (rfl(min(v.x, v.y)) >= t || spent(c)) break; }
  }
}
DI void post(CW& c, int word, unsigned v) { vst<unsigned>(c.fa + (word - L_FCNT), v); }
DI void release(CW& c) { c.s += 1; c.pos = (c.pos + 1) & (NSLOT - 1); post(c, L_FCNT, min(c.s + NSLOT, c.perm)); }
DI void permit(CW& c, unsigned v) { c.perm = v; post(c, L_FCNT, min(c.s + NSLOT, v)); }
DI void set_flag(CW& c, int off) {
  const char* base = uni(c.ws + off);
  asm volatile("s_mov_b64 exec, 1\n\ts_nop 4\n\tglobal_atomic_swap %0, %1, %2\n\ts_mov_b64 exec, -1" ::"v"(0u), "v"(c.tag), "s"(base) : "memory");
}
DI void wait_flags(CW& c, int off, unsigned long long mask) {
  unsigned f, bud = rfl(c.budget);
  const unsigned voff = c.lane * 4;
  const char* base = uni(c.ws + off);
  asm volatile(
      "s_mov_b64 exec, %5\n\ts_nop 4\n\t"
      "1:\n\t"
      "global_atomic_or %0, %2, %6, %3 sc0\n\t"
      "s_waitcnt vmcnt(0)\n\t"
      "v_cmp_ne_u32 vcc, %4, %0\n\t"
      "s_cbranch_vccz 2f\n\t"
      "s_sub_i32 %1, %1, 8\n\t"
      "s_cmp_ge_i32 %1, 0\n\t"
      "s_cbranch_scc1 1b\n\t"
      "2:\n\t"
      "s_mov_b64 exec, -1\n\t"
      : "=&v"(f), "+s"(bud) : "v"(voff), "s"(base), "s"(c.tag), "s"(mask), "v"(0u) : "memory", "vcc", "scc");
  c.budget = rfl(bud);
}
DI unsigned poll_issue(CW& c, int off, unsigned long long mask) {
  unsigned f = c.tag;
  const unsigned voff = c.lane * 4;
  const char* base = uni(c.ws + off);
  asm volatile("s_mov_b64 exec, %3\n\tv_not_b32 %0, %0\n\ts_nop 4\n\tglobal_atomic_or %0, %1, %4, %2 sc0\n\ts_mov_b64 exec, -1"
               : "+v"(f) : "v"(voff), "s"(base), "s"(mask), "v"(0u) : "memory");
  return f;
}
template <int N> DI void poll_done(CW& c, unsigned f, int off, unsigned long long mask) {
  asm volatile("s_waitcnt vmcnt(%c1)" : "+v"(f) : "i"(N) : "memory");
  if (__builtin_expect(__builtin_amdgcn_ballot_w64(f != c.tag) != 0ull, 0)) wait_flags(c, off, mask);
}

template <int V> struct IC { static constexpr int value = V; DI constexpr operator int() const { return V; } };
template <int I, int E, class F> DI void static_for(F&& f) { if constexpr (I < E) { f(IC<I>{}); static_for<I + 1, E>(f); } }
struct Pipe { bf16x8 wa[PD + 1], wb[PD + 1]; v2u pl; unsigned ad, ad2, adn, have, rel; };
#define SPIN(x) asm volatile("" : "+s"(x))
template <bool Q0> DI void slot_first(CW& c, Pipe& q) {
  if (Q0) wait_count(c, 0u, L_RDY, 4u); else wait_slot(c, 0u, c.s + 1);
  q.adn = c.lane_tr + c.pos * 16384u;
}
template <int K, bool NX, int CK, bool Q0 = false> DI void book(CW& c, Pipe& q) {
  if constexpr (CK && K == 0) { if (Q0) q.pl.x = vld<unsigned>(L_RDY + (c.s + 1) * 4); else q.pl = vld<v2u>(L_PL); }
  if constexpr (NX && K == 2) { q.adn = c.lane_tr + ((c.pos + 1) & (NSLOT - 1)) * 16384u; PIN(q.adn); }
  if constexpr (CK && K == 3) { q.have = rfl(Q0 ? q.pl.x : min(q.pl.x, q.pl.y)); SPIN(q.have); }
  if constexpr (CK && K == 4) { if (Q0) wait_count(c, q.have, L_RDY + (c.s + 1) * 4, 4u); else wait_slot(c, q.have, c.s + CK); }
  if constexpr (K == 5) { c.s += 1; c.pos = (c.pos + 1) & (NSLOT - 1); q.rel = min(c.s + NSLOT, c.perm); PIN(q.rel); }
  if constexpr (K == 6) post(c, L_FCNT, q.rel);
}
template <bool SWAP, int I0, int I1, int NEND, bool MORE, bool FR = true, class HOOK>
DI void pair_steps(CW& c, Pipe& q, v4u (&B)[32], f32x16& acc0, f32x16& acc1, HOOK hook) {
  const unsigned keep = c.lane_tr;
  if (FR) c.lane_tr = c.lane * 16;
  static_for<I0, I1>([&](auto ic) __attribute__((always_inline)) {
    constexpr int i = ic.value, n = i + PD;
    constexpr bool F = n < NEND, NX = F && (MORE || n + 8 - n % 8 < NEND);
    constexpr int CK = !NX ? 0 : !FR ? 1 : (n / 8) % 2 ? 2 : 0;
    if constexpr (n == 0) slot_first<!FR>(c, q);
    if constexpr (i >= 0) acc0 = SWAP ? mfma(asb(B[i % 32]), q.wa[i % (PD + 1)], acc0) : mfma(q.wa[i % (PD + 1)], asb(B[i % 32]), acc0);
    SCHED();
    if constexpr (F && n % 8 == 0) q.ad = q.adn;
    if constexpr (F) q.wa[n % (PD + 1)] = FR ? asb(vld<v4u>(q.ad + (n % 8) * 1024)) : ldw(q.ad + (n % 8) * 1024, 512);
    if constexpr (F && n % 8 >= 2) book<n % 8 - 2, NX, CK, !FR>(c, q);
    hook(IC<(i < 0 ? i : i % 32)>{}, IC<0>{});
    SCHED();
    if constexpr (i >= 0) acc1 = SWAP ? mfma(asb(B[i % 32]), q.wb[i % (PD + 1)], acc1) : mfma(q.wb[i % (PD + 1)], asb(B[i % 32]), acc1);
    SCHED();
    if constexpr (F) q.wb[n % (PD + 1)] = FR ? asb(vld<v4u>(q.ad + 8192 + (n % 8) * 1024)) : ldw(q.ad + 8192 + (n % 8) * 1024, 512);
    if constexpr (F && n % 8 == 7) book<6, NX, CK>(c, q);
    hook(IC<(i < 0 ? i : i % 32)>{}, IC<1>{});
    SCHED();
  });
  c.lane_tr = keep;
}
template <bool SWAP, class HOOK>
DI void gemm_pair(CW& c, v4u (&B)[32], f32x16& acc0, f32x16& acc1, HOOK hook) {
  Pipe q;
  pair_steps<SWAP, -PD, 32, 32, false, false>(c, q, B, acc0, acc1, hook);
}
template <class HOOK>
DI void gemm_single(CW& c, v4u (&B)[32], f32x16& acc, HOOK hook) {
  bf16x8 wa[5];
  Pipe q;
  static_for<-4, 32>([&](auto ic) __attribute__((always_inline)) {
    constexpr int i = ic.value, n = i + 4;
    if constexpr (n == 0) { wait_slot(c, 0u, c.s); q.adn = c.lane_tr + c.pos * 16384u; }
    if constexpr (i >= 0) acc = mfma(wa[i % 5], asb(B[i]), acc);
    SCHED();
    if constexpr (n < 32) {
      if constexpr (n % 16 == 0) q.ad = q.adn;
      wa[n % 5] = ldw(q.ad + (n % 16) * 1024, 512);
      if constexpr (n % 16 == 6) book<0, (n < 16), (n < 16)>(c, q);
      if constexpr (n % 16 >= 10) book<n % 16 - 9, (n < 16), (n < 16)>(c, q);
    }
    hook(ic);
    SCHED();
  });
}
constexpr int TL = 6;
template <int PW> struct Wide { bf16x8 wa[PW + 1 + TL], wb[PW + 1]; Pipe q; unsigned dO, lt; };
template <int NK, int I0, int I1, int PW, class HOOK>
DI void wide_steps(CW& c, Wide<PW>& d, v4u (&B)[32], f32x16& accM, f32x16& accO, int kh, HOOK hook) {
  Pipe& q = d.q;
  const unsigned keep = c.lane_tr;
  if constexpr (I0 == -PW) { d.lt = c.lane_tr + kh * (8192u + 512u); d.dO = kh ? -512u : 512u; }
  c.lane_tr = d.lt;
  static_for<I0, I1>([&](auto ic) __attribute__((always_inline)) {
    constexpr int i = ic.value, n = i + PW;
    constexpr bool F = n < NK, NX = F && n + 4 - n % 4 < NK;
    constexpr int CK = !NX || (n / 4) % 2 == 0 ? 0 : n + 8 - n % 4 < NK ? 2 : 1;
    if constexpr (n == 0) slot_first<false>(c, q);
    if constexpr (i >= 0) accO = mfma(d.wb[i % (PW + 1)], asb(B[i % 16]), accO);
    SCHED();
    if constexpr (F && n % 4 == 0) { q.ad = q.adn; if constexpr (n + 3 == NK) q.ad -= kh * 2048u; q.ad2 = q.ad + d.dO; }
    if constexpr (F) d.wb[n % (PW + 1)] = ldw(q.ad2 + (n % 4) * 2048, 1024);
    if constexpr (F && n % 4 == 1) { book<2, NX, CK>(c, q); }
    if constexpr (F && n % 4 == 2) { book<3, NX, CK>(c, q); }
    if constexpr (F && (n % 4 == 3 || n == NK - 1)) { book<5, NX, CK>(c, q); }
    hook(ic, IC<0>{});
    SCHED();
    if constexpr (i >= 0 && i < NK - TL) accM = mfma(d.wa[i % (PW + 1 + TL)], asb(B[i % 16]), accM);
    SCHED();
    if constexpr (F) d.wa[n % (PW + 1 + TL)] = ldw(q.ad + (n % 4) * 2048, 1024);
    if constexpr (F && n % 4 == 0) book<0, NX, CK>(c, q);
    if constexpr (F && n % 4 == 2) book<4, NX, CK>(c, q);
    if constexpr (F && (n % 4 == 3 || n == NK - 1)) book<6, NX, CK>(c, q);
    hook(ic, IC<1>{});
    SCHED();
  });
  c.lane_tr = keep;
}
struct NoHook { template <class A, class B> DI void operator()(A, B) const {} };

DI void ln_factors(float sum, float sq, float& a, unsigned& mf, bool up) {
  float mean = sum * (1.0f / 512.0f);
  PIN(mean);
  const float var = fmaxf(sq * (1.0f / 512.0f) - mean * mean, 0.f);
  a = __builtin_amdgcn_rsqf(var + 1e-5f);
  const unsigned hi = pk2(-mean, -mean);
  const float lo = -mean - blo(hi);
  mf = up ? pk2(lo, lo) : hi;
}
struct Stats { v4u sv[4]; f32x4 t; f32x2 u[4]; float mean, var; };
constexpr int K0 = 34;
DI void stats_hook(CW& c, Stats& t, int k, rsrc_t rs, int soff, float& a, unsigned& mf) {
  if (k == 3 || k == 7 || k == 11 || k == 15) t.sv[k >> 2] = ld16(rs, c.lane * 16 + (k >> 2) * 1024, soff);
  const unsigned sc = L_SCR + (c.w ^ 2) * 4096;
  if (k >= K0 && k < K0 + 4) { const int e = k - K0; t.t[e] = fadd1(fadd1(u2f(t.sv[0][e]), u2f(t.sv[1][e])), fadd1(u2f(t.sv[2][e]), u2f(t.sv[3][e]))); PIN(t.t[e]); }
  if (k == K0 + 4) vst<f32x4>(sc + c.lane * 16, t.t);
  if (k == K0 + 5) { t.u[0] = vld<f32x2>(sc + (c.lane & 31) * 8); t.u[1] = vld<f32x2>(sc + 256 + (c.lane & 31) * 8); }
  if (k == K0 + 6) { t.u[2] = vld<f32x2>(sc + 512 + (c.lane & 31) * 8); t.u[3] = vld<f32x2>(sc + 768 + (c.lane & 31) * 8); }
  if (k == K0 + 10) { t.mean = fadd1(fadd1(t.u[0].x, t.u[1].x), fadd1(t.u[2].x, t.u[3].x)); PIN(t.mean); }
  if (k == K0 + 11) { t.var = fadd1(fadd1(t.u[0].y, t.u[1].y), fadd1(t.u[2].y, t.u[3].y)); PIN(t.var); }
  if (k == K0 + 12) { t.mean *= -1.0f / 512.0f; PIN(t.mean); t.var = t.var * (1.0f / 512.0f) + 1e-5f; PIN(t.var); }
  if (k == K0 + 13) { t.var = fmaxf(t.var - t.mean * t.mean, 1e-5f); PIN(t.var); a = __builtin_amdgcn_rsqf(t.var); PIN(a); }
  if (k == K0 + 14) { mf = pk2(t.mean, t.mean); t.mean -= blo(mf); PIN(mf); PIN(t.mean); }
  if (k == K0 + 15) { mf = c.lane >= 32 ? pk2(t.mean, t.mean) : mf; PIN(mf); }
}
DI f32x16 unpack(const unsigned (&R)[8]) {
  f32x16 a;
#pragma unroll
  for (int j = 0; j < 8; ++j) { a[2 * j] = blo(R[j]); a[2 * j + 1] = bhi(R[j]); }
  return a;
}
template <int NK, int PW>
DI void finish_wide(CW& c, Wide<PW>& d, v4u (&B)[32], f32x16& accM, const f32x16& accO, unsigned (&R)[8], unsigned seq) {
  const unsigned mine = L_SCR + c.w * 4096 + c.lane * 16, other = L_SCR + (c.w ^ 2) * 4096 + c.lane * 16;
  auto step = [&](int k) __attribute__((always_inline)) { accM = mfma(d.wa[(NK - TL + k) % (PW + 1 + TL)], asb(B[(NK - TL + k) % 16]), accM); SCHED(); };
  auto put = [&](int g) __attribute__((always_inline)) { vst<f32x4>(mine + g * 1024, f32x4{accO[4 * g], accO[4 * g + 1], accO[4 * g + 2], accO[4 * g + 3]}); };
  step(0); step(1);
  put(0); put(1); SCHED();
  step(2);
  put(2); put(3); post(c, L_F3, seq); SCHED();
  step(3); step(4);
  unsigned fl = vld<unsigned>(L_F3 + (c.w ^ 2) * 4);
  f32x4 v[4];
#pragma unroll
  for (int g = 0; g < 4; ++g) v[g] = vld<f32x4>(other + g * 1024);
  SCHED();
  step(5);
  if (rfl(fl) < seq) {
    for (;;) {
      fl = vld<unsigned>(L_F3 + (c.w ^ 2) * 4);
#pragma unroll
      for (int g = 0; g < 4; ++g) v[g] = vld<f32x4>(other + g * 1024);
      if (rfl(fl) >= seq || spent(c)) break;
    }
  }
#pragma unroll
  for (int g = 0; g < 4; ++g) {
    R[2 * g] = pk2(pair(accM, 2 * g) + f32x2{v[g].x, v[g].y});
    R[2 * g + 1] = pk2(pair(accM, 2 * g + 1) + f32x2{v[g].z, v[g].w});
  }
}
DI void publish_x(CW& c, rsrc_t rs, const unsigned (&R)[8], int xoff, int stoff) {
  st16(rs, v4u{R[0], R[1], R[2], R[3]}, c.lane * 16, xoff);
  st16(rs, v4u{R[4], R[5], R[6], R[7]}, c.lane * 16 + 1024, xoff);
  SCHED();
  float s0 = 0.f, q0 = 0.f, s1 = 0.f, q1 = 0.f;
#pragma unroll
  for (int i = 0; i < 4; ++i) { s0 = dot2(R[i], ONES, s0); q0 = dot2(R[i], R[i], q0); s1 = dot2(R[4 + i], ONES, s1); q1 = dot2(R[4 + i], R[4 + i], q1); }
  const float s = sum_halves(s0 + s1), q = sum_halves(q0 + q1);
  st8(rs, v2u{f2u(s), f2u(q)}, (c.lane & 31) * 8, stoff);
}

DI void dma4(const char* base, unsigned v0, unsigned v1, unsigned v2, unsigned v3, unsigned dst) {
  asm volatile("s_mov_b32 m0, %5\n\ts_nop 3\n\t"
               "global_load_lds_dwordx4 %0, %4\n\tglobal_load_lds_dwordx4 %1, %4 offset:1024\n\t"
               "global_load_lds_dwordx4 %2, %4 offset:2048\n\tglobal_load_lds_dwordx4 %3, %4 offset:3072"
               ::"v"(v0), "v"(v1), "v"(v2), "v"(v3), "s"(uni(base)), "s"(rfl(dst)) : "memory");
}
#define PACE "s_sleep 1\n\t"
DI void dma4p(const char* base, unsigned v0, unsigned v1, unsigned v2, unsigned v3, unsigned dst) {
  asm volatile("s_mov_b32 m0, %5\n\ts_nop 3\n\t"
               "global_load_lds_dwordx4 %0, %4\n\t" PACE "global_load_lds_dwordx4 %1, %4 offset:1024\n\t" PACE
               "global_load_lds_dwordx4 %2, %4 offset:2048\n\t" PACE "global_load_lds_dwordx4 %3, %4 offset:3072\n\t" PACE
               ::"v"(v0), "v"(v1), "v"(v2), "v"(v3), "s"(uni(base)), "s"(rfl(dst)) : "memory");
}
DI void dma4_sc1(const char* base, unsigned v, unsigned dst) {
  asm volatile("s_mov_b32 m0, %2\n\ts_nop 3\n\t"
               "global_load_lds_dwordx4 %0, %1 sc1\n\t" PACE "global_load_lds_dwordx4 %0, %1 offset:1024 sc1\n\t" PACE
               "global_load_lds_dwordx4 %0, %1 offset:2048 sc1\n\t" PACE "global_load_lds_dwordx4 %0, %1 offset:3072 sc1\n\t" PACE
               ::"v"(v), "s"(uni(base)), "s"(rfl(dst)) : "memory");
}
DI void dma4_sc1f(const char* base, unsigned v, unsigned dst) {
  asm volatile("s_mov_b32 m0, %2\n\ts_nop 3\n\t"
               "global_load_lds_dwordx4 %0, %1 sc1\n\tglobal_load_lds_dwordx4 %0, %1 offset:1024 sc1\n\t"
               "global_load_lds_dwordx4 %0, %1 offset:2048 sc1\n\tglobal_load_lds_dwordx4 %0, %1 offset:3072 sc1"
               ::"v"(v), "s"(uni(base)), "s"(rfl(dst)) : "memory");
}
template <int N> DI void wait_vm() { asm volatile("s_waitcnt vmcnt(%c0)" ::"i"(N) : "memory"); }
DI unsigned tr_lane(int lane) { return (4 * (lane >> 5) + ((lane & 15) >> 2)) * 64 + ((lane >> 4) & 1) * 32 + (lane & 3) * 8; }
DI int fresh_lane() { int l; asm volatile("v_mbcnt_lo_u32_b32 %0, -1, 0\n\tv_mbcnt_hi_u32_b32 %0, -1, %0" : "=v"(l)); return l; }
#define LSTAMP(v, s) do {} while (0)
DI void lds_add(unsigned pa, unsigned off, unsigned n) { CFENCE(); (void)__hip_atomic_fetch_add(lp<unsigned>(pa + off), n, __ATOMIC_RELAXED, __HIP_MEMORY_SCOPE_WORKGROUP); CFENCE(); }
DI void loader(const Params& P, int p, int m, int lw) {
  const int lane = fresh_lane();
  const unsigned smem_base = (unsigned)(uintptr_t)(LAS char*)smem;
  const unsigned nr = GM(m) < 11 ? 3 : 2, NS = 21 + 4 * nr, NT = 2 * NS;
  const unsigned v_lin = lane * 16, v_wide = ((lane & 31) >> 2) * 1024 + (lane >> 5) * 64 + (lane & 3) * 16, v_raw = (lane >> 2) * 3072 + (lane & 3) * 16;
  const unsigned pa = lane == 0 ? L_RDY : L_DUMMY + lane * 4;
  const unsigned pp = pa + (L_PL - L_RDY) + lw * 4;
  if (blockIdx.x < 16) {
    const unsigned vi = blockIdx.x * 2 + lw, first = lw * 8192u, poff = WS_PROBE + vi * 128u;
    unsigned long long t0, t1, t2; unsigned j0, j1, epoch;
    asm volatile("s_load_dword %5, %8, 0x0\n\ts_load_dword %3, %6, 0x1000\n\ts_waitcnt lgkmcnt(0)\n\t"
                 "s_memtime %0\n\ts_waitcnt lgkmcnt(0)\n\ts_load_dword %3, %6, 0x0\n\ts_waitcnt lgkmcnt(0)\n\ts_memtime %1\n\ts_waitcnt lgkmcnt(0)\n\t"
                 "s_load_dword %4, %7, 0x0\n\ts_waitcnt lgkmcnt(0)\n\ts_memtime %2\n\ts_waitcnt lgkmcnt(0)"
                 : "=&s"(t0), "=&s"(t1), "=&s"(t2), "=&s"(j0), "=&s"(j1), "=&s"(epoch)
                 : "s"(uni(P.ws + poff + first)), "s"(uni(P.ws + poff + (first ^ 8192u))), "s"(uni(P.ws)) : "memory");
    int d = min(max((int)((unsigned)(t1 - t0) - (unsigned)(t2 - t1)), -VCL), VCL);
    if ((lw ^ (p >> 3) ^ (poff >> 21)) & 1) d = -d;
    st8(__builtin_amdgcn_make_buffer_rsrc((void*)P.ws, 0, WS_END, 0x00020000), v2u{(unsigned)d, epoch + 1u}, 0, WS_VOTE + vi * 8);
  }
  __builtin_amdgcn_s_setprio(LPRIO);
  unsigned limit = 0;
  int budget = BUDGET;
  bool pend = false;
  auto entry = [&](unsigned s) __attribute__((always_inline)) -> unsigned long long {
    const bool l2 = s >= NS;
    const unsigned q = lane & 3, i = l2 ? s - NS : s, g = i - 10u, d = g - 4u * nr, r4 = 4u * ROT(m);
    const unsigned blk = i < 4u ? q >> 1 : 2u, ks0 = ((i < 4u ? 8u * i + (q & 1) * 4u : 16u * (i - 4u) + q * 4u) + r4) & 31u;
    if (i < 6u && l2) return (unsigned long long)P.ws + WS_RQ + ((m * 3 + blk) * 32u + ks0) * 1024u + 1u;
    if (i < 6u) return (unsigned long long)P.wqkv + m * 64 + ks0 * (16u * 3072u);
    if (i < 10u) return (unsigned long long)P.wo + ((i - 6u) * 128u + q * 32u) * 1024u + (m & 7) * 128 + 2u;
    if (g < 4u * nr) return (unsigned long long)P.ws + ((q >> 1) ? WS_RU : WS_RG) + ((16u * (g >> 2) + GM(m)) * 32u + ((8u * (g & 3u) + (q & 1) * 4u + r4) & 31u)) * 1024u + 1u;
    return (unsigned long long)P.wd + min((d < 8u ? (d & 4u) | ((d + ROT(m)) & 3u) : d) * 128u + q * 32u, 1344u) * 1024u + (m & 7) * 128 + 2u;
  };
  unsigned long long tab = entry(4u + lw + 2u * (lane >> 2));
  const unsigned long long tab2 = entry(36u + lw + 2u * (lane >> 2));
  for (unsigned s = 4 + lw, t = 0; s < NT; s += 2, t = (t + 4) & 63u) {
    const unsigned i = s >= NS ? s - NS : s;
    if (s >= 36u) tab = tab2;
    const char* src[4];
#pragma unroll
    for (int q = 0; q < 4; ++q)
      src[q] = (const char*)(((unsigned long long)__builtin_amdgcn_readlane((unsigned)(tab >> 32), t + q) << 32) | (__builtin_amdgcn_readlane((unsigned)tab, t + q) & ~3u));
    const unsigned kind = __builtin_amdgcn_readlane((unsigned)tab, t) & 3u;
    if (pend && i < THINB) { wait_vm<0>(); vst<unsigned>(pp, s - 1); LSTAMP(tlan, s - 2); pend = false; }
    while (s >= limit) {
      limit = min4(vld<v4u>(L_FCNT));
      if (s < limit) break;
      if (pend) { wait_vm<0>(); vst<unsigned>(pp, s - 1); LSTAMP(tlan, s - 2); pend = false; continue; }
      __builtin_amdgcn_s_sleep(LSLEEP);
      if (--budget < 0) break;
    }
    LSTAMP(tiss, s);
    const unsigned dst = smem_base + L_RING + (s & (NSLOT - 1)) * 16384u;
    const unsigned v0 = kind == 2u ? v_wide : v_raw, st = kind == 2u ? 7168u : 48128u;
    if (i >= PACEB && i < 18u && s >= 6u) {
      if (kind == 1u) { dma4_sc1(src[0], v_lin, dst); dma4_sc1(src[1], v_lin, dst + 4096); dma4_sc1(src[2], v_lin, dst + 8192); dma4_sc1(src[3], v_lin, dst + 12288); }
      else {
#pragma unroll
        for (int q = 0; q < 4; ++q) dma4p(src[q], v0, v0 + st, v0 + 2 * st, v0 + 3 * st, dst + q * 4096);
      }
    } else {
      if (kind == 1u) { dma4_sc1f(src[0], v_lin, dst); dma4_sc1f(src[1], v_lin, dst + 4096); dma4_sc1f(src[2], v_lin, dst + 8192); dma4_sc1f(src[3], v_lin, dst + 12288); }
      else {
#pragma unroll
        for (int q = 0; q < 4; ++q) dma4(src[q], v0, v0 + st, v0 + 2 * st, v0 + 3 * st, dst + q * 4096);
      }
    }
    if (pend) { wait_vm<16>(); vst<unsigned>(pp, s - 1); LSTAMP(tlan, s - 2); }
    LSTAMP(tis2, s);
    pend = true;
    if (s < 6u) {
      wait_vm<0>(); vst<unsigned>(pp, s + 1); LSTAMP(tlan, s); pend = false;
      __builtin_amdgcn_s_setprio(3);
      const unsigned a = tr_lane(lane) + s * 16384u;
      float c0 = 0.f, c1 = 0.f;
#pragma unroll
      for (int n = 0; n < 16; ++n) {
        const v4u f = __builtin_bit_cast(v4u, ldw(a + n * 1024, 512));
        c0 = dot2(f.x, ONES, c0); c1 = dot2(f.y, ONES, c1); c0 = dot2(f.z, ONES, c0); c1 = dot2(f.w, ONES, c1);
      }
      const float t = sum_halves(c0 + c1);
      vst<v2u>(L_CSF + 2 * 512 + (lane & 31) * 16 + lw * 8, split3(t));
      lds_add(pa, L_CSC - L_RDY + 4, 1);
      __builtin_amdgcn_s_setprio(LPRIO);
    }
    if (s + 2 >= NT) { wait_vm<0>(); vst<unsigned>(pp, s + 1); }
  }
}
DI void server(const Params& P, int p, int m, int sw) {
  const int lane = fresh_lane();
  unsigned epoch;
  asm volatile("s_load_dword %0, %1, 0x0" : "=s"(epoch) : "s"(P.ws) : "memory");
  const unsigned pa = lane == 0 ? L_RDY : L_DUMMY + lane * 4;
  const rsrc_t rs = __builtin_amdgcn_make_buffer_rsrc((void*)P.ws, 0, WS_END, 0x00020000);
  CW c;
  c.lane = lane; c.ws = P.ws; c.budget = BUDGET;
  asm volatile("s_waitcnt lgkmcnt(0)" : "+s"(epoch) :: "memory");
  c.tag = epoch + 1u;
  const int co = WS_CSP + (m * 2 + sw) * 16 * 768;
  STAMP(7);
  DRAIN();
  STAMP(10);
  lds_add(pa, L_RPD - L_RDY, 1);
  {
    const unsigned idx = (blockIdx.x * 2 + sw) * 4 + (lane & 3), line = (idx / 3u) % 576u;
    asm volatile("s_mov_b64 exec, 0xf\n\ts_nop 4\n\tglobal_atomic_or %0, %1, %2\n\ts_mov_b64 exec, -1" ::"v"(WS_FKV + line * 256u + (idx % 3u) * 64u), "v"(0u), "s"(uni(P.ws)) : "memory");
  }
  {
    {
      const unsigned b = blockIdx.x;
      const char* tb = sw ? P.wo : P.wd;
      const unsigned to = sw ? (b * 16u + (lane & 15)) * 128u : (b * 43u + min(lane, 42)) * 128u;
      asm volatile("s_mov_b32 m0, %2\n\ts_nop 3\n\tglobal_load_lds_dword %0, %1" :: "v"(to), "s"(uni(tb)), "s"((unsigned)(uintptr_t)(LAS char*)smem + L_DUMMY) : "memory");
    }
    while (rfl(vld<unsigned>(L_RPD)) < 2u && !spent(c)) __builtin_amdgcn_s_sleep(1);
  }
  for (unsigned layer = 0; layer < 2; ++layer) {
    __builtin_amdgcn_s_setprio(3);
    f32x4 tk[4], tz[4];
    while (rfl(vld<unsigned>(L_FCNT)) < layer * (GM(m) < 11 ? 33u : 29u) + 9u && !spent(c)) __builtin_amdgcn_s_sleep(8);
    for (;;) {
      const v4u fl = vld<v4u>(L_F1);
      if (min4(fl) > layer || spent(c)) break;
      __builtin_amdgcn_s_sleep(1);
    }
    STAMP(11 + 4 * layer);
#pragma unroll
    for (int u = 0; u < 4; ++u) { tk[u] = vld<f32x4>(L_KVS + u * 2048 + sw * 1024 + lane * 16); tz[u] = vld<f32x4>(L_ZS + u * 128 + (lane & 3) * 32 + sw * 16); }
    const int go = WS_KVP + ((layer * 16 + m) * 2 + sw) * 16 * KVW, fo = WS_FKV + ((layer * 16 + m) * 2 + sw) * 256;
    v4u sk, sz;
#pragma unroll
    for (int e = 0; e < 4; ++e) { sk[e] = f2u(fadd1(fadd1(tk[0][e], tk[1][e]), fadd1(tk[2][e], tk[3][e]))); sz[e] = f2u(fadd1(fadd1(tz[0][e], tz[1][e]), fadd1(tz[2][e], tz[3][e]))); }
    st16(rs, sk, lane * 16, go + p * KVW);
    st16(rs, sz, 1024 + (lane & 7) * 16, go + p * KVW);
    DRAIN();
    set_flag(c, fo + p * 4);
    STAMP(12 + 4 * layer);
    wait_flags(c, fo, 0xffffull);
    STAMP(13 + 4 * layer);
    v4u vt = ld16(rs, (lane & 15) * 16, WS_VOTE);
    const v4u zl = ld16(rs, 1024 + (lane & 15) * KVW + (lane >> 4) * 16, go);
    v4u g[16];
#pragma unroll
    for (int i = 0; i < 16; ++i) { SCHED(); g[i] = ld16(rs, lane * 16, go + i * KVW); }
    SCHED();
    const float z0 = row_sum(u2f(zl.x)), z1 = row_sum(u2f(zl.y)), z2 = row_sum(u2f(zl.z)), z3 = row_sum(u2f(zl.w));
    if ((lane & 15) == 0) vst<v2u>(L_ZR + (lane >> 4) * 16 + sw * 8, v2u{pk2(z0, z1), pk2(z2, z3)});
    f32x4 r = __builtin_bit_cast(f32x4, g[0]);
#pragma unroll
    for (int i = 1; i < 16; ++i) r += __builtin_bit_cast(f32x4, g[i]);
    vst<v2u>(L_KVR + lane * 16 + sw * 8, v2u{pk2(r.x, r.y), pk2(r.z, r.w)});
    while (__builtin_amdgcn_ballot_w64(vt.y != c.tag || vt.w != c.tag) != 0ull && !spent(c)) { CFENCE(); vt = ld16(rs, (lane & 15) * 16, WS_VOTE); }
    vst<unsigned>(L_CB, row_sum((float)((int)vt.x + (int)vt.z)) > 0.f ? 1u : 0u);
    lds_add(pa, L_F2 - L_RDY, 2);
    STAMP(14 + 4 * layer);
    __builtin_amdgcn_s_setprio(0);
    if (layer == 0) {
      v4u t[16];
#pragma unroll
      for (int i = 0; i < 16; ++i) t[i] = ld16(rs, min(lane, 47) * 16, co + i * 768);
      SCHED();
      f32x4 v = __builtin_bit_cast(f32x4, t[0]);
#pragma unroll
      for (int i = 1; i < 16; ++i) v += __builtin_bit_cast(f32x4, t[i]);
      if (lane < 48) {
#pragma unroll
        for (int e = 0; e < 4; ++e) vst<v2u>(L_CSF + 2048 + (lane * 4 + e) * 16 + sw * 8, split3(v[e]));
      }
      lds_add(pa, L_CSC - L_RDY + 8, 2); lds_add(pa, L_CSC - L_RDY + 12, 2); lds_add(pa, L_CSC - L_RDY + 16, 2);
    }
  }
}
template <int AT, bool TWO> DI void cs_hook(CW& c, int i, unsigned k, v4u& a, v4u& b) {
  if (i == AT) a.x = vld<unsigned>(L_CSC + k * 4);
  if (i == AT + 3) {
    wait_count(c, a.x, L_CSC + k * 4, TWO ? 4u : 2u);
    a = vld<v4u>(L_CSF + k * 1024 + (c.lane & 31) * 16);
    if (TWO) b = vld<v4u>(L_CSF + k * 1024 + 512 + (c.lane & 31) * 16);
  }
}

__global__ void __launch_bounds__(512, 2) fused_fwd(Params P) {
  const int tid = threadIdx.x; int lane = tid & 63; const int wv = __builtin_amdgcn_readfirstlane(tid >> 6);
  const int b = blockIdx.x;
  {
    const unsigned co = CODE_BYTES - 256u - min(((b >> 3) * 8 + wv) * 256u, CODE_BYTES - 256u) + lane * 4;
    unsigned long long pc;
    asm volatile("s_mov_b32 m0, %2\n\ts_getpc_b64 %0\n\tglobal_load_lds_dword %1, %0" : "=&s"(pc) : "v"(co), "s"((unsigned)(uintptr_t)(LAS char*)smem + L_DUMMY) : "memory");
  }
  int p, m;
  if (MAPMODE == 0) { p = (b & 7) * 2 + (b >> 7); m = (b >> 3) & 15; }
  else if (MAPMODE == 1) { const int x = b & 7, i = b >> 3; p = (x >> 1) * 4 + (i >> 3); m = (x & 1) * 8 + (i & 7); }
  else if (MAPMODE == 3) { const int x = b & 7, i = b >> 3; p = (x >> 2) * 8 + (i >> 2); m = (x & 3) * 4 + (i & 3); }
  else { const int x = b & 7, i = b >> 3; p = i >> 1; m = x * 2 + (i & 1); }
  if (tid < (L_END - L_RDY) / 4) vst<unsigned>(L_RDY + tid * 4, tid == 4 || tid == 5 ? tid - 1u : 0u);
  asm volatile("s_waitcnt lgkmcnt(0)\n\ts_barrier" ::: "memory");
  if (wv >= 4) __builtin_amdgcn_s_setprio(3);
  STAMP(21);
  if (b < 16 && (wv & 6) == 4) {
    const unsigned po = WS_PROBE + (b * 2 + (wv & 1)) * 128u + (lane & 1) * 64 + (lane >> 1) * 4096;
    asm volatile("s_mov_b64 exec, 0xff\n\ts_nop 4\n\tglobal_atomic_or %0, %1, %2\n\ts_mov_b64 exec, -1" ::"v"(po), "v"(0u), "s"(uni(P.ws)) : "memory");
  }
  const unsigned smem_base = (unsigned)(uintptr_t)(LAS char*)smem;
  int w = wv & 3;
  unsigned xv[4];
  const char* xbase = (wv < 4 ? P.x : P.wqkv) + (unsigned)sopaque(wv < 4 ? (4 * p + w) * 32768 : ((w >> 1) + 1) * 1024 + m * 64);
  auto xbuf = [&](int q) -> unsigned { const int j = q % 5; return (j == 0 ? L_SCR : j == 1 ? 7 * 16384 : j == 2 ? 6 * 16384 : j == 3 ? 4 * 16384 : 5 * 16384) + w * 4096; };
  {
#pragma unroll
    for (int k = 0; k < 4; ++k) {
      const unsigned a = (8 * k + (lane >> 3)) * 1024 + (((lane & 7) ^ ((4 * k + (lane >> 4)) & 7)) * 16) - k * 1024;
      const unsigned c = (lane >> 2) * 3072 + (lane & 3) * 16 + k * 48128;
      xv[k] = wv < 4 ? a : c;
    }
    const unsigned cw = wv < 4, stride = sopaque(cw ? 128 : 64 * 3072), r0 = ROT(m) + (cw ? 0 : (w & 1));
    dma4(xbase + (r0 & 7) * stride, xv[0], xv[1], xv[2], xv[3], smem_base + (cw ? xbuf(0) : w * 4096));
    if (!cw) {
      const unsigned pa = smem_base + (lane == 0 ? L_RDY : L_DUMMY + lane * 4), a = tr_lane(lane) + w * 4096u;
      float c0 = 0.f, c1 = 0.f;
      auto cs = [&](int j) __attribute__((always_inline)) {
#pragma unroll
        for (int n = 0; n < 4; ++n) {
          const v4u f = __builtin_bit_cast(v4u, ldw(a + j * 16384 + n * 1024, 512));
          c0 = dot2(f.x, ONES, c0); c1 = dot2(f.y, ONES, c1); c0 = dot2(f.z, ONES, c0); c1 = dot2(f.w, ONES, c1);
        }
        PIN(c0); PIN(c1);
      };
      STAMP(2);
      asm volatile("s_waitcnt vmcnt(0)\n\tds_add_u32 %0, %1" ::"v"(pa), "v"(1u) : "memory");
      STAMP(3);
      dma4(xbase + ((2 + r0) & 7) * stride, xv[0], xv[1], xv[2], xv[3], smem_base + 1 * 16384 + w * 4096);
      cs(0);
      asm volatile("s_waitcnt vmcnt(0)\n\tds_add_u32 %0, %1 offset:4" ::"v"(pa), "v"(1u) : "memory");
      STAMP(4);
      dma4(xbase + ((4 + r0) & 7) * stride, xv[0], xv[1], xv[2], xv[3], smem_base + 2 * 16384 + w * 4096);
      cs(1);
      asm volatile("s_waitcnt vmcnt(0)\n\tds_add_u32 %0, %1 offset:8" ::"v"(pa), "v"(1u) : "memory");
      STAMP(5);
      dma4(xbase + ((6 + r0) & 7) * stride, xv[0], xv[1], xv[2], xv[3], smem_base + 3 * 16384 + w * 4096);
      cs(2);
      asm volatile("s_waitcnt vmcnt(0)\n\tds_add_u32 %0, %1 offset:12" ::"v"(pa), "v"(1u) : "memory");
      STAMP(6);
      const int ks = 2 * p + (w & 1), r2 = GM(m) < 11 ? 2 : 1;
      v4u rp[9];
      if (w >= 2) {
        __builtin_amdgcn_s_setprio(RPP);
        const GAS char* sq = (const GAS char*)P.wqkv + m * 64 + ks * (16 * 3072) + xv[0];
        const unsigned vg = GM(m) * 64 + ks * (16 * 2752) + (lane >> 2) * 2752 + (lane & 3) * 16;
        const GAS char* sg = (const GAS char*)P.wg + vg; const GAS char* su = (const GAS char*)P.wu + vg;
        rp[0] = *(const GAS v4u*)(sg); rp[1] = *(const GAS v4u*)(su); rp[2] = *(const GAS v4u*)(sg + 1024); rp[3] = *(const GAS v4u*)(su + 1024);
        rp[4] = *(const GAS v4u*)(sg + r2 * 1024); rp[5] = *(const GAS v4u*)(su + r2 * 1024);
        rp[6] = *(const GAS v4u*)(sq + 1024); rp[7] = *(const GAS v4u*)(sq + 2048); rp[8] = *(const GAS v4u*)(sq);
        SCHED();
        __builtin_amdgcn_s_setprio(3);
      }
      cs(3);
      const float t = sum_halves(c0 + c1);
      vst<v2u>(L_CSF + (w >> 1) * 512 + (lane & 31) * 16 + (w & 1) * 8, split3(t));
      asm volatile("ds_add_u32 %0, %1 offset:%c2" ::"v"(pa), "v"(1u), "i"(L_CSC - L_RDY) : "memory");
      if (sopaque(w) >= 2) {
        const rsrc_t rs = __builtin_amdgcn_make_buffer_rsrc((void*)P.ws, 0, WS_END, 0x00020000);
        const int dg = (GM(m) * 32 + ks) * 1024, dq = WS_RQ + (m * 96 + ks) * 1024;
        const unsigned SCX = L_CSF + 3072 + (w & 1) * 1024;
        v4u f[8];
#pragma unroll
        for (int u = 0; u < 8; ++u) {
          vst<v4u>(SCX + lane * 16, rp[u]);
          CFENCE();
          f[u] = __builtin_bit_cast(v4u, ldw(SCX + tr_lane(lane), 512));
          CFENCE();
        }
        st16(rs, f[0], lane * 16, WS_RG + dg); st16(rs, f[1], lane * 16, WS_RU + dg);
        st16(rs, f[2], lane * 16, WS_RG + dg + 16 * 32768); st16(rs, f[3], lane * 16, WS_RU + dg + 16 * 32768);
        st16(rs, f[4], lane * 16, WS_RG + dg + r2 * (16 * 32768)); st16(rs, f[5], lane * 16, WS_RU + dg + r2 * (16 * 32768));
        st16(rs, f[6], lane * 16, dq); st16(rs, f[7], lane * 16, dq + 32768); st16(rs, rp[8], lane * 16, dq + 65536);
        STAMP(8);
        float cs6[6];
#pragma unroll
        for (int u = 0; u < 6; ++u) cs6[u] = dot2(f[u].x, ONES, dot2(f[u].y, ONES, dot2(f[u].z, ONES, dot2(f[u].w, ONES, 0.f))));
#pragma unroll
        for (int u = 0; u < 3; ++u) {
          const auto r = __builtin_amdgcn_permlane32_swap(f2u(cs6[2 * u]), f2u(cs6[2 * u + 1]), false, false);
          __builtin_amdgcn_raw_buffer_store_b32(f2u(u2f(r[0]) + u2f(r[1])), rs, lane * 4 + u * 256, WS_CSP + ((m * 2 + (w & 1)) * 16 + p) * 768, 16);
        }
        STAMP(9);
      }
    }
  }
  STAMPV(0, t00);
  STAMP(61);
  if (sopaque(wv) >= 6) {
    server(P, p, m, wv - 6);
    STAMP(1); STAMP_END(); return; }
  if (sopaque(wv) >= 4) { loader(P, p, m, wv - 4); STAMP(1); STAMP_END(); return; }
  __builtin_amdgcn_s_setprio(2);

  unsigned epoch;
  asm volatile("s_load_dword %0, %1, 0x0" : "=s"(epoch) : "s"(P.ws) : "memory");
  v4u B[32];
  auto xdma = [&](int q) __attribute__((always_inline)) { dma4(xbase + ((q + ROT(m)) & 7) * 128, xv[0], xv[1], xv[2], xv[3], smem_base + xbuf(q)); };
  xdma(1);
  const rsrc_t rs = __builtin_amdgcn_make_buffer_rsrc((void*)P.ws, 0, WS_END, 0x00020000);
  asm volatile("s_waitcnt lgkmcnt(0)" : "+s"(epoch) :: "memory");
  STAMP(19);
  CW c;
  c.s = 0; c.pos = 0; c.perm = 4; c.lane_tr = tr_lane(lane); c.fa = lane == 0 ? L_FCNT + w * 4 : L_DUMMY + lane * 4;
  c.budget = BUDGET; c.lane = lane; c.w = w; c.ws = P.ws; c.tag = epoch + 1u;
  int h, t32, tbq, tbw, kh, cb;
  int sdn = 0;
#define FRESH() do { lane = opaque(lane); h = lane >> 5; t32 = lane & 31; c.lane = lane; p = sopaque(p); m = sopaque(m); w = sopaque(w); c.w = w; \
    tbq = layer * 64 + 4 * p + w; kh = w >> 1; tbw = layer * 64 + 4 * p + 2 * (m >> 3) + (w & 1); cb = 2 * (m & 7) + kh; } while (0)
  unsigned R[8];

  int layer = 0;
    f32x16 aK = {}, aV = {};
    float la; unsigned mf;
    f32x4 ar[4];
    v4u ck, cv;
    FRESH();
    auto rows_ab = [&]() __attribute__((always_inline)) {
      vst<float>(L_SA + w * 128 + t32 * 4, la);
#pragma unroll
      for (int g = 0; g < 4; ++g) ar[g] = vld<f32x4>(L_SA + w * 128 + (8 * g + 4 * h) * 4);
    };
    {
      float s1 = 0.f, s2 = 0.f, s3 = 0.f, s4 = 0.f;
      const unsigned xl = t32 * 128 + 8 * h, xf = (t32 >> 1) & 7;
      auto hook = [&](int i, int hf) __attribute__((always_inline)) {
        const int n = i + 2;
        if (n >= 0 && n < 32 && hf == 0) {
          const int q = n / 4, j = n % 4;
          if (j == 0) {
            if (q == 0) { STAMPB(39); wait_vm<4 * (XUP - 1)>(); STAMPB(40); }
            if (q == 1 || q == 2 || q == 3) wait_vm<12>();
            if (q == 4) wait_vm<12>();
            if (q == 5) wait_vm<8>();
            if (q == 6) wait_vm<4>();
            if (q == 7) wait_vm<0>();
          }
          const v2u lo = vld<v2u>(xbuf(q) + xl + (((2 * j) ^ xf) * 16)), hi = vld<v2u>(xbuf(q) + xl + (((2 * j + 1) ^ xf) * 16));
          B[n] = v4u{lo.x, lo.y, hi.x, hi.y};
        }
        if (hf == 1) {
          if (i == -1) xdma(2);
          if (i == 0) xdma(3);
          if (i == 1) xdma(4);
          if (i == 3 || i == 7 || i == 11) xdma(5 + i / 4);
          if (i >= 0 && i % 4 == 3) {
#pragma unroll
            for (int u = i - 3; u <= i; ++u) {
              const v4u f = B[u];
              s1 = dot2(f.x, ONES, s1); s2 = dot2(f.x, f.x, s2); s3 = dot2(f.y, ONES, s3); s4 = dot2(f.y, f.y, s4);
              s1 = dot2(f.z, ONES, s1); s2 = dot2(f.z, f.z, s2); s3 = dot2(f.w, ONES, s3); s4 = dot2(f.w, f.w, s4);
            }
            PIN(s1); PIN(s2); PIN(s3); PIN(s4);
          }
          cs_hook<26, true>(c, i, 0, ck, cv);
          if (n == 15 || n == 19) permit(c, n / 4 + 2);
          if (i == 30) permit(c, 10);
        }
      };
      gemm_pair<true>(c, B, aK, aV, hook);
      ln_factors(sum_halves(s1 + s3), sum_halves(s2 + s4), la, mf, h != 0);
      rows_ab();
    }
  for (; layer < 2; ++layer) {
    const bf16x8 mfr = asb(v4u{mf, mf, mf, mf});
    aK = mfma(mfr, asb(ck), aK);
    aV = mfma(mfr, asb(cv), aV);
    STAMPB(2 + 20 * layer);
    FRESH();
    {
      unsigned pkk[8], pkv[8];
#pragma unroll
      for (int j = 0; j < 8; ++j) {
        const f32x2 a = (j & 1) ? f32x2{ar[j >> 1].z, ar[j >> 1].w} : f32x2{ar[j >> 1].x, ar[j >> 1].y};
        pkk[j] = phi2(pair(aK, j), a);
        pkv[j] = pk2(pair(aV, j) * a);
      }
      const bf16x8 k0 = asb(v4u{pkk[0], pkk[1], pkk[2], pkk[3]}), k1 = asb(v4u{pkk[4], pkk[5], pkk[6], pkk[7]});
      const bf16x8 v0 = asb(v4u{pkv[0], pkv[1], pkv[2], pkv[3]}), v1 = asb(v4u{pkv[4], pkv[5], pkv[6], pkv[7]});
      const bf16x8 one = asb(v4u{ONES, ONES, ONES, ONES});
      f32x16 kv = {}, zz = {};
      kv = mfma(k0, v0, kv); zz = mfma(k0, one, zz); kv = mfma(k1, v1, kv); zz = mfma(k1, one, zz);
      const bool hd = (lane & 16) != 0;
      f32x4 q0, q1, z0, z1;
#pragma unroll
      for (int j = 0; j < 4; ++j) { q0[j] = sel(hd, kv[8 + j], kv[j]); q1[j] = sel(hd, kv[12 + j], kv[4 + j]); }
      vst<f32x4>(L_KVS + w * 2048 + lane * 16, q0);
      vst<f32x4>(L_KVS + w * 2048 + 1024 + lane * 16, q1);
#pragma unroll
      for (int j = 0; j < 4; ++j) { z0[j] = sel(hd, zz[8 + j], zz[j]); z1[j] = sel(hd, zz[12 + j], zz[4 + j]); }
      vst<f32x4>(L_ZS + w * 128 + (lane >> 4) * 32, z0);
      vst<f32x4>(L_ZS + w * 128 + (lane >> 4) * 32 + 16, z1);
      post(c, L_F1, layer + 1);
    }
    STAMPB(3 + 20 * layer);
    unsigned pq[8];
    {
      f32x16 aQ = {};
      v4u c0;
      auto hook = [&](int i) __attribute__((always_inline)) {
        if (i == 20) {
          if (layer == 0) {
            const GAS char* xr = (const GAS char*)P.x + (size_t)(tbw * 32 + t32) * 1024 + cb * 64 + h * 8;
#pragma unroll
            for (int g = 0; g < 4; ++g) { const v2u v = *(const GAS v2u*)(xr + g * 16); R[2 * g] = v.x; R[2 * g + 1] = v.y; }
          }
        }
        cs_hook<24, false>(c, i, 1, c0, c0);
      };
      gemm_single(c, B, aQ, hook);
      aQ = mfma(asb(c0), mfr, aQ);
#pragma unroll
      for (int j = 0; j < 8; ++j) { pq[j] = phi2(pair(aQ, j), f32x2{la, la}); PIN(pq[j]); }
    }
    STAMPB(4 + 20 * layer);
    {
      FRESH();
      f32x16 aM = {}, aO = {};
      Wide<16> d;
      const unsigned s0 = c.s;
      wide_steps<16, -16, 0>(c, d, B, aM, aO, kh, NoHook());
      STAMPB(54);
      v4u fk, fz; unsigned cbv;
      for (;;) {
        const unsigned fl = vld<unsigned>(L_F2);
        fk = vld<v4u>(L_KVR + lane * 16); fz = vld<v4u>(L_ZR + (lane >> 4) * 16); cbv = vld<unsigned>(L_CB);
        if (rfl(fl) >= 4u * layer + 4u || spent(c)) break;
      }
      STAMPB(5 + 20 * layer);
      sdn = (p >> 3) ^ rfl(cbv);
      permit(c, s0 + FPN);
      const bool hd = (lane & 16) != 0;
      const v4u zero = {0u, 0u, 0u, 0u};
      const bf16x8 p0 = asb(v4u{pq[0], pq[1], pq[2], pq[3]}), p1 = asb(v4u{pq[4], pq[5], pq[6], pq[7]});
      f32x16 num = {}, den = {};
      den = mfma(asb(hd ? zero : fz), p0, den); den = mfma(asb(hd ? fz : zero), p1, den);
      num = mfma(asb(hd ? zero : fk), p0, num); num = mfma(asb(hd ? fk : zero), p1, num);
      const float i0 = __builtin_amdgcn_rcpf(den[0] + 1e-6f), i1 = __builtin_amdgcn_rcpf(den[8] + 1e-6f);
      const int os = a_act(WS_O, tbq, sdn) + fr(2 * m);
      st16(rs, v4u{pk2(pair(num, 0) * i0), pk2(pair(num, 1) * i0), pk2(pair(num, 2) * i0), pk2(pair(num, 3) * i0)}, lane * 16, os);
      st16(rs, v4u{pk2(pair(num, 4) * i1), pk2(pair(num, 5) * i1), pk2(pair(num, 6) * i1), pk2(pair(num, 7) * i1)}, lane * 16 + 1024, os);
      aM = unpack(R); PIN(aM);
      int oo = a_act(WS_O, tbw, sdn) + kh * 4096, xs = AX(WS_X1, tbw) + FX(2 * cb), ss = a_stat(WS_ST1, tbw, sdn) + cb * 256;
      SPIN(oo); SPIN(xs); SPIN(ss);
      DRAIN();
      set_flag(c, a_flag(WS_FO, tbq, sdn) + m * 4);
      STAMPB(6 + 20 * layer);
      wait_flags(c, a_flag(WS_FO, tbw, sdn), kh ? 0xccccull : 0x3333ull);
      STAMPB(7 + 20 * layer);
#pragma unroll
      for (int i = 0; i < AW; ++i) B[i] = ld16(rs, lane * 16 + (i & 3) * 1024, oo + (i >> 2) * 16384);
      SCHED();
      auto hook = [&](int i, int hf) __attribute__((always_inline)) {
        const int n = i + AW;
        if (hf == 0) return;
        if (n < 16) B[n] = ld16(rs, lane * 16 + (n & 3) * 1024, oo + (n >> 2) * 16384);
        if (n == WPN) permit(c, s0 + 12);
      };
      wide_steps<16, 0, 16>(c, d, B, aM, aO, kh, hook);
      STAMPB(8 + 20 * layer);
      finish_wide<16>(c, d, B, aM, aO, R, 2 * layer + 1);
      publish_x(c, rs, R, xs, ss);
      vst<v4u>(L_KVS + w * 2048 + lane * 16, v4u{R[0], R[1], R[2], R[3]});
      vst<v4u>(L_KVS + w * 2048 + 1024 + lane * 16, v4u{R[4], R[5], R[6], R[7]});
    }
    unsigned pf;
    f32x16 aM = {}, aO = {};
    Wide<4> d;
    unsigned sd;
    int fd, hd0, fl2, xs2, ss2; unsigned long long mk;
    auto ldh = [&](int n) __attribute__((always_inline)) {
      if (n < 32) B[n % 16] = ld16(rs, lane * 16 + (n & 3) * 1024, hd0 + (((n >> 2) & 4) | (((n >> 2) + ROT(m)) & 3)) * HS);
      else if (n < 40) B[n % 16] = ld16(rs, lane * 16 + (n & 3) * 1024, hd0 + (n >> 2) * HS);
      else B[n % 16] = ld16(rs, lane * 16 + (n & 3) * 1024, hd0 + 10 * HS - kh * 1024);
    };
    {
      FRESH();
      f32x16 G0 = {}, U0 = {}, G1 = {}, U1 = {};
      Pipe q;
      const unsigned s0 = c.s;
      pair_steps<false, -PD, 0, 64, false>(c, q, B, G0, U0, NoHook());
      const int xo = AX(WS_X1, tbq);
      int xa = xo + FX(4 * ROT(m)), xb = xo + FX(4 * ((ROT(m) + 1) & 7));
      SPIN(xa); SPIN(xb);
      DRAIN();
      set_flag(c, a_flag(WS_FX1, tbw, sdn) + cb * 4);
      STAMPB(9 + 20 * layer);
      wait_flags(c, a_flag(WS_FX1, tbq, sdn), 0xffffull);
      STAMPB(10 + 20 * layer);
      Stats sv;
#pragma unroll
      for (int i = 0; i < 8; ++i) B[i] = ld16(rs, lane * 16 + (i & 3) * 1024, i < 4 ? xa : xb);
      SCHED();
      float a2; unsigned gm;
      v4u cg, cu;
      unsigned hp[8];
      const int ho = AHH(tbq) + FH(2 * GM(m)), fo = a_flag(WS_FH, tbq, sdn) + GM(m) * 4;
      float hv;
      fd = a_flag(WS_FH, tbw, sdn); mk = kh ? 0xccccull : 0x3333ull; hd0 = AHH(tbw) + kh * 4096;
      fl2 = fo + 128;
      auto piece = [&](int e, const f32x16& G, const f32x16& U) __attribute__((always_inline)) {
        float g;
        if (e & 1) asm volatile("v_max_f32 %1, 0, %2\n\tv_mul_f32 %0, %3, %4\n\tv_mul_f32 %1, %1, %0\n\tv_cvt_pk_bf16_f32 %0, %5, %1" : "=&v"(hp[e >> 1]), "=&v"(g) : "v"(G[e]), "v"(U[e]), "v"(a2), "v"(hv));
        else asm volatile("v_max_f32 %1, 0, %2\n\tv_mul_f32 %0, %3, %4\n\tv_mul_f32 %0, %1, %0" : "=&v"(hv), "=&v"(g) : "v"(G[e]), "v"(U[e]), "v"(a2));
      };
      auto tail = [&](const f32x16& G, const f32x16& U) __attribute__((always_inline)) {
#pragma unroll
        for (int j = 0; j < 8; ++j) {
          const f32x2 g = {fmaxf(G[2 * j], 0.f), fmaxf(G[2 * j + 1], 0.f)};
          hp[j] = pk2(g * (pair(U, j) * a2));
        }
      };
      auto store_h = [&](int r, int half) __attribute__((always_inline)) { st16(rs, v4u{hp[4 * half], hp[4 * half + 1], hp[4 * half + 2], hp[4 * half + 3]}, lane * 16 + half * 1024, ho + r * (4 * HS)); };
      auto correct = [&](f32x16& G, f32x16& U) __attribute__((always_inline)) {
        const bf16x8 f = asb(v4u{gm, gm, gm, gm});
        G = mfma(asb(cg), f, G); U = mfma(asb(cu), f, U);
      };
      auto shadow = [&](int i, int hf, int r, const f32x16& G, const f32x16& U) __attribute__((always_inline)) {
        constexpr int at[16] = {3, 5, 7, 9, 12, 13, 14, 15, 17, 19, 21, 23, 25, 28, 29, 30};
#pragma unroll
        for (int e = 0; e < 16; ++e) if (2 * i + hf == at[e]) piece(e, G, U);
        if (2 * i + hf == 31) store_h(r, 0);
        if (2 * i + hf == 33) store_h(r, 1);
        if (hf == 0 && i == 25) { if (r == 0) DRAIN(); else wait_vm<8>(); set_flag(c, fo + r * 64); }
        if (hf == 1) cs_hook<25, true>(c, i, 3 + r, cg, cu);
        if (r == 1) {
          if (hf == 0 && i == 8) pf = poll_issue(c, fd, mk);
          if (hf == 0 && i == 17) { poll_done<2>(c, pf, fd, mk); if (layer) STAMPB(51); }
          if (hf == 1 && i >= 17) ldh(i - 17);
          if (hf == 0 && i == 30) pf = poll_issue(c, fd, mk << 16);
        }
      };
      pair_steps<false, 0, 32, 64, false>(c, q, B, G0, U0, [&](int i, int hf) __attribute__((always_inline)) {
        const int n = i + AH;
        stats_hook(c, sv, 2 * i + hf, rs, a_stat(WS_ST1, tbq, sdn), a2, gm);
        if (hf == 0) return;
        if (n < 32) B[n] = ld16(rs, lane * 16 + (n & 3) * 1024, xo + FX(4 * (((n >> 2) + ROT(m)) & 7)));
        if (n == GPN) permit(c, s0 + (GM(m) < 11 ? 23 : 19));
        if (i == 26) { a2 = fmul1(a2, a2); PIN(a2); }
        cs_hook<25, true>(c, i, 2, cg, cu);
      });
      correct(G0, U0);
      STAMPB(11 + 20 * layer);
      pair_steps<false, 32, 64, 96, false>(c, q, B, G1, U1, [&](int i, int hf) __attribute__((always_inline)) { shadow(i, hf, 0, G0, U0); });
      correct(G1, U1);
      STAMPB(12 + 20 * layer);
      pf = ~c.tag;
      if (sopaque(GM(m)) >= 11) {
        tail(G1, U1);
        store_h(1, 0); store_h(1, 1);
        DRAIN();
        set_flag(c, fo + 64);
        wait_flags(c, fd, mk);
      }
      if (sopaque(GM(m)) < 11) {
        G0 = f32x16{}; U0 = f32x16{};
        pair_steps<false, 64, 96, 96, false>(c, q, B, G0, U0, [&](int i, int hf) __attribute__((always_inline)) { shadow(i, hf, 1, G1, U1); });
        correct(G0, U0);
        tail(G0, U0);
        store_h(2, 0); store_h(2, 1);
        STAMPB(13 + 20 * layer);
      }
      if (sopaque(GM(m)) >= 11) {
#pragma unroll
        for (int i = 0; i < 15; ++i) ldh(i);
      }
      sd = c.s;
      const v4u r0 = vld<v4u>(L_KVS + w * 2048 + lane * 16), r1 = vld<v4u>(L_KVS + w * 2048 + 1024 + lane * 16);
      wide_steps<43, -4, 0>(c, d, B, aM, aO, kh, NoHook());
      R[0] = r0.x; R[1] = r0.y; R[2] = r0.z; R[3] = r0.w; R[4] = r1.x; R[5] = r1.y; R[6] = r1.z; R[7] = r1.w;
      aM = unpack(R);
    }
    STAMPB(14 + 20 * layer);
    {
      unsigned pf2;
      auto hook = [&](int i, int hf) __attribute__((always_inline)) {
        if (hf == 0) {
          if (i == FS) { wait_vm<FS>(); set_flag(c, fl2); }
          if (i == PI) pf2 = poll_issue(c, fd, 0x7ffull << 32);
          return;
        }
        if (i == 1) { poll_done<5>(c, pf, fd, mk << 16); if (layer) STAMPB(47); }
        if (i == 19 && layer) STAMPB(48);
        if (i == 31 && layer) STAMPB(50);
        if (i + 15 < 32) ldh(i + 15);
        if (i == PC) { poll_done<(PI < 17 ? 17 - PI : 0)>(c, pf2, fd, 0x7ffull << 32); if (layer) STAMPB(49); }
        if (PC == 20) {
          if (i >= 20 && i <= 23) { ldh(2 * i - 8); ldh(2 * i - 7); }
          if (i >= 24 && i <= 26) ldh(i + 16);
        } else {
          if (i >= PC && i < PC + 5) { ldh(32 + 2 * (i - PC)); ldh(33 + 2 * (i - PC)); }
          if (i == PC + 5) ldh(42);
        }
        if (i == PKVAT) permit(c, sd + 11 + PKV);
        if (i == 34) { xs2 = AX(WS_X2, tbw) + FX(2 * cb); ss2 = a_stat(WS_ST2, tbw, sdn) + cb * 256; SPIN(xs2); SPIN(ss2); }
      };
      wide_steps<43, 0, 43>(c, d, B, aM, aO, kh, hook);
      STAMPB(17 + 20 * layer);
      finish_wide<43>(c, d, B, aM, aO, R, 2 * layer + 2);
    }
#pragma unroll
    for (int i = 0; i < 32; ++i) asm volatile("" : "=v"(B[i]));
    asm volatile("" : "=v"(aK), "=v"(aV), "=v"(la), "=v"(mf), "=v"(ck), "=v"(cv));
    asm volatile("" : "=v"(ar[0]), "=v"(ar[1]), "=v"(ar[2]), "=v"(ar[3]));
    if (layer == 0) {
      publish_x(c, rs, R, xs2, ss2);
      FRESH();
      aK = f32x16{}; aV = f32x16{};
      Pipe q;
      const unsigned s0 = c.s;
      pair_steps<true, -PD, 0, 32, false>(c, q, B, aK, aV, NoHook());
      const int xo = AX(WS_X2, tbq);
      int xa = xo + FX(4 * ROT(m)), xb = xo + FX(4 * ((ROT(m) + 1) & 7));
      SPIN(xa); SPIN(xb);
      DRAIN();
      set_flag(c, a_flag(WS_FX2, tbw, sdn) + cb * 4);
      STAMPB(18);
      wait_flags(c, a_flag(WS_FX2, tbq, sdn), 0xffffull);
      STAMPB(20);
      Stats sv;
#pragma unroll
      for (int i = 0; i < 8; ++i) B[i] = ld16(rs, lane * 16 + (i & 3) * 1024, i < 4 ? xa : xb);
      SCHED();
      auto hook = [&](int i, int hf) __attribute__((always_inline)) {
        const int n = i + AH;
        stats_hook(c, sv, 2 * i + hf, rs, a_stat(WS_ST2, tbq, sdn), la, mf);
        if (hf == 0) {
          if (i == 25) vst<float>(L_SA + w * 128 + t32 * 4, la);
          if (i == 26) { ar[0] = vld<f32x4>(L_SA + w * 128 + 4 * h * 4); ar[1] = vld<f32x4>(L_SA + w * 128 + (8 + 4 * h) * 4); }
          if (i == 27) { ar[2] = vld<f32x4>(L_SA + w * 128 + (16 + 4 * h) * 4); ar[3] = vld<f32x4>(L_SA + w * 128 + (24 + 4 * h) * 4); }
          return;
        }
        if (n < 32) B[n] = ld16(rs, lane * 16 + (n & 3) * 1024, xo + FX(4 * (((n >> 2) + ROT(m)) & 7)));
        if (n == KPN) permit(c, s0 + KVP2);
        cs_hook<26, true>(c, i, 0, ck, cv);
      };
      pair_steps<true, 0, 32, 32, false>(c, q, B, aK, aV, hook);
    }
  }
  {
    layer = 0;
    FRESH();
    const rsrc_t ro = __builtin_amdgcn_make_buffer_rsrc((void*)P.out, 0, 2048 * 1024, 0x00020000);
#pragma unroll
    for (int g = 0; g < 2; ++g) {
      const auto r0 = __builtin_amdgcn_permlane32_swap(R[4 * g], R[4 * g + 2], false, false), r1 = __builtin_amdgcn_permlane32_swap(R[4 * g + 1], R[4 * g + 3], false, false);
      __builtin_amdgcn_raw_buffer_store_b128(v4u{r0[0], r1[0], r0[1], r1[1]}, ro, (tbw * 32 + t32) * 1024 + cb * 64 + g * 32 + h * 16, 0, OAUX);
    }
  }
  STAMPB(38);
  STAMP_END();
  STAMPV(42, (unsigned long long)(BUDGET - c.budget));
  if (b == 0 && w == 0 && lane == 0) *(GAS unsigned*)(P.ws + WS_EPOCH) = c.tag;
}
}

extern "C" void kernel_launch(void* const* d_in, const int* in_sizes, int n_in, void* d_out, int out_size, void* d_ws,
                              size_t ws_size, hipStream_t stream) {
  Params P;
  P.x = (const char*)d_in[0]; P.wqkv = (const char*)d_in[1]; P.wo = (const char*)d_in[2];
  P.wg = (const char*)d_in[4]; P.wu = (const char*)d_in[5]; P.wd = (const char*)d_in[6];
  P.out = (char*)d_out; P.ws = (char*)(((uintptr_t)d_ws + 0x1fffff) & ~(uintptr_t)0x1fffff);
  (void)hipFuncSetAttribute((const void*)fused_fwd, hipFuncAttributeMaxDynamicSharedMemorySize, LDS_BYTES);
  fused_fwd<<<256, 512, LDS_BYTES, stream>>>(P);
}
```

```cpp
#include <hip/hip_runtime.h>
#include <stdint.h>

#define GAS __attribute__((address_space(1)))
#define LAS __attribute__((address_space(3)))
#define DI __device__ __forceinline__
typedef unsigned v4u __attribute__((ext_vector_type(4)));
typedef unsigned v2u __attribute__((ext_vector_type(2)));
typedef float f32x16 __attribute__((ext_vector_type(16)));
typedef float f32x4 __attribute__((ext_vector_type(4)));
typedef float f32x2 __attribute__((ext_vector_type(2)));
typedef __bf16 bf16x8 __attribute__((ext_vector_type(8)));
typedef __bf16 bf16x2 __attribute__((ext_vector_type(2)));
typedef short s16x4 __attribute__((ext_vector_type(4)));

#define MAPMODE 1
#define LDEPTH 8
#define LPRIO 0
#define LSLEEP 1
#define AW 8
#define PKV 8
#define THIN 2
#define PD 2
#define PKVAT 16
#define THINB 18
#define KVP2 14
#define PACEB 10
#define XUP 2
#define RPP 0
#define FS 7
#define PI 13
#define PC 20
#define VCL 400
#define GPN 32
#define KPN 32
#define WPN 16
#define FPN 8
#define OAUX 0
#define AH 8

#define RM 1
#define RP 2
#define ROT(m) ((RM * (m) + RP * p) & 7)
#define GM(m) ((((m) & 7) << 1) | ((m) >> 3))
static_assert(AH == 8, "");
namespace {
constexpr int KB = 1024;
constexpr int WS_EPOCH = 0;
constexpr int WS_VOTE = 1024;
constexpr unsigned WS_PROBE = 192 * KB;
constexpr int WS_FKV = 16 * KB;
constexpr int WS_FO = WS_FKV + 32 * KB;
constexpr int WS_FX1 = WS_FO + 32 * KB;
constexpr int WS_FH = WS_FX1 + 32 * KB;
constexpr int WS_FX2 = WS_FH + 32 * KB;
constexpr int WS_ST1 = 256 * KB;
constexpr int WS_ST2 = WS_ST1 + 512 * KB;
constexpr int WS_CSP = WS_ST2 + 256 * KB;
constexpr int WS_O = 4096 * KB;
constexpr int WS_X1 = WS_O + 4096 * KB;
constexpr int WS_X2 = WS_X1 + 4096 * KB;
constexpr int WS_H = WS_X2 + 2048 * KB;
constexpr int WS_KVP = WS_H + 16384 * KB;
constexpr int WS_RQ = WS_KVP + 4096 * KB;
constexpr int WS_RG = WS_RQ + 1536 * KB;
constexpr int WS_RU = WS_RG + 43 * 32 * KB;
constexpr int WS_DBG = WS_RU + 43 * 32 * KB;
constexpr int WS_END = WS_DBG + 256 * 8 * 64 * 8;
constexpr int NSLOT = 8;
constexpr int L_RING = 0;
constexpr int L_SCR = NSLOT * 16 * KB;
constexpr int L_KVS = L_SCR + 16 * KB;
constexpr int L_CSF = L_KVS + 8 * KB;
constexpr int L_SA = L_CSF + 5120;
constexpr int L_KVR = L_SA + 512;
constexpr int L_ZR = L_KVR + 1024;
constexpr int L_ZS = L_ZR + 64;
constexpr int L_DUMMY = L_ZS + 512;
constexpr int L_RDY = L_DUMMY + 448;
constexpr int L_PL = L_RDY + 16;
constexpr int L_CSC = L_RDY + 32;
constexpr int L_FCNT = L_CSC + 32;
constexpr int L_F1 = L_FCNT + 16;
constexpr int L_F2 = L_F1 + 16;
constexpr int L_F3 = L_F2 + 16;
constexpr int L_RPD = L_F3 + 16;
constexpr int L_CB = L_RPD + 4;
constexpr int L_END = L_RPD + 16;
static_assert(L_END <= 160 * KB, "LDS");
constexpr int LDS_BYTES = (L_END + 15) / 16 * 16;
constexpr unsigned CODE_BYTES = 43264;
constexpr unsigned BUDGET = 1u << 18;

struct Params {
  const char* x; const char* wqkv; const char* wo; const char* wg; const char* wu; const char* wd;
  char* out; char* ws;
};

extern __shared__ __attribute__((aligned(16))) char smem[];
template <class T> DI LAS T* lp(unsigned off) { return (LAS T*)((LAS char*)smem + off); }
template <class T> DI T vld(unsigned off) { return *(volatile LAS T*)((LAS char*)smem + off); }
template <class T> DI void vst(unsigned off, T v) { *(volatile LAS T*)((LAS char*)smem + off) = v; }
DI unsigned rfl(unsigned v) { return __builtin_amdgcn_readfirstlane(v); }
DI const char* uni(const char* p) { const unsigned long long v = (unsigned long long)p; return (const char*)(((unsigned long long)rfl((unsigned)(v >> 32)) << 32) | rfl((unsigned)v)); }
#define SCHED() __builtin_amdgcn_sched_barrier(0)
#define CFENCE() asm volatile("" ::: "memory")
#define DRAIN() asm volatile("s_waitcnt vmcnt(0)" ::: "memory")
#define PIN(x) asm volatile("" : "+v"(x))

#define STAMP(i) do {} while (0)
#define STAMPV(i, v) do {} while (0)
#define STAMPB(i) do {} while (0)
#define STAMP_END() do {} while (0)

DI bf16x8 ldw(unsigned a, int second) {
  s16x4 lo = __builtin_amdgcn_ds_read_tr16_b64_v4i16(lp<s16x4>(a));
  s16x4 hi = __builtin_amdgcn_ds_read_tr16_b64_v4i16(lp<s16x4>(a + second));
  return __builtin_bit_cast(bf16x8, __builtin_shufflevector(lo, hi, 0, 1, 2, 3, 4, 5, 6, 7));
}
DI f32x16 mfma(bf16x8 a, bf16x8 b, f32x16 c) { return __builtin_amdgcn_mfma_f32_32x32x16_bf16(a, b, c, 0, 0, 0); }
DI bf16x8 asb(v4u v) { return __builtin_bit_cast(bf16x8, v); }
DI unsigned pk2(float lo, float hi) { bf16x2 r = {(__bf16)lo, (__bf16)hi}; return __builtin_bit_cast(unsigned, r); }
DI unsigned pk2(f32x2 v) { return pk2(v.x, v.y); }
DI float dot2(unsigned a, unsigned b, float c) { return __builtin_amdgcn_fdot2_f32_bf16(__builtin_bit_cast(bf16x2, a), __builtin_bit_cast(bf16x2, b), c, false); }
DI float u2f(unsigned u) { return __builtin_bit_cast(float, u); }
DI unsigned f2u(float f) { return __builtin_bit_cast(unsigned, f); }
DI float sel(bool c, float a, float b) { asm volatile("" : "+v"(a), "+v"(b)); return c ? a : b; }
DI int opaque(int v) { asm volatile("" : "+v"(v)); return v; }
DI int sopaque(int v) { asm volatile("" : "+s"(v)); return v; }
DI float blo(unsigned u) { return u2f(u << 16); }
DI float bhi(unsigned u) { return u2f(u & 0xffff0000u); }
DI float sum_halves(float v) {
  auto r = __builtin_amdgcn_permlane32_swap(f2u(v), f2u(v), false, false);
  float o; asm("v_add_f32 %0, %1, %2" : "=v"(o) : "v"(r[0]), "v"(r[1])); return o;
}
template <int CTRL> DI float dpp_add(float v) { return v + u2f(__builtin_amdgcn_update_dpp(0u, f2u(v), CTRL, 0xf, 0xf, false)); }
DI float row_sum(float v) { v = dpp_add<0x128>(v); v = dpp_add<0x124>(v); v = dpp_add<0x122>(v); return dpp_add<0x121>(v); }
DI f32x2 pair(const f32x16& a, int j) { return f32x2{a[2 * j], a[2 * j + 1]}; }
DI unsigned phi2(f32x2 x, f32x2 a) {
  const f32x2 t = x * a + 1e-6f;
  const f32x2 e2 = t * 1.4426950408889634f - 1.4426950408889634e-6f;
  const f32x2 e = {__builtin_amdgcn_fmed3f(__builtin_amdgcn_exp2f(e2.x), 0.f, 1.f), __builtin_amdgcn_fmed3f(__builtin_amdgcn_exp2f(e2.y), 0.f, 1.f)};
  const f32x2 mx = {fmaxf(t.x, 1e-6f), fmaxf(t.y, 1e-6f)};
  return pk2(mx + e);
}
constexpr unsigned ONES = 0x3f803f80u;
DI v2u split3(float t) {
  const float r1 = t - blo(pk2(t, t)), r2 = r1 - blo(pk2(r1, r1));
  return v2u{pk2(t, r1), pk2(r2, 0.f)};
}
DI float fadd1(float a, float b) { float r; asm("v_add_f32 %0, %1, %2" : "=v"(r) : "v"(a), "v"(b)); return r; }
DI float fmul1(float a, float b) { float r; asm("v_mul_f32 %0, %1, %2" : "=v"(r) : "v"(a), "v"(b)); return r; }

typedef __amdgpu_buffer_rsrc_t rsrc_t;
DI v4u ld16(rsrc_t rs, int voff, int soff) { return __builtin_amdgcn_raw_buffer_load_b128(rs, voff, soff, 16); }
DI v2u ld8(rsrc_t rs, int voff, int soff) { return __builtin_amdgcn_raw_buffer_load_b64(rs, voff, soff, 16); }
DI void st16(rsrc_t rs, v4u v, int voff, int soff) {
  __builtin_amdgcn_raw_buffer_store_b128(v, rs, voff, soff, 16);
  asm volatile("s_nop 1" ::"v"(v) : "memory");
}
DI void st8(rsrc_t rs, v2u v, int voff, int soff) { __builtin_amdgcn_raw_buffer_store_b64(v, rs, voff, soff, 16); }

DI int near(int off, int sd) { return off + (((sd ^ (off >> 21)) & 1) << 13); }
DI int tbi(int t) { return (t >> 6) * 32 + (t & 31); }
DI int a_act(int region, int t, int sd) { return near(region + tbi(t) * 65536, sd); }
DI int a_hid(int t, int sd) { return near(WS_H + tbi(t) * 262144, sd); }
DI int a_flag(int region, int t, int sd) { return near(region + (t >> 6) * 16384 + (t & 31) * 256, sd); }
DI int a_stat(int region, int t, int sd) { return near(region + (tbi(t) >> 1) * 16384 + (t & 1) * 4096, sd); }
DI int fr(int f) { return (f >> 3) * 16384 + (f & 7) * 1024; }
#define AX(r, t) a_act(r, t, sdn)
#define FX(f) fr(f)
#define AHH(t) a_hid(t, sdn)
#define FH(f) fr(f)
constexpr int HS = 16384;
struct CW {
  unsigned s, pos;
  unsigned lane_tr;
  unsigned fa;
  unsigned tag, perm;
  int budget;
  int lane, w;
  char* ws;
};
DI bool spent(CW& c) { c.budget -= 1; return c.budget < 0; }
DI unsigned min4(v4u v) { return rfl(min(min(v.x, v.y), min(v.z, v.w))); }
DI void wait_count(CW& c, unsigned have, unsigned addr, unsigned need) {
  if (__builtin_expect(rfl(have) < need, 0)) {
    for (;;) { if (rfl(vld<unsigned>(addr)) >= need || spent(c)) break; }
  }
}
DI void wait_slot(CW& c, unsigned have, unsigned t) {
  if (__builtin_expect(have < t, 0)) {
    for (;;) { const v2u v = vld<v2u>(L_PL); if (rfl(min(v.x, v.y)) >= t || spent(c)) break; }
  }
}
DI void post(CW& c, int word, unsigned v) { vst<unsigned>(c.fa + (word - L_FCNT), v); }
DI void release(CW& c) { c.s += 1; c.pos = (c.pos + 1) & (NSLOT - 1); post(c, L_FCNT, min(c.s + NSLOT, c.perm)); }
DI void permit(CW& c, unsigned v) { c.perm = v; post(c, L_FCNT, min(c.s + NSLOT, v)); }
DI void set_flag(CW& c, int off) {
  const char* base = uni(c.ws + off);
  asm volatile("s_mov_b64 exec, 1\n\ts_nop 4\n\tglobal_atomic_swap %0, %1, %2\n\ts_mov_b64 exec, -1" ::"v"(0u), "v"(c.tag), "s"(base) : "memory");
}
DI void wait_flags(CW& c, int off, unsigned long long mask) {
  unsigned f, bud = rfl(c.budget);
  const unsigned voff = c.lane * 4;
  const char* base = uni(c.ws + off);
  asm volatile(
      "s_mov_b64 exec, %5\n\ts_nop 4\n\t"
      "1:\n\t"
      "global_atomic_or %0, %2, %6, %3 sc0\n\t"
      "s_waitcnt vmcnt(0)\n\t"
      "v_cmp_ne_u32 vcc, %4, %0\n\t"
      "s_cbranch_vccz 2f\n\t"
      "s_sub_i32 %1, %1, 8\n\t"
      "s_cmp_ge_i32 %1, 0\n\t"
      "s_cbranch_scc1 1b\n\t"
      "2:\n\t"
      "s_mov_b64 exec, -1\n\t"
      : "=&v"(f), "+s"(bud) : "v"(voff), "s"(base), "s"(c.tag), "s"(mask), "v"(0u) : "memory", "vcc", "scc");
  c.budget = rfl(bud);
}
DI unsigned poll_issue(CW& c, int off, unsigned long long mask) {
  unsigned f = c.tag;
  const unsigned voff = c.lane * 4;
  const char* base = uni(c.ws + off);
  asm volatile("s_mov_b64 exec, %3\n\tv_not_b32 %0, %0\n\ts_nop 4\n\tglobal_atomic_or %0, %1, %4, %2 sc0\n\ts_mov_b64 exec, -1"
               : "+v"(f) : "v"(voff), "s"(base), "s"(mask), "v"(0u) : "memory");
  return f;
}
template <int N> DI void poll_done(CW& c, unsigned f, int off, unsigned long long mask) {
  asm volatile("s_waitcnt vmcnt(%c1)" : "+v"(f) : "i"(N) : "memory");
  if (__builtin_expect(__builtin_amdgcn_ballot_w64(f != c.tag) != 0ull, 0)) wait_flags(c, off, mask);
}

template <int V> struct IC { static constexpr int value = V; DI constexpr operator int() const { return V; } };
template <int I, int E, class F> DI void static_for(F&& f) { if constexpr (I < E) { f(IC<I>{}); static_for<I + 1, E>(f); } }
struct Pipe { bf16x8 wa[PD + 1], wb[PD + 1]; v2u pl; unsigned ad, ad2, adn, have, rel; };
#define SPIN(x) asm volatile("" : "+s"(x))
template <bool Q0> DI void slot_first(CW& c, Pipe& q) {
  if (Q0) wait_count(c, 0u, L_RDY, 4u); else { const v2u v = vld<v2u>(L_PL); wait_slot(c, rfl(min(v.x, v.y)), c.s + 1); }
  q.adn = c.lane_tr + c.pos * 16384u;
}
template <int K, bool NX, int CK, bool Q0 = false> DI void book(CW& c, Pipe& q) {
  if constexpr (CK && K == 0) { if (Q0) q.pl.x = vld<unsigned>(L_RDY + (c.s + 1) * 4); else q.pl = vld<v2u>(L_PL); }
  if constexpr (NX && K == 2) { q.adn = c.lane_tr + ((c.pos + 1) & (NSLOT - 1)) * 16384u; PIN(q.adn); }
  if constexpr (CK && K == 3) { q.have = rfl(Q0 ? q.pl.x : min(q.pl.x, q.pl.y)); SPIN(q.have); }
  if constexpr (CK && K == 4) { if (Q0) wait_count(c, q.have, L_RDY + (c.s + 1) * 4, 4u); else wait_slot(c, q.have, c.s + CK); }
  if constexpr (K == 5) { c.s += 1; c.pos = (c.pos + 1) & (NSLOT - 1); q.rel = min(c.s + NSLOT, c.perm); PIN(q.rel); }
  if constexpr (K == 6) post(c, L_FCNT, q.rel);
}
template <bool SWAP, int I0, int I1, int NEND, bool MORE, bool FR = true, class HOOK>
DI void pair_steps(CW& c, Pipe& q, v4u (&B)[32], f32x16& acc0, f32x16& acc1, HOOK hook) {
  const unsigned keep = c.lane_tr;
  if (FR) c.lane_tr = c.lane * 16;
  static_for<I0, I1>([&](auto ic) __attribute__((always_inline)) {
    constexpr int i = ic.value, n = i + PD;
    constexpr bool F = n < NEND, NX = F && (MORE || n + 8 - n % 8 < NEND);
    constexpr int CK = !NX ? 0 : !FR ? 1 : (n / 8) % 2 ? 2 : 0;
    if constexpr (n == 0) slot_first<!FR>(c, q);
    if constexpr (i >= 0) acc0 = SWAP ? mfma(asb(B[i % 32]), q.wa[i % (PD + 1)], acc0) : mfma(q.wa[i % (PD + 1)], asb(B[i % 32]), acc0);
    SCHED();
    if constexpr (F && n % 8 == 0) q.ad = q.adn;
    if constexpr (F) q.wa[n % (PD + 1)] = FR ? asb(vld<v4u>(q.ad + (n % 8) * 1024)) : ldw(q.ad + (n % 8) * 1024, 512);
    if constexpr (F && n % 8 >= 2) book<n % 8 - 2, NX, CK, !FR>(c, q);
    hook(IC<(i < 0 ? i : i % 32)>{}, IC<0>{});
    SCHED();
    if constexpr (i >= 0) acc1 = SWAP ? mfma(asb(B[i % 32]), q.wb[i % (PD + 1)], acc1) : mfma(q.wb[i % (PD + 1)], asb(B[i % 32]), acc1);
    SCHED();
    if constexpr (F) q.wb[n % (PD + 1)] = FR ? asb(vld<v4u>(q.ad + 8192 + (n % 8) * 1024)) : ldw(q.ad + 8192 + (n % 8) * 1024, 512);
    if constexpr (F && n % 8 == 7) book<6, NX, CK>(c, q);
    hook(IC<(i < 0 ? i : i % 32)>{}, IC<1>{});
    SCHED();
  });
  c.lane_tr = keep;
}
template <bool SWAP, class HOOK>
DI void gemm_pair(CW& c, v4u (&B)[32], f32x16& acc0, f32x16& acc1, HOOK hook) {
  Pipe q;
  pair_steps<SWAP, -PD, 32, 32, false, false>(c, q, B, acc0, acc1, hook);
}
template <class HOOK>
DI void gemm_single(CW& c, v4u (&B)[32], f32x16& acc, HOOK hook) {
  bf16x8 wa[5];
  Pipe q;
  static_for<-4, 32>([&](auto ic) __attribute__((always_inline)) {
    constexpr int i = ic.value, n = i + 4;
    if constexpr (n == 0) { const v2u v = vld<v2u>(L_PL); wait_slot(c, rfl(min(v.x, v.y)), c.s); q.adn = c.lane_tr + c.pos * 16384u; }
    if constexpr (i >= 0) acc = mfma(wa[i % 5], asb(B[i]), acc);
    SCHED();
    if constexpr (n < 32) {
      if constexpr (n % 16 == 0) q.ad = q.adn;
      wa[n % 5] = ldw(q.ad + (n % 16) * 1024, 512);
      if constexpr (n % 16 == 6) book<0, (n < 16), (n < 16)>(c, q);
      if constexpr (n % 16 >= 10) book<n % 16 - 9, (n < 16), (n < 16)>(c, q);
    }
    hook(ic);
    SCHED();
  });
}
constexpr int TL = 6;
template <int PW> struct Wide { bf16x8 wa[PW + 1 + TL], wb[PW + 1]; Pipe q; unsigned dO, lt; };
template <int NK, int I0, int I1, int PW, class HOOK>
DI void wide_steps(CW& c, Wide<PW>& d, v4u (&B)[32], f32x16& accM, f32x16& accO, int kh, HOOK hook) {
  Pipe& q = d.q;
  const unsigned keep = c.lane_tr;
  if constexpr (I0 == -PW) { d.lt = c.lane_tr + kh * (8192u + 512u); d.dO = kh ? -512u : 512u; }
  c.lane_tr = d.lt;
  static_for<I0, I1>([&](auto ic) __attribute__((always_inline)) {
    constexpr int i = ic.value, n = i + PW;
    constexpr bool F = n < NK, NX = F && n + 4 - n % 4 < NK;
    constexpr int CK = !NX || (n / 4) % 2 == 0 ? 0 : n + 8 - n % 4 < NK ? 2 : 1;
    if constexpr (n == 0) slot_first<false>(c, q);
    if constexpr (i >= 0) accO = mfma(d.wb[i % (PW + 1)], asb(B[i % 16]), accO);
    SCHED();
    if constexpr (F && n % 4 == 0) { q.ad = q.adn; if constexpr (n + 3 == NK) q.ad -= kh * 2048u; q.ad2 = q.ad + d.dO; }
    if constexpr (F) d.wb[n % (PW + 1)] = ldw(q.ad2 + (n % 4) * 2048, 1024);
    if constexpr (F && n % 4 == 1) { book<2, NX, CK>(c, q); }
    if constexpr (F && n % 4 == 2) { book<3, NX, CK>(c, q); }
    if constexpr (F && (n % 4 == 3 || n == NK - 1)) { book<5, NX, CK>(c, q); }
    hook(ic, IC<0>{});
    SCHED();
    if constexpr (i >= 0 && i < NK - TL) accM = mfma(d.wa[i % (PW + 1 + TL)], asb(B[i % 16]), accM);
    SCHED();
    if constexpr (F) d.wa[n % (PW + 1 + TL)] = ldw(q.ad + (n % 4) * 2048, 1024);
    if constexpr (F && n % 4 == 0) book<0, NX, CK>(c, q);
    if constexpr (F && n % 4 == 2) book<4, NX, CK>(c, q);
    if constexpr (F && (n % 4 == 3 || n == NK - 1)) book<6, NX, CK>(c, q);
    hook(ic, IC<1>{});
    SCHED();
  });
  c.lane_tr = keep;
}
struct NoHook { template <class A, class B> DI void operator()(A, B) const {} };

DI void ln_factors(float sum, float sq, float& a, unsigned& mf, bool up) {
  float mean = sum * (1.0f / 512.0f);
  PIN(mean);
  const float var = fmaxf(sq * (1.0f / 512.0f) - mean * mean, 0.f);
  a = __builtin_amdgcn_rsqf(var + 1e-5f);
  const unsigned hi = pk2(-mean, -mean);
  const float lo = -mean - blo(hi);
  mf = up ? pk2(lo, lo) : hi;
}
struct Stats { v4u sv[4]; f32x4 t; f32x2 u[4]; float mean, var; };
constexpr int K0 = 34;
DI void stats_hook(CW& c, Stats& t, int k, rsrc_t rs, int soff, float& a, unsigned& mf) {
  if (k == 3 || k == 7 || k == 11 || k == 15) t.sv[k >> 2] = ld16(rs, c.lane * 16 + (k >> 2) * 1024, soff);
  const unsigned sc = L_SCR + (c.w ^ 2) * 4096;
  if (k >= K0 && k < K0 + 4) { const int e = k - K0; t.t[e] = fadd1(fadd1(u2f(t.sv[0][e]), u2f(t.sv[1][e])), fadd1(u2f(t.sv[2][e]), u2f(t.sv[3][e]))); PIN(t.t[e]); }
  if (k == K0 + 4) vst<f32x4>(sc + c.lane * 16, t.t);
  if (k == K0 + 5) { t.u[0] = vld<f32x2>(sc + (c.lane & 31) * 8); t.u[1] = vld<f32x2>(sc + 256 + (c.lane & 31) * 8); }
  if (k == K0 + 6) { t.u[2] = vld<f32x2>(sc + 512 + (c.lane & 31) * 8); t.u[3] = vld<f32x2>(sc + 768 + (c.lane & 31) * 8); }
  if (k == K0 + 10) { t.mean = fadd1(fadd1(t.u[0].x, t.u[1].x), fadd1(t.u[2].x, t.u[3].x)); PIN(t.mean); }
  if (k == K0 + 11) { t.var = fadd1(fadd1(t.u[0].y, t.u[1].y), fadd1(t.u[2].y, t.u[3].y)); PIN(t.var); }
  if (k == K0 + 12) { t.mean *= -1.0f / 512.0f; PIN(t.mean); t.var = t.var * (1.0f / 512.0f) + 1e-5f; PIN(t.var); }
  if (k == K0 + 13) { t.var = fmaxf(t.var - t.mean * t.mean, 1e-5f); PIN(t.var); a = __builtin_amdgcn_rsqf(t.var); PIN(a); }
  if (k == K0 + 14) { mf = pk2(t.mean, t.mean); t.mean -= blo(mf); PIN(mf); PIN(t.mean); }
  if (k == K0 + 15) { mf = c.lane >= 32 ? pk2(t.mean, t.mean) : mf; PIN(mf); }
}
DI f32x16 unpack(const unsigned (&R)[8]) {
  f32x16 a;
#pragma unroll
  for (int j = 0; j < 8; ++j) { a[2 * j] = blo(R[j]); a[2 * j + 1] = bhi(R[j]); }
  return a;
}
template <int NK, int PW>
DI void finish_wide(CW& c, Wide<PW>& d, v4u (&B)[32], f32x16& accM, const f32x16& accO, unsigned (&R)[8], unsigned seq) {
  const unsigned mine = L_SCR + c.w * 4096 + c.lane * 16, other = L_SCR + (c.w ^ 2) * 4096 + c.lane * 16;
  auto step = [&](int k) __attribute__((always_inline)) { accM = mfma(d.wa[(NK - TL + k) % (PW + 1 + TL)], asb(B[(NK - TL + k) % 16]), accM); SCHED(); };
  auto put = [&](int g) __attribute__((always_inline)) { vst<f32x4>(mine + g * 1024, f32x4{accO[4 * g], accO[4 * g + 1], accO[4 * g + 2], accO[4 * g + 3]}); };
  step(0); step(1);
  put(0); put(1); SCHED();
  step(2);
  put(2); put(3); post(c, L_F3, seq); SCHED();
  step(3); step(4);
  unsigned fl = vld<unsigned>(L_F3 + (c.w ^ 2) * 4);
  f32x4 v[4];
#pragma unroll
  for (int g = 0; g < 4; ++g) v[g] = vld<f32x4>(other + g * 1024);
  SCHED();
  step(5);
  if (rfl(fl) < seq) {
    for (;;) {
      fl = vld<unsigned>(L_F3 + (c.w ^ 2) * 4);
#pragma unroll
      for (int g = 0; g < 4; ++g) v[g] = vld<f32x4>(other + g * 1024);
      if (rfl(fl) >= seq || spent(c)) break;
    }
  }
#pragma unroll
  for (int g = 0; g < 4; ++g) {
    R[2 * g] = pk2(pair(accM, 2 * g) + f32x2{v[g].x, v[g].y});
    R[2 * g + 1] = pk2(pair(accM, 2 * g + 1) + f32x2{v[g].z, v[g].w});
  }
}
DI void publish_x(CW& c, rsrc_t rs, const unsigned (&R)[8], int xoff, int stoff) {
  st16(rs, v4u{R[0], R[1], R[2], R[3]}, c.lane * 16, xoff);
  st16(rs, v4u{R[4], R[5], R[6], R[7]}, c.lane * 16 + 1024, xoff);
  SCHED();
  float s0 = 0.f, q0 = 0.f, s1 = 0.f, q1 = 0.f;
#pragma unroll
  for (int i = 0; i < 4; ++i) { s0 = dot2(R[i], ONES, s0); q0 = dot2(R[i], R[i], q0); s1 = dot2(R[4 + i], ONES, s1); q1 = dot2(R[4 + i], R[4 + i], q1); }
  const float s = sum_halves(s0 + s1), q = sum_halves(q0 + q1);
  st8(rs, v2u{f2u(s), f2u(q)}, (c.lane & 31) * 8, stoff);
}

DI void dma4(const char* base, unsigned v0, unsigned v1, unsigned v2, unsigned v3, unsigned dst) {
  asm volatile("s_mov_b32 m0, %5\n\ts_nop 3\n\t"
               "global_load_lds_dwordx4 %0, %4\n\tglobal_load_lds_dwordx4 %1, %4 offset:1024\n\t"
               "global_load_lds_dwordx4 %2, %4 offset:2048\n\tglobal_load_lds_dwordx4 %3, %4 offset:3072"
               ::"v"(v0), "v"(v1), "v"(v2), "v"(v3), "s"(uni(base)), "s"(rfl(dst)) : "memory");
}
#define PACE "s_sleep 1\n\t"
DI void dma4p(const char* base, unsigned v0, unsigned v1, unsigned v2, unsigned v3, unsigned dst) {
  asm volatile("s_mov_b32 m0, %5\n\ts_nop 3\n\t"
               "global_load_lds_dwordx4 %0, %4\n\t" PACE "global_load_lds_dwordx4 %1, %4 offset:1024\n\t" PACE
               "global_load_lds_dwordx4 %2, %4 offset:2048\n\t" PACE "global_load_lds_dwordx4 %3, %4 offset:3072\n\t" PACE
               ::"v"(v0), "v"(v1), "v"(v2), "v"(v3), "s"(uni(base)), "s"(rfl(dst)) : "memory");
}
DI void dma4_sc1(const char* base, unsigned v, unsigned dst) {
  asm volatile("s_mov_b32 m0, %2\n\ts_nop 3\n\t"
               "global_load_lds_dwordx4 %0, %1 sc1\n\t" PACE "global_load_lds_dwordx4 %0, %1 offset:1024 sc1\n\t" PACE
               "global_load_lds_dwordx4 %0, %1 offset:2048 sc1\n\t" PACE "global_load_lds_dwordx4 %0, %1 offset:3072 sc1\n\t" PACE
               ::"v"(v), "s"(uni(base)), "s"(rfl(dst)) : "memory");
}
DI void dma4_sc1f(const char* base, unsigned v, unsigned dst) {
  asm volatile("s_mov_b32 m0, %2\n\ts_nop 3\n\t"
               "global_load_lds_dwordx4 %0, %1 sc1\n\tglobal_load_lds_dwordx4 %0, %1 offset:1024 sc1\n\t"
               "global_load_lds_dwordx4 %0, %1 offset:2048 sc1\n\tglobal_load_lds_dwordx4 %0, %1 offset:3072 sc1"
               ::"v"(v), "s"(uni(base)), "s"(rfl(dst)) : "memory");
}
template <int N> DI void wait_vm() { asm volatile("s_waitcnt vmcnt(%c0)" ::"i"(N) : "memory"); }
DI unsigned tr_lane(int lane) { return (4 * (lane >> 5) + ((lane & 15) >> 2)) * 64 + ((lane >> 4) & 1) * 32 + (lane & 3) * 8; }
DI int fresh_lane() { int l; asm volatile("v_mbcnt_lo_u32_b32 %0, -1, 0\n\tv_mbcnt_hi_u32_b32 %0, -1, %0" : "=v"(l)); return l; }
#define LSTAMP(v, s) do {} while (0)
DI void lds_add(unsigned pa, unsigned off, unsigned n) { CFENCE(); (void)__hip_atomic_fetch_add(lp<unsigned>(pa + off), n, __ATOMIC_RELAXED, __HIP_MEMORY_SCOPE_WORKGROUP); CFENCE(); }
DI void loader(const Params& P, int p, int m, int lw) {
  const int lane = fresh_lane();
  const unsigned smem_base = (unsigned)(uintptr_t)(LAS char*)smem;
  const unsigned nr = GM(m) < 11 ? 3 : 2, NS = 21 + 4 * nr, NT = 2 * NS;
  const unsigned v_lin = lane * 16, v_wide = ((lane & 31) >> 2) * 1024 + (lane >> 5) * 64 + (lane & 3) * 16, v_raw = (lane >> 2) * 3072 + (lane & 3) * 16;
  const unsigned pa = lane == 0 ? L_RDY : L_DUMMY + lane * 4;
  const unsigned pp = pa + (L_PL - L_RDY) + lw * 4;
  if (blockIdx.x < 16) {
    const unsigned vi = blockIdx.x * 2 + lw, first = lw * 8192u, poff = WS_PROBE + vi * 128u;
    unsigned long long t0, t1, t2; unsigned j0, j1, epoch;
    asm volatile("s_load_dword %5, %8, 0x0\n\ts_load_dword %3, %6, 0x1000\n\ts_waitcnt lgkmcnt(0)\n\t"
                 "s_memtime %0\n\ts_waitcnt lgkmcnt(0)\n\ts_load_dword %3, %6, 0x0\n\ts_waitcnt lgkmcnt(0)\n\ts_memtime %1\n\ts_waitcnt lgkmcnt(0)\n\t"
                 "s_load_dword %4, %7, 0x0\n\ts_waitcnt lgkmcnt(0)\n\ts_memtime %2\n\ts_waitcnt lgkmcnt(0)"
                 : "=&s"(t0), "=&s"(t1), "=&s"(t2), "=&s"(j0), "=&s"(j1), "=&s"(epoch)
                 : "s"(uni(P.ws + poff + first)), "s"(uni(P.ws + poff + (first ^ 8192u))), "s"(uni(P.ws)) : "memory");
    int d = min(max((int)((unsigned)(t1 - t0) - (unsigned)(t2 - t1)), -VCL), VCL);
    if ((lw ^ (p >> 3) ^ (poff >> 21)) & 1) d = -d;
    st8(__builtin_amdgcn_make_buffer_rsrc((void*)P.ws, 0, WS_END, 0x00020000), v2u{(unsigned)d, epoch + 1u}, 0, WS_VOTE + vi * 8);
  }
  __builtin_amdgcn_s_setprio(LPRIO);
  unsigned limit = 0;
  int budget = BUDGET;
  bool pend = false;
  auto entry = [&](unsigned s) __attribute__((always_inline)) -> unsigned long long {
    const bool l2 = s >= NS;
    const unsigned q = lane & 3, i = l2 ? s - NS : s, g = i - 10u, d = g - 4u * nr, r4 = 4u * ROT(m);
    const unsigned blk = i < 4u ? q >> 1 : 2u, ks0 = ((i < 4u ? 8u * i + (q & 1) * 4u : 16u * (i - 4u) + q * 4u) + r4) & 31u;
    if (i < 6u && l2) return (unsigned long long)P.ws + WS_RQ + ((m * 3 + blk) * 32u + ks0) * 1024u + 1u;
    if (i < 6u) return (unsigned long long)P.wqkv + m * 64 + ks0 * (16u * 3072u);
    if (i < 10u) return (unsigned long long)P.wo + ((i - 6u) * 128u + q * 32u) * 1024u + (m & 7) * 128 + 2u;
    if (g < 4u * nr) return (unsigned long long)P.ws + ((q >> 1) ? WS_RU : WS_RG) + ((16u * (g >> 2) + GM(m)) * 32u + ((8u * (g & 3u) + (q & 1) * 4u + r4) & 31u)) * 1024u + 1u;
    return (unsigned long long)P.wd + min((d < 8u ? (d & 4u) | ((d + ROT(m)) & 3u) : d) * 128u + q * 32u, 1344u) * 1024u + (m & 7) * 128 + 2u;
  };
  unsigned long long tab = entry(4u + lw + 2u * (lane >> 2));
  const unsigned long long tab2 = entry(36u + lw + 2u * (lane >> 2));
  for (unsigned s = 4 + lw, t = 0; s < NT; s += 2, t = (t + 4) & 63u) {
    const unsigned i = s >= NS ? s - NS : s;
    if (s >= 36u) tab = tab2;
    const char* src[4];
#pragma unroll
    for (int q = 0; q < 4; ++q)
      src[q] = (const char*)(((unsigned long long)__builtin_amdgcn_readlane((unsigned)(tab >> 32), t + q) << 32) | (__builtin_amdgcn_readlane((unsigned)tab, t + q) & ~3u));
    const unsigned kind = __builtin_amdgcn_readlane((unsigned)tab, t) & 3u;
    if (pend && i < THINB) { wait_vm<0>(); vst<unsigned>(pp, s - 1); LSTAMP(tlan, s - 2); pend = false; }
    while (s >= limit) {
      limit = min4(vld<v4u>(L_FCNT));
      if (s < limit) break;
      if (pend) { wait_vm<0>(); vst<unsigned>(pp, s - 1); LSTAMP(tlan, s - 2); pend = false; continue; }
      __builtin_amdgcn_s_sleep(LSLEEP);
      if (--budget < 0) break;
    }
    LSTAMP(tiss, s);
    const unsigned dst = smem_base + L_RING + (s & (NSLOT - 1)) * 16384u;
    const unsigned v0 = kind == 2u ? v_wide : v_raw, st = kind == 2u ? 7168u : 48128u;
    if (i >= PACEB && i < 18u && s >= 6u) {
      if (kind == 1u) { dma4_sc1(src[0], v_lin, dst); dma4_sc1(src[1], v_lin, dst + 4096); dma4_sc1(src[2], v_lin, dst + 8192); dma4_sc1(src[3], v_lin, dst + 12288); }
      else {
#pragma unroll
        for (int q = 0; q < 4; ++q) dma4p(src[q], v0, v0 + st, v0 + 2 * st, v0 + 3 * st, dst + q * 4096);
      }
    } else {
      if (kind == 1u) { dma4_sc1f(src[0], v_lin, dst); dma4_sc1f(src[1], v_lin, dst + 4096); dma4_sc1f(src[2], v_lin, dst + 8192); dma4_sc1f(src[3], v_lin, dst + 12288); }
      else {
#pragma unroll
        for (int q = 0; q < 4; ++q) dma4(src[q], v0, v0 + st, v0 + 2 * st, v0 + 3 * st, dst + q * 4096);
      }
    }
    if (pend) { wait_vm<16>(); vst<unsigned>(pp, s - 1); LSTAMP(tlan, s - 2); }
    LSTAMP(tis2, s);
    pend = true;
    if (s < 6u) {
      wait_vm<0>(); vst<unsigned>(pp, s + 1); LSTAMP(tlan, s); pend = false;
      __builtin_amdgcn_s_setprio(3);
      const unsigned a = tr_lane(lane) + s * 16384u;
      float c0 = 0.f, c1 = 0.f;
#pragma unroll
      for (int n = 0; n < 16; ++n) {
        const v4u f = __builtin_bit_cast(v4u, ldw(a + n * 1024, 512));
        c0 = dot2(f.x, ONES, c0); c1 = dot2(f.y, ONES, c1); c0 = dot2(f.z, ONES, c0); c1 = dot2(f.w, ONES, c1);
      }
      const float t = sum_halves(c0 + c1);
      vst<v2u>(L_CSF + 2 * 512 + (lane & 31) * 16 + lw * 8, split3(t));
      lds_add(pa, L_CSC - L_RDY + 4, 1);
      __builtin_amdgcn_s_setprio(LPRIO);
    }
    if (s + 2 >= NT) { wait_vm<0>(); vst<unsigned>(pp, s + 1); }
  }
}
DI void server(const Params& P, int p, int m, int sw) {
  const int lane = fresh_lane();
  unsigned epoch;
  asm volatile("s_load_dword %0, %1, 0x0" : "=s"(epoch) : "s"(P.ws) : "memory");
  const unsigned pa = lane == 0 ? L_RDY : L_DUMMY + lane * 4;
  const rsrc_t rs = __builtin_amdgcn_make_buffer_rsrc((void*)P.ws, 0, WS_END, 0x00020000);
  CW c;
  c.lane = lane; c.ws = P.ws; c.budget = BUDGET;
  asm volatile("s_waitcnt lgkmcnt(0)" : "+s"(epoch) :: "memory");
  c.tag = epoch + 1u;
  const int co = WS_CSP + (m * 2 + sw) * 16 * 768;
  STAMP(7);
  DRAIN();
  STAMP(10);
  lds_add(pa, L_RPD - L_RDY, 1);
  {
    const unsigned idx = (blockIdx.x * 2 + sw) * 4 + (lane & 3), line = (idx / 3u) % 576u;
    asm volatile("s_mov_b64 exec, 0xf\n\ts_nop 4\n\tglobal_atomic_or %0, %1, %2\n\ts_mov_b64 exec, -1" ::"v"(WS_FKV + line * 256u + (idx % 3u) * 64u), "v"(0u), "s"(uni(P.ws)) : "memory");
  }
  {
    {
      const unsigned b = blockIdx.x;
      const char* tb = sw ? P.wo : P.wd;
      const unsigned to = sw ? (b * 16u + (lane & 15)) * 128u : (b * 43u + min(lane, 42)) * 128u;
      asm volatile("s_mov_b32 m0, %2\n\ts_nop 3\n\tglobal_load_lds_dword %0, %1" :: "v"(to), "s"(uni(tb)), "s"((unsigned)(uintptr_t)(LAS char*)smem + L_DUMMY) : "memory");
    }
    while (rfl(vld<unsigned>(L_RPD)) < 2u && !spent(c)) __builtin_amdgcn_s_sleep(1);
  }
  for (unsigned layer = 0; layer < 2; ++layer) {
    __builtin_amdgcn_s_setprio(3);
    f32x4 tk[4], tz[4];
    while (rfl(vld<unsigned>(L_FCNT)) < layer * (GM(m) < 11 ? 33u : 29u) + 9u && !spent(c)) __builtin_amdgcn_s_sleep(8);
    for (;;) {
      const v4u fl = vld<v4u>(L_F1);
      if (min4(fl) > layer || spent(c)) break;
      __builtin_amdgcn_s_sleep(1);
    }
    STAMP(11 + 4 * layer);
#pragma unroll
    for (int u = 0; u < 4; ++u) { tk[u] = vld<f32x4>(L_KVS + u * 2048 + sw * 1024 + lane * 16); tz[u] = vld<f32x4>(L_ZS + u * 128 + (lane & 1) * 64 + sw * 32 + (lane & 2) * 8); }
    const int g0 = WS_KVP + ((layer * 16 + m) * 2 + sw) * 65536, f0 = WS_FKV + layer * 16384 + (m * 2 + sw) * 256, po = g0 + fr(2 * p);
    v4u sk, sz;
#pragma unroll
    for (int e = 0; e < 4; ++e) { sk[e] = f2u(fadd1(fadd1(tk[0][e], tk[1][e]), fadd1(tk[2][e], tk[3][e]))); sz[e] = f2u(fadd1(fadd1(tz[0][e], tz[1][e]), fadd1(tz[2][e], tz[3][e]))); }
    st16(rs, sk, lane * 16, po); st16(rs, sk, lane * 16, po + 8192);
    st16(rs, sz, 1024 + (lane & 7) * 16 + (lane & 8) * 1024, po);
    v4u vt = ld16(rs, (lane & 15) * 16, WS_VOTE);
    DRAIN();
    asm volatile("s_mov_b64 exec, 3\n\ts_nop 4\n\tglobal_atomic_swap %0, %1, %2\n\ts_mov_b64 exec, -1" ::"v"(lane * 8192), "v"(c.tag), "s"(uni(P.ws + f0 + p * 4)) : "memory");
    while (__builtin_amdgcn_ballot_w64(vt.y != c.tag || vt.w != c.tag) != 0ull && !spent(c)) { CFENCE(); vt = ld16(rs, (lane & 15) * 16, WS_VOTE); }
    const int cbit = rfl(row_sum((float)((int)vt.x + (int)vt.z)) > 0.f ? 1u : 0u);
    vst<unsigned>(L_CB, cbit);
    const int go = near(g0, cbit ^ (p >> 3)), fo = near(f0, cbit ^ (p >> 3));
    STAMP(12 + 4 * layer);
    wait_flags(c, fo, 0xffffull);
    STAMP(13 + 4 * layer);
    const v4u zl = ld16(rs, 1024 + fr(2 * (lane & 15)) + (lane >> 4) * 16, go);
    v4u g[16];
#pragma unroll
    for (int i = 0; i < 16; ++i) { SCHED(); g[i] = ld16(rs, lane * 16 + (i & 1) * 2048, go + fr(4 * (i >> 1))); }
    SCHED();
    const float z0 = row_sum(u2f(zl.x)), z1 = row_sum(u2f(zl.y)), z2 = row_sum(u2f(zl.z)), z3 = row_sum(u2f(zl.w));
    if ((lane & 15) == 0) vst<v2u>(L_ZR + (lane >> 4) * 16 + sw * 8, v2u{pk2(z0, z1), pk2(z2, z3)});
    f32x4 r = __builtin_bit_cast(f32x4, g[0]);
#pragma unroll
    for (int i = 1; i < 16; ++i) r += __builtin_bit_cast(f32x4, g[i]);
    vst<v2u>(L_KVR + lane * 16 + sw * 8, v2u{pk2(r.x, r.y), pk2(r.z, r.w)});
    lds_add(pa, L_F2 - L_RDY, 2);
    STAMP(14 + 4 * layer);
    __builtin_amdgcn_s_setprio(0);
    if (layer == 0) {
      v4u t[16];
#pragma unroll
      for (int i = 0; i < 16; ++i) t[i] = ld16(rs, min(lane, 47) * 16, co + i * 768);
      SCHED();
      f32x4 v = __builtin_bit_cast(f32x4, t[0]);
#pragma unroll
      for (int i = 1; i < 16; ++i) v += __builtin_bit_cast(f32x4, t[i]);
      if (lane < 48) {
#pragma unroll
        for (int e = 0; e < 4; ++e) vst<v2u>(L_CSF + 2048 + (lane * 4 + e) * 16 + sw * 8, split3(v[e]));
      }
      lds_add(pa, L_CSC - L_RDY + 8, 2); lds_add(pa, L_CSC - L_RDY + 12, 2); lds_add(pa, L_CSC - L_RDY + 16, 2);
    }
  }
}
template <int AT, bool TWO> DI void cs_hook(CW& c, int i, unsigned k, v4u& a, v4u& b) {
  if (i == AT) a.x = vld<unsigned>(L_CSC + k * 4);
  if (i == AT + 3) {
    wait_count(c, a.x, L_CSC + k * 4, TWO ? 4u : 2u);
    a = vld<v4u>(L_CSF + k * 1024 + (c.lane & 31) * 16);
    if (TWO) b = vld<v4u>(L_CSF + k * 1024 + 512 + (c.lane & 31) * 16);
  }
}

__global__ void __launch_bounds__(512, 2) fused_fwd(Params P) {
  const int tid = threadIdx.x; int lane = tid & 63; const int wv = __builtin_amdgcn_readfirstlane(tid >> 6);
  const int b = blockIdx.x;
  {
    const unsigned co = CODE_BYTES - 256u - min(((b >> 3) * 8 + wv) * 256u, CODE_BYTES - 256u) + lane * 4;
    unsigned long long pc;
    asm volatile("s_mov_b32 m0, %2\n\ts_getpc_b64 %0\n\tglobal_load_lds_dword %1, %0" : "=&s"(pc) : "v"(co), "s"((unsigned)(uintptr_t)(LAS char*)smem + L_DUMMY) : "memory");
  }
  int p, m;
  if (MAPMODE == 0) { p = (b & 7) * 2 + (b >> 7); m = (b >> 3) & 15; }
  else if (MAPMODE == 1) { const int x = b & 7, i = b >> 3; p = (x >> 1) * 4 + (i >> 3); m = (x & 1) * 8 + (i & 7); }
  else if (MAPMODE == 3) { const int x = b & 7, i = b >> 3; p = (x >> 2) * 8 + (i >> 2); m = (x & 3) * 4 + (i & 3); }
  else { const int x = b & 7, i = b >> 3; p = i >> 1; m = x * 2 + (i & 1); }
  if (tid < (L_END - L_RDY) / 4) vst<unsigned>(L_RDY + tid * 4, tid == 4 || tid == 5 ? tid - 1u : 0u);
  asm volatile("s_waitcnt lgkmcnt(0)\n\ts_barrier" ::: "memory");
  if (wv >= 4) __builtin_amdgcn_s_setprio(3);
  STAMP(21);
  if (b < 16 && (wv & 6) == 4) {
    const unsigned po = WS_PROBE + (b * 2 + (wv & 1)) * 128u + (lane & 1) * 64 + (lane >> 1) * 4096;
    asm volatile("s_mov_b64 exec, 0xff\n\ts_nop 4\n\tglobal_atomic_or %0, %1, %2\n\ts_mov_b64 exec, -1" ::"v"(po), "v"(0u), "s"(uni(P.ws)) : "memory");
  }
  const unsigned smem_base = (unsigned)(uintptr_t)(LAS char*)smem;
  int w = wv & 3;
  unsigned xv[4];
  const char* xbase = (wv < 4 ? P.x : P.wqkv) + (unsigned)sopaque(wv < 4 ? (4 * p + w) * 32768 : ((w >> 1) + 1) * 1024 + m * 64);
  auto xbuf = [&](int q) -> unsigned { const int j = q % 5; return (j == 0 ? L_SCR : j == 1 ? 7 * 16384 : j == 2 ? 6 * 16384 : j == 3 ? 4 * 16384 : 5 * 16384) + w * 4096; };
  {
#pragma unroll
    for (int k = 0; k < 4; ++k) {
      const unsigned a = (8 * k + (lane >> 3)) * 1024 + (((lane & 7) ^ ((4 * k + (lane >> 4)) & 7)) * 16) - k * 1024;
      const unsigned c = (lane >> 2) * 3072 + (lane & 3) * 16 + k * 48128;
      xv[k] = wv < 4 ? a : c;
    }
    const unsigned cw = wv < 4, stride = sopaque(cw ? 128 : 64 * 3072), r0 = ROT(m) + (cw ? 0 : (w & 1));
    dma4(xbase + (r0 & 7) * stride, xv[0], xv[1], xv[2], xv[3], smem_base + (cw ? xbuf(0) : w * 4096));
    if (!cw) {
      const unsigned pa = smem_base + (lane == 0 ? L_RDY : L_DUMMY + lane * 4), a = tr_lane(lane) + w * 4096u;
      float c0 = 0.f, c1 = 0.f;
      auto cs = [&](int j) __attribute__((always_inline)) {
#pragma unroll
        for (int n = 0; n < 4; ++n) {
          const v4u f = __builtin_bit_cast(v4u, ldw(a + j * 16384 + n * 1024, 512));
          c0 = dot2(f.x, ONES, c0); c1 = dot2(f.y, ONES, c1); c0 = dot2(f.z, ONES, c0); c1 = dot2(f.w, ONES, c1);
        }
        PIN(c0); PIN(c1);
      };
      STAMP(2);
      asm volatile("s_waitcnt vmcnt(0)\n\tds_add_u32 %0, %1" ::"v"(pa), "v"(1u) : "memory");
      STAMP(3);
      dma4(xbase + ((2 + r0) & 7) * stride, xv[0], xv[1], xv[2], xv[3], smem_base + 1 * 16384 + w * 4096);
      cs(0);
      asm volatile("s_waitcnt vmcnt(0)\n\tds_add_u32 %0, %1 offset:4" ::"v"(pa), "v"(1u) : "memory");
      STAMP(4);
      dma4(xbase + ((4 + r0) & 7) * stride, xv[0], xv[1], xv[2], xv[3], smem_base + 2 * 16384 + w * 4096);
      cs(1);
      asm volatile("s_waitcnt vmcnt(0)\n\tds_add_u32 %0, %1 offset:8" ::"v"(pa), "v"(1u) : "memory");
      STAMP(5);
      dma4(xbase + ((6 + r0) & 7) * stride, xv[0], xv[1], xv[2], xv[3], smem_base + 3 * 16384 + w * 4096);
      cs(2);
      asm volatile("s_waitcnt vmcnt(0)\n\tds_add_u32 %0, %1 offset:12" ::"v"(pa), "v"(1u) : "memory");
      STAMP(6);
      const int ks = 2 * p + (w & 1), r2 = GM(m) < 11 ? 2 : 1;
      v4u rp[9];
      if (w >= 2) {
        __builtin_amdgcn_s_setprio(RPP);
        const GAS char* sq = (const GAS char*)P.wqkv + m * 64 + ks * (16 * 3072) + xv[0];
        const unsigned vg = GM(m) * 64 + ks * (16 * 2752) + (lane >> 2) * 2752 + (lane & 3) * 16;
        const GAS char* sg = (const GAS char*)P.wg + vg; const GAS char* su = (const GAS char*)P.wu + vg;
        rp[0] = *(const GAS v4u*)(sg); rp[1] = *(const GAS v4u*)(su); rp[2] = *(const GAS v4u*)(sg + 1024); rp[3] = *(const GAS v4u*)(su + 1024);
        rp[4] = *(const GAS v4u*)(sg + r2 * 1024); rp[5] = *(const GAS v4u*)(su + r2 * 1024);
        rp[6] = *(const GAS v4u*)(sq + 1024); rp[7] = *(const GAS v4u*)(sq + 2048); rp[8] = *(const GAS v4u*)(sq);
        SCHED();
        __builtin_amdgcn_s_setprio(3);
      }
      cs(3);
      const float t = sum_halves(c0 + c1);
      vst<v2u>(L_CSF + (w >> 1) * 512 + (lane & 31) * 16 + (w & 1) * 8, split3(t));
      asm volatile("ds_add_u32 %0, %1 offset:%c2" ::"v"(pa), "v"(1u), "i"(L_CSC - L_RDY) : "memory");
      if (sopaque(w) >= 2) {
        const rsrc_t rs = __builtin_amdgcn_make_buffer_rsrc((void*)P.ws, 0, WS_END, 0x00020000);
        const int dg = (GM(m) * 32 + ks) * 1024, dq = WS_RQ + (m * 96 + ks) * 1024;
        const unsigned SCX = L_CSF + 3072 + (w & 1) * 1024;
        v4u f[8];
#pragma unroll
        for (int u = 0; u < 8; ++u) {
          vst<v4u>(SCX + lane * 16, rp[u]);
          CFENCE();
          f[u] = __builtin_bit_cast(v4u, ldw(SCX + tr_lane(lane), 512));
          CFENCE();
        }
        st16(rs, f[0], lane * 16, WS_RG + dg); st16(rs, f[1], lane * 16, WS_RU + dg);
        st16(rs, f[2], lane * 16, WS_RG + dg + 16 * 32768); st16(rs, f[3], lane * 16, WS_RU + dg + 16 * 32768);
        st16(rs, f[4], lane * 16, WS_RG + dg + r2 * (16 * 32768)); st16(rs, f[5], lane * 16, WS_RU + dg + r2 * (16 * 32768));
        st16(rs, f[6], lane * 16, dq); st16(rs, f[7], lane * 16, dq + 32768); st16(rs, rp[8], lane * 16, dq + 65536);
        STAMP(8);
        float cs6[6];
#pragma unroll
        for (int u = 0; u < 6; ++u) cs6[u] = dot2(f[u].x, ONES, dot2(f[u].y, ONES, dot2(f[u].z, ONES, dot2(f[u].w, ONES, 0.f))));
#pragma unroll
        for (int u = 0; u < 3; ++u) {
          const auto r = __builtin_amdgcn_permlane32_swap(f2u(cs6[2 * u]), f2u(cs6[2 * u + 1]), false, false);
          __builtin_amdgcn_raw_buffer_store_b32(f2u(u2f(r[0]) + u2f(r[1])), rs, lane * 4 + u * 256, WS_CSP + ((m * 2 + (w & 1)) * 16 + p) * 768, 16);
        }
        STAMP(9);
      }
    }
  }
  STAMPV(0, t00);
  STAMP(61);
  if (sopaque(wv) >= 6) {
    server(P, p, m, wv - 6);
    STAMP(1); STAMP_END(); return; }
  if (sopaque(wv) >= 4) { loader(P, p, m, wv - 4); STAMP(1); STAMP_END(); return; }
  __builtin_amdgcn_s_setprio(2);

  unsigned epoch;
  asm volatile("s_load_dword %0, %1, 0x0" : "=s"(epoch) : "s"(P.ws) : "memory");
  v4u B[32];
  auto xdma = [&](int q) __attribute__((always_inline)) { dma4(xbase + ((q + ROT(m)) & 7) * 128, xv[0], xv[1], xv[2], xv[3], smem_base + xbuf(q)); };
  xdma(1);
  const rsrc_t rs = __builtin_amdgcn_make_buffer_rsrc((void*)P.ws, 0, WS_END, 0x00020000);
  asm volatile("s_waitcnt lgkmcnt(0)" : "+s"(epoch) :: "memory");
  STAMP(19);
  CW c;
  c.s = 0; c.pos = 0; c.perm = 4; c.lane_tr = tr_lane(lane); c.fa = lane == 0 ? L_FCNT + w * 4 : L_DUMMY + lane * 4;
  c.budget = BUDGET; c.lane = lane; c.w = w; c.ws = P.ws; c.tag = epoch + 1u;
  int h, t32, tbq, tbw, kh, cb;
  int sdn = 0;
#define FRESH() do { lane = opaque(lane); h = lane >> 5; t32 = lane & 31; c.lane = lane; p = sopaque(p); m = sopaque(m); w = sopaque(w); c.w = w; \
    tbq = layer * 64 + 4 * p + w; kh = w >> 1; tbw = layer * 64 + 4 * p + 2 * (m >> 3) + (w & 1); cb = 2 * (m & 7) + kh; } while (0)
  unsigned R[8];

  int layer = 0;
    f32x16 aK = {}, aV = {};
    float la; unsigned mf;
    f32x4 ar[4];
    v4u ck, cv;
    FRESH();
    auto rows_ab = [&]() __attribute__((always_inline)) {
      vst<float>(L_SA + w * 128 + t32 * 4, la);
#pragma unroll
      for (int g = 0; g < 4; ++g) ar[g] = vld<f32x4>(L_SA + w * 128 + (8 * g + 4 * h) * 4);
    };
    {
      float s1 = 0.f, s2 = 0.f, s3 = 0.f, s4 = 0.f;
      const unsigned xl = t32 * 128 + 8 * h, xf = (t32 >> 1) & 7;
      auto hook = [&](int i, int hf) __attribute__((always_inline)) {
        const int n = i + 2;
        if (n >= 0 && n < 32 && hf == 0) {
          const int q = n / 4, j = n % 4;
          if (j == 0) {
            if (q == 0) { STAMPB(39); wait_vm<4 * (XUP - 1)>(); STAMPB(40); }
            if (q == 1 || q == 2 || q == 3) wait_vm<12>();
            if (q == 4) wait_vm<12>();
            if (q == 5) wait_vm<8>();
            if (q == 6) wait_vm<4>();
            if (q == 7) wait_vm<0>();
          }
          const v2u lo = vld<v2u>(xbuf(q) + xl + (((2 * j) ^ xf) * 16)), hi = vld<v2u>(xbuf(q) + xl + (((2 * j + 1) ^ xf) * 16));
          B[n] = v4u{lo.x, lo.y, hi.x, hi.y};
        }
        if (hf == 1) {
          if (i == -1) xdma(2);
          if (i == 0) xdma(3);
          if (i == 1) xdma(4);
          if (i == 3 || i == 7 || i == 11) xdma(5 + i / 4);
          if (i >= 0 && i % 4 == 3) {
#pragma unroll
            for (int u = i - 3; u <= i; ++u) {
              const v4u f = B[u];
              s1 = dot2(f.x, ONES, s1); s2 = dot2(f.x, f.x, s2); s3 = dot2(f.y, ONES, s3); s4 = dot2(f.y, f.y, s4);
              s1 = dot2(f.z, ONES, s1); s2 = dot2(f.z, f.z, s2); s3 = dot2(f.w, ONES, s3); s4 = dot2(f.w, f.w, s4);
            }
            PIN(s1); PIN(s2); PIN(s3); PIN(s4);
          }
          cs_hook<26, true>(c, i, 0, ck, cv);
          if (n == 15 || n == 19) permit(c, n / 4 + 2);
          if (i == 30) permit(c, 10);
        }
      };
      gemm_pair<true>(c, B, aK, aV, hook);
      ln_factors(sum_halves(s1 + s3), sum_halves(s2 + s4), la, mf, h != 0);
      rows_ab();
    }
  for (; layer < 2; ++layer) {
    const bf16x8 mfr = asb(v4u{mf, mf, mf, mf});
    aK = mfma(mfr, asb(ck), aK);
    aV = mfma(mfr, asb(cv), aV);
    STAMPB(2 + 20 * layer);
    FRESH();
    {
      unsigned pkk[8], pkv[8];
#pragma unroll
      for (int j = 0; j < 8; ++j) {
        const f32x2 a = (j & 1) ? f32x2{ar[j >> 1].z, ar[j >> 1].w} : f32x2{ar[j >> 1].x, ar[j >> 1].y};
        pkk[j] = phi2(pair(aK, j), a);
        pkv[j] = pk2(pair(aV, j) * a);
      }
      const bf16x8 k0 = asb(v4u{pkk[0], pkk[1], pkk[2], pkk[3]}), k1 = asb(v4u{pkk[4], pkk[5], pkk[6], pkk[7]});
      const bf16x8 v0 = asb(v4u{pkv[0], pkv[1], pkv[2], pkv[3]}), v1 = asb(v4u{pkv[4], pkv[5], pkv[6], pkv[7]});
      const bf16x8 one = asb(v4u{ONES, ONES, ONES, ONES});
      f32x16 kv = {}, zz = {};
      unsigned ka = L_KVS + w * 2048 + lane * 16, za = L_ZS + w * 128 + t32 * 4;
      PIN(ka); PIN(za);
      SCHED();
      kv = mfma(k0, v0, kv); kv = mfma(k1, v1, kv); zz = mfma(one, k0, zz); zz = mfma(one, k1, zz);
      SCHED();
      const bool hd = (lane & 16) != 0;
      f32x4 q0, q1;
#pragma unroll
      for (int j = 0; j < 4; ++j) { q0[j] = sel(hd, kv[8 + j], kv[j]); q1[j] = sel(hd, kv[12 + j], kv[4 + j]); }
      vst<f32x4>(ka, q0);
      vst<f32x4>(ka + 1024, q1);
      vst<float>(za, zz[0]);
      post(c, L_F1, layer + 1);
    }
    STAMPB(3 + 20 * layer);
    unsigned pq[8];
    {
      f32x16 aQ = {};
      v4u c0;
      auto hook = [&](int i) __attribute__((always_inline)) {
        if (i == 20) {
          if (layer == 0) {
            const GAS char* xr = (const GAS char*)P.x + (size_t)(tbw * 32 + t32) * 1024 + cb * 64 + h * 8;
#pragma unroll
            for (int g = 0; g < 4; ++g) { const v2u v = *(const GAS v2u*)(xr + g * 16); R[2 * g] = v.x; R[2 * g + 1] = v.y; }
          }
        }
        cs_hook<24, false>(c, i, 1, c0, c0);
      };
      gemm_single(c, B, aQ, hook);
      aQ = mfma(asb(c0), mfr, aQ);
#pragma unroll
      for (int j = 0; j < 8; ++j) { pq[j] = phi2(pair(aQ, j), f32x2{la, la}); PIN(pq[j]); }
    }
    STAMPB(4 + 20 * layer);
    {
      FRESH();
      f32x16 aM = {}, aO = {};
      Wide<16> d;
      const unsigned s0 = c.s;
      wide_steps<16, -16, 0>(c, d, B, aM, aO, kh, NoHook());
      STAMPB(54);
      v4u fk, fz; unsigned cbv;
      for (;;) {
        const unsigned fl = vld<unsigned>(L_F2);
        fk = vld<v4u>(L_KVR + lane * 16); fz = vld<v4u>(L_ZR + (lane >> 4) * 16); cbv = vld<unsigned>(L_CB);
        if (rfl(fl) >= 4u * layer + 4u || spent(c)) break;
      }
      STAMPB(5 + 20 * layer);
      sdn = (p >> 3) ^ rfl(cbv);
      permit(c, s0 + FPN);
      const bool hd = (lane & 16) != 0;
      const v4u zero = {0u, 0u, 0u, 0u};
      const bf16x8 p0 = asb(v4u{pq[0], pq[1], pq[2], pq[3]}), p1 = asb(v4u{pq[4], pq[5], pq[6], pq[7]});
      f32x16 num = {}, den = {};
      den = mfma(asb(hd ? zero : fz), p0, den); den = mfma(asb(hd ? fz : zero), p1, den);
      num = mfma(asb(hd ? zero : fk), p0, num); num = mfma(asb(hd ? fk : zero), p1, num);
      const float i0 = __builtin_amdgcn_rcpf(den[0] + 1e-6f), i1 = __builtin_amdgcn_rcpf(den[8] + 1e-6f);
      const int os = a_act(WS_O, tbq, sdn) + fr(2 * m);
      st16(rs, v4u{pk2(pair(num, 0) * i0), pk2(pair(num, 1) * i0), pk2(pair(num, 2) * i0), pk2(pair(num, 3) * i0)}, lane * 16, os);
      st16(rs, v4u{pk2(pair(num, 4) * i1), pk2(pair(num, 5) * i1), pk2(pair(num, 6) * i1), pk2(pair(num, 7) * i1)}, lane * 16 + 1024, os);
      aM = unpack(R); PIN(aM);
      int oo = a_act(WS_O, tbw, sdn) + kh * 4096, xs = AX(WS_X1, tbw) + FX(2 * cb), ss = a_stat(WS_ST1, tbw, sdn) + cb * 256;
      SPIN(oo); SPIN(xs); SPIN(ss);
      DRAIN();
      set_flag(c, a_flag(WS_FO, tbq, sdn) + m * 4);
      STAMPB(6 + 20 * layer);
      wait_flags(c, a_flag(WS_FO, tbw, sdn), kh ? 0xccccull : 0x3333ull);
      STAMPB(7 + 20 * layer);
#pragma unroll
      for (int i = 0; i < AW; ++i) B[i] = ld16(rs, lane * 16 + (i & 3) * 1024, oo + (i >> 2) * 16384);
      SCHED();
      auto hook = [&](int i, int hf) __attribute__((always_inline)) {
        const int n = i + AW;
        if (hf == 0) return;
        if (n < 16) B[n] = ld16(rs, lane * 16 + (n & 3) * 1024, oo + (n >> 2) * 16384);
        if (n == WPN) permit(c, s0 + 12);
      };
      wide_steps<16, 0, 16>(c, d, B, aM, aO, kh, hook);
      STAMPB(8 + 20 * layer);
      finish_wide<16>(c, d, B, aM, aO, R, 2 * layer + 1);
      publish_x(c, rs, R, xs, ss);
      vst<v4u>(L_KVS + w * 2048 + lane * 16, v4u{R[0], R[1], R[2], R[3]});
      vst<v4u>(L_KVS + w * 2048 + 1024 + lane * 16, v4u{R[4], R[5], R[6], R[7]});
    }
    unsigned pf;
    f32x16 aM = {}, aO = {};
    Wide<4> d;
    unsigned sd;
    int fd, hd0, fl2, xs2, ss2; unsigned long long mk;
    auto ldh = [&](int n) __attribute__((always_inline)) {
      if (n < 32) B[n % 16] = ld16(rs, lane * 16 + (n & 3) * 1024, hd0 + (((n >> 2) & 4) | (((n >> 2) + ROT(m)) & 3)) * HS);
      else if (n < 40) B[n % 16] = ld16(rs, lane * 16 + (n & 3) * 1024, hd0 + (n >> 2) * HS);
      else B[n % 16] = ld16(rs, lane * 16 + (n & 3) * 1024, hd0 + 10 * HS - kh * 1024);
    };
    {
      FRESH();
      f32x16 G0 = {}, U0 = {}, G1 = {}, U1 = {};
      Pipe q;
      const unsigned s0 = c.s;
      pair_steps<false, -PD, 0, 64, false>(c, q, B, G0, U0, NoHook());
      const int xo = AX(WS_X1, tbq);
      int xa = xo + FX(4 * ROT(m)), xb = xo + FX(4 * ((ROT(m) + 1) & 7));
      SPIN(xa); SPIN(xb);
      DRAIN();
      set_flag(c, a_flag(WS_FX1, tbw, sdn) + cb * 4);
      STAMPB(9 + 20 * layer);
      wait_flags(c, a_flag(WS_FX1, tbq, sdn), 0xffffull);
      STAMPB(10 + 20 * layer);
      Stats sv;
#pragma unroll
      for (int i = 0; i < 8; ++i) B[i] = ld16(rs, lane * 16 + (i & 3) * 1024, i < 4 ? xa : xb);
      SCHED();
      float a2; unsigned gm;
      v4u cg, cu;
      unsigned hp[8];
      const int ho = AHH(tbq) + FH(2 * GM(m)), fo = a_flag(WS_FH, tbq, sdn) + GM(m) * 4;
      float hv;
      fd = a_flag(WS_FH, tbw, sdn); mk = kh ? 0xccccull : 0x3333ull; hd0 = AHH(tbw) + kh * 4096;
      fl2 = fo + 128;
      auto piece = [&](int e, const f32x16& G, const f32x16& U) __attribute__((always_inline)) {
        float g;
        if (e & 1) asm volatile("v_max_f32 %1, 0, %2\n\tv_mul_f32 %0, %3, %4\n\tv_mul_f32 %1, %1, %0\n\tv_cvt_pk_bf16_f32 %0, %5, %1" : "=&v"(hp[e >> 1]), "=&v"(g) : "v"(G[e]), "v"(U[e]), "v"(a2), "v"(hv));
        else asm volatile("v_max_f32 %1, 0, %2\n\tv_mul_f32 %0, %3, %4\n\tv_mul_f32 %0, %1, %0" : "=&v"(hv), "=&v"(g) : "v"(G[e]), "v"(U[e]), "v"(a2));
      };
      auto tail = [&](const f32x16& G, const f32x16& U) __attribute__((always_inline)) {
#pragma unroll
        for (int j = 0; j < 8; ++j) {
          const f32x2 g = {fmaxf(G[2 * j], 0.f), fmaxf(G[2 * j + 1], 0.f)};
          hp[j] = pk2(g * (pair(U, j) * a2));
        }
      };
      auto store_h = [&](int r, int half) __attribute__((always_inline)) { st16(rs, v4u{hp[4 * half], hp[4 * half + 1], hp[4 * half + 2], hp[4 * half + 3]}, lane * 16 + half * 1024, ho + r * (4 * HS)); };
      auto correct = [&](f32x16& G, f32x16& U) __attribute__((always_inline)) {
        const bf16x8 f = asb(v4u{gm, gm, gm, gm});
        G = mfma(asb(cg), f, G); U = mfma(asb(cu), f, U);
      };
      auto shadow = [&](int i, int hf, int r, const f32x16& G, const f32x16& U) __attribute__((always_inline)) {
        constexpr int at[16] = {3, 5, 7, 9, 12, 13, 14, 15, 17, 19, 21, 23, 25, 28, 29, 30};
#pragma unroll
        for (int e = 0; e < 16; ++e) if (2 * i + hf == at[e]) piece(e, G, U);
        if (2 * i + hf == 31) store_h(r, 0);
        if (2 * i + hf == 33) store_h(r, 1);
        if (hf == 0 && i == 25) { if (r == 0) DRAIN(); else wait_vm<8>(); set_flag(c, fo + r * 64); }
        if (hf == 1) cs_hook<25, true>(c, i, 3 + r, cg, cu);
        if (r == 1) {
          if (hf == 0 && i == 8) pf = poll_issue(c, fd, mk);
          if (hf == 0 && i == 17) { poll_done<2>(c, pf, fd, mk); if (layer) STAMPB(51); }
          if (hf == 1 && i >= 17) ldh(i - 17);
          if (hf == 0 && i == 30) pf = poll_issue(c, fd, mk << 16);
        }
      };
      pair_steps<false, 0, 32, 64, false>(c, q, B, G0, U0, [&](int i, int hf) __attribute__((always_inline)) {
        const int n = i + AH;
        stats_hook(c, sv, 2 * i + hf, rs, a_stat(WS_ST1, tbq, sdn), a2, gm);
        if (hf == 0) return;
        if (n < 32) B[n] = ld16(rs, lane * 16 + (n & 3) * 1024, xo + FX(4 * (((n >> 2) + ROT(m)) & 7)));
        if (n == GPN) permit(c, s0 + (GM(m) < 11 ? 23 : 19));
        if (i == 26) { a2 = fmul1(a2, a2); PIN(a2); }
        cs_hook<25, true>(c, i, 2, cg, cu);
      });
      correct(G0, U0);
      STAMPB(11 + 20 * layer);
      pair_steps<false, 32, 64, 96, false>(c, q, B, G1, U1, [&](int i, int hf) __attribute__((always_inline)) { shadow(i, hf, 0, G0, U0); });
      correct(G1, U1);
      STAMPB(12 + 20 * layer);
      pf = ~c.tag;
      if (sopaque(GM(m)) >= 11) {
        tail(G1, U1);
        store_h(1, 0); store_h(1, 1);
        DRAIN();
        set_flag(c, fo + 64);
        wait_flags(c, fd, mk);
      }
      asm volatile("" : "=v"(G0), "=v"(U0));
      if (sopaque(GM(m)) < 11) {
        G0 = f32x16{}; U0 = f32x16{};
        pair_steps<false, 64, 96, 96, false>(c, q, B, G0, U0, [&](int i, int hf) __attribute__((always_inline)) { shadow(i, hf, 1, G1, U1); });
        correct(G0, U0);
      }
      if (sopaque(GM(m)) >= 11) {
#pragma unroll
        for (int i = 0; i < 15; ++i) ldh(i);
      }
      sd = c.s;
      const v4u r0 = vld<v4u>(L_KVS + w * 2048 + lane * 16), r1 = vld<v4u>(L_KVS + w * 2048 + 1024 + lane * 16);
      wide_steps<43, -4, 0>(c, d, B, aM, aO, kh, NoHook());
      if (sopaque(GM(m)) < 11) {
        tail(G0, U0);
        store_h(2, 0); store_h(2, 1);
        STAMPB(13 + 20 * layer);
      }
      R[0] = r0.x; R[1] = r0.y; R[2] = r0.z; R[3] = r0.w; R[4] = r1.x; R[5] = r1.y; R[6] = r1.z; R[7] = r1.w;
      aM = unpack(R);
    }
    STAMPB(14 + 20 * layer);
    {
      unsigned pf2;
      auto hook = [&](int i, int hf) __attribute__((always_inline)) {
        if (hf == 0) {
          if (i == FS) { wait_vm<FS>(); set_flag(c, fl2); }
          if (i == PI) pf2 = poll_issue(c, fd, 0x7ffull << 32);
          return;
        }
        if (i == 1) { poll_done<5>(c, pf, fd, mk << 16); if (layer) STAMPB(47); }
        if (i == 19 && layer) STAMPB(48);
        if (i == 31 && layer) STAMPB(50);
        if (i + 15 < 32) ldh(i + 15);
        if (i == PC) { poll_done<(PI < 17 ? 17 - PI : 0)>(c, pf2, fd, 0x7ffull << 32); if (layer) STAMPB(49); }
        if (PC == 20) {
          if (i >= 20 && i <= 23) { ldh(2 * i - 8); ldh(2 * i - 7); }
          if (i >= 24 && i <= 26) ldh(i + 16);
        } else {
          if (i >= PC && i < PC + 5) { ldh(32 + 2 * (i - PC)); ldh(33 + 2 * (i - PC)); }
          if (i == PC + 5) ldh(42);
        }
        if (i == PKVAT) permit(c, sd + 11 + PKV);
        if (i == 34) { xs2 = AX(WS_X2, tbw) + FX(2 * cb); ss2 = a_stat(WS_ST2, tbw, sdn) + cb * 256; SPIN(xs2); SPIN(ss2); }
      };
      wide_steps<43, 0, 43>(c, d, B, aM, aO, kh, hook);
      STAMPB(17 + 20 * layer);
      finish_wide<43>(c, d, B, aM, aO, R, 2 * layer + 2);
    }
#pragma unroll
    for (int i = 0; i < 32; ++i) asm volatile("" : "=v"(B[i]));
    asm volatile("" : "=v"(aK), "=v"(aV), "=v"(la), "=v"(mf), "=v"(ck), "=v"(cv));
    asm volatile("" : "=v"(ar[0]), "=v"(ar[1]), "=v"(ar[2]), "=v"(ar[3]));
    if (layer == 0) {
      publish_x(c, rs, R, xs2, ss2);
      FRESH();
      aK = f32x16{}; aV = f32x16{};
      Pipe q;
      const unsigned s0 = c.s;
      pair_steps<true, -PD, 0, 32, false>(c, q, B, aK, aV, NoHook());
      const int xo = AX(WS_X2, tbq);
      int xa = xo + FX(4 * ROT(m)), xb = xo + FX(4 * ((ROT(m) + 1) & 7));
      SPIN(xa); SPIN(xb);
      DRAIN();
      set_flag(c, a_flag(WS_FX2, tbw, sdn) + cb * 4);
      STAMPB(18);
      wait_flags(c, a_flag(WS_FX2, tbq, sdn), 0xffffull);
      STAMPB(20);
      Stats sv;
#pragma unroll
      for (int i = 0; i < 8; ++i) B[i] = ld16(rs, lane * 16 + (i & 3) * 1024, i < 4 ? xa : xb);
      SCHED();
      auto hook = [&](int i, int hf) __attribute__((always_inline)) {
        const int n = i + AH;
        stats_hook(c, sv, 2 * i + hf, rs, a_stat(WS_ST2, tbq, sdn), la, mf);
        if (hf == 0) {
          if (i == 25) vst<float>(L_SA + w * 128 + t32 * 4, la);
          if (i == 26) { ar[0] = vld<f32x4>(L_SA + w * 128 + 4 * h * 4); ar[1] = vld<f32x4>(L_SA + w * 128 + (8 + 4 * h) * 4); }
          if (i == 27) { ar[2] = vld<f32x4>(L_SA + w * 128 + (16 + 4 * h) * 4); ar[3] = vld<f32x4>(L_SA + w * 128 + (24 + 4 * h) * 4); }
          return;
        }
        if (n < 32) B[n] = ld16(rs, lane * 16 + (n & 3) * 1024, xo + FX(4 * (((n >> 2) + ROT(m)) & 7)));
        if (n == KPN) permit(c, s0 + KVP2);
        cs_hook<26, true>(c, i, 0, ck, cv);
      };
      pair_steps<true, 0, 32, 32, false>(c, q, B, aK, aV, hook);
    }
  }
  {
    layer = 0;
    FRESH();
    const rsrc_t ro = __builtin_amdgcn_make_buffer_rsrc((void*)P.out, 0, 2048 * 1024, 0x00020000);
#pragma unroll
    for (int g = 0; g < 2; ++g) {
      const auto r0 = __builtin_amdgcn_permlane32_swap(R[4 * g], R[4 * g + 2], false, false), r1 = __builtin_amdgcn_permlane32_swap(R[4 * g + 1], R[4 * g + 3], false, false);
      __builtin_amdgcn_raw_buffer_store_b128(v4u{r0[0], r1[0], r0[1], r1[1]}, ro, (tbw * 32 + t32) * 1024 + cb * 64 + g * 32 + h * 16, 0, OAUX);
    }
  }
  STAMPB(38);
  STAMP_END();
  STAMPV(42, (unsigned long long)(BUDGET - c.budget));
  if (b == 0 && w == 0 && lane == 0) *(GAS unsigned*)(P.ws + WS_EPOCH) = c.tag;
}
}

extern "C" void kernel_launch(void* const* d_in, const int* in_sizes, int n_in, void* d_out, int out_size, void* d_ws,
                              size_t ws_size, hipStream_t stream) {
  Params P;
  P.x = (const char*)d_in[0]; P.wqkv = (const char*)d_in[1]; P.wo = (const char*)d_in[2];
  P.wg = (const char*)d_in[4]; P.wu = (const char*)d_in[5]; P.wd = (const char*)d_in[6];
  P.out = (char*)d_out; P.ws = (char*)(((uintptr_t)d_ws + 0x1fffff) & ~(uintptr_t)0x1fffff);
  (void)hipFuncSetAttribute((const void*)fused_fwd, hipFuncAttributeMaxDynamicSharedMemorySize, LDS_BYTES);
  fused_fwd<<<256, 512, LDS_BYTES, stream>>>(P);
}
```

```cpp
#include <hip/hip_runtime.h>
#include <stdint.h>

#define GAS __attribute__((address_space(1)))
#define LAS __attribute__((address_space(3)))
#define DI __device__ __forceinline__
typedef unsigned v4u __attribute__((ext_vector_type(4)));
typedef unsigned v2u __attribute__((ext_vector_type(2)));
typedef float f32x16 __attribute__((ext_vector_type(16)));
typedef float f32x4 __attribute__((ext_vector_type(4)));
typedef float f32x2 __attribute__((ext_vector_type(2)));
typedef __bf16 bf16x8 __attribute__((ext_vector_type(8)));
typedef __bf16 bf16x2 __attribute__((ext_vector_type(2)));
typedef short s16x4 __attribute__((ext_vector_type(4)));

#define ROT(m) (((m) + 2 * p) & 7)
#define GM(m) ((((m) & 7) << 1) | ((m) >> 3))
namespace {
constexpr int KB = 1024;
constexpr int AH = 8, AW = 8;
constexpr int PD = 2;
constexpr int WS_EPOCH = 0;
constexpr int WS_VOTE = 1024;
constexpr unsigned WS_PROBE = 192 * KB;
constexpr int WS_FKV = 16 * KB;
constexpr int WS_FO = WS_FKV + 32 * KB;
constexpr int WS_FX1 = WS_FO + 32 * KB;
constexpr int WS_FH = WS_FX1 + 32 * KB;
constexpr int WS_FX2 = WS_FH + 32 * KB;
constexpr int WS_ST1 = 256 * KB;
constexpr int WS_ST2 = WS_ST1 + 512 * KB;
constexpr int WS_CSP = WS_ST2 + 256 * KB;
constexpr int WS_O = 4096 * KB;
constexpr int WS_X1 = WS_O + 4096 * KB;
constexpr int WS_X2 = WS_X1 + 4096 * KB;
constexpr int WS_H = WS_X2 + 2048 * KB;
constexpr int WS_KVP = WS_H + 16384 * KB;
constexpr int WS_RQ = WS_KVP + 4096 * KB;
constexpr int WS_RG = WS_RQ + 1536 * KB;
constexpr int WS_RU = WS_RG + 43 * 32 * KB;
constexpr int WS_END = WS_RU + 43 * 32 * KB;
constexpr int NSLOT = 8;
constexpr int L_RING = 0;
constexpr int L_SCR = NSLOT * 16 * KB;
constexpr int L_KVS = L_SCR + 16 * KB;
constexpr int L_CSF = L_KVS + 8 * KB;
constexpr int L_SA = L_CSF + 5120;
constexpr int L_KVR = L_SA + 512;
constexpr int L_ZR = L_KVR + 1024;
constexpr int L_ZS = L_ZR + 64;
constexpr int L_DUMMY = L_ZS + 512;
constexpr int L_RDY = L_DUMMY + 448;
constexpr int L_PL = L_RDY + 16;
constexpr int L_CSC = L_RDY + 32;
constexpr int L_FCNT = L_CSC + 32;
constexpr int L_F1 = L_FCNT + 16;
constexpr int L_F2 = L_F1 + 16;
constexpr int L_F3 = L_F2 + 16;
constexpr int L_RPD = L_F3 + 16;
constexpr int L_CB = L_RPD + 8;
constexpr int L_END = L_RPD + 16;
static_assert(L_END <= 160 * KB, "LDS");
constexpr int LDS_BYTES = (L_END + 15) / 16 * 16;
constexpr unsigned CODE_BYTES = 41728;
constexpr unsigned BUDGET = 1u << 18;

struct Params {
  const char* x; const char* wqkv; const char* wo; const char* wg; const char* wu; const char* wd;
  char* out; char* ws;
};

extern __shared__ __attribute__((aligned(16))) char smem[];
template <class T> DI LAS T* lp(unsigned off) { return (LAS T*)((LAS char*)smem + off); }
template <class T> DI T vld(unsigned off) { return *(volatile LAS T*)((LAS char*)smem + off); }
template <class T> DI void vst(unsigned off, T v) { *(volatile LAS T*)((LAS char*)smem + off) = v; }
DI unsigned rfl(unsigned v) { return __builtin_amdgcn_readfirstlane(v); }
DI const char* uni(const char* p) { const unsigned long long v = (unsigned long long)p; return (const char*)(((unsigned long long)rfl((unsigned)(v >> 32)) << 32) | rfl((unsigned)v)); }
#define SCHED() __builtin_amdgcn_sched_barrier(0)
#define CFENCE() asm volatile("" ::: "memory")
#define DRAIN() asm volatile("s_waitcnt vmcnt(0)" ::: "memory")
#define PIN(x) asm volatile("" : "+v"(x))

DI bf16x8 ldw(unsigned a, int second) {
  s16x4 lo = __builtin_amdgcn_ds_read_tr16_b64_v4i16(lp<s16x4>(a));
  s16x4 hi = __builtin_amdgcn_ds_read_tr16_b64_v4i16(lp<s16x4>(a + second));
  return __builtin_bit_cast(bf16x8, __builtin_shufflevector(lo, hi, 0, 1, 2, 3, 4, 5, 6, 7));
}
DI f32x16 mfma(bf16x8 a, bf16x8 b, f32x16 c) { return __builtin_amdgcn_mfma_f32_32x32x16_bf16(a, b, c, 0, 0, 0); }
DI bf16x8 asb(v4u v) { return __builtin_bit_cast(bf16x8, v); }
DI unsigned pk2(float lo, float hi) { bf16x2 r = {(__bf16)lo, (__bf16)hi}; return __builtin_bit_cast(unsigned, r); }
DI unsigned pk2(f32x2 v) { return pk2(v.x, v.y); }
DI float dot2(unsigned a, unsigned b, float c) { return __builtin_amdgcn_fdot2_f32_bf16(__builtin_bit_cast(bf16x2, a), __builtin_bit_cast(bf16x2, b), c, false); }
DI float u2f(unsigned u) { return __builtin_bit_cast(float, u); }
DI unsigned f2u(float f) { return __builtin_bit_cast(unsigned, f); }
DI float sel(bool c, float a, float b) { asm volatile("" : "+v"(a), "+v"(b)); return c ? a : b; }
DI int opaque(int v) { asm volatile("" : "+v"(v)); return v; }
DI int sopaque(int v) { asm volatile("" : "+s"(v)); return v; }
DI float blo(unsigned u) { return u2f(u << 16); }
DI float bhi(unsigned u) { return u2f(u & 0xffff0000u); }
DI float sum_halves(float v) {
  auto r = __builtin_amdgcn_permlane32_swap(f2u(v), f2u(v), false, false);
  float o; asm("v_add_f32 %0, %1, %2" : "=v"(o) : "v"(r[0]), "v"(r[1])); return o;
}
template <int CTRL> DI float dpp_add(float v) { return v + u2f(__builtin_amdgcn_update_dpp(0u, f2u(v), CTRL, 0xf, 0xf, false)); }
DI float row_sum(float v) { v = dpp_add<0x128>(v); v = dpp_add<0x124>(v); v = dpp_add<0x122>(v); return dpp_add<0x121>(v); }
DI f32x2 pair(const f32x16& a, int j) { return f32x2{a[2 * j], a[2 * j + 1]}; }
DI unsigned phi2(f32x2 x, f32x2 a) {
  const f32x2 t = x * a + 1e-6f;
  const f32x2 e2 = t * 1.4426950408889634f - 1.4426950408889634e-6f;
  const f32x2 e = {__builtin_amdgcn_fmed3f(__builtin_amdgcn_exp2f(e2.x), 0.f, 1.f), __builtin_amdgcn_fmed3f(__builtin_amdgcn_exp2f(e2.y), 0.f, 1.f)};
  const f32x2 mx = {fmaxf(t.x, 1e-6f), fmaxf(t.y, 1e-6f)};
  return pk2(mx + e);
}
constexpr unsigned ONES = 0x3f803f80u;
DI v2u split3(float t) {
  const float r1 = t - blo(pk2(t, t)), r2 = r1 - blo(pk2(r1, r1));
  return v2u{pk2(t, r1), pk2(r2, 0.f)};
}
DI float fadd1(float a, float b) { float r; asm("v_add_f32 %0, %1, %2" : "=v"(r) : "v"(a), "v"(b)); return r; }
DI float fmul1(float a, float b) { float r; asm("v_mul_f32 %0, %1, %2" : "=v"(r) : "v"(a), "v"(b)); return r; }

typedef __amdgpu_buffer_rsrc_t rsrc_t;
DI v4u ld16(rsrc_t rs, int voff, int soff) { return __builtin_amdgcn_raw_buffer_load_b128(rs, voff, soff, 16); }
DI void st16(rsrc_t rs, v4u v, int voff, int soff) {
  __builtin_amdgcn_raw_buffer_store_b128(v, rs, voff, soff, 16);
  asm volatile("s_nop 1" ::"v"(v) : "memory");
}
DI void st8(rsrc_t rs, v2u v, int voff, int soff) { __builtin_amdgcn_raw_buffer_store_b64(v, rs, voff, soff, 16); }

DI int near(int off, int sd) { return off + (((sd ^ (off >> 21)) & 1) << 13); }
DI int tbi(int t) { return (t >> 6) * 32 + (t & 31); }
DI int a_act(int region, int t, int sd) { return near(region + tbi(t) * 65536, sd); }
DI int a_hid(int t, int sd) { return near(WS_H + tbi(t) * 262144, sd); }
DI int a_flag(int region, int t, int sd) { return near(region + (t >> 6) * 16384 + (t & 31) * 256, sd); }
DI int a_stat(int region, int t, int sd) { return near(region + (tbi(t) >> 1) * 16384 + (t & 1) * 4096, sd); }
DI int fr(int f) { return (f >> 3) * 16384 + (f & 7) * 1024; }
struct CW {
  unsigned s, pos;
  unsigned lane_tr;
  unsigned fa;
  unsigned tag, perm;
  int budget;
  int lane, w;
  char* ws;
};
DI bool spent(CW& c) { c.budget -= 1; return c.budget < 0; }
DI unsigned min4(v4u v) { return rfl(min(min(v.x, v.y), min(v.z, v.w))); }
DI void wait_count(CW& c, unsigned have, unsigned addr, unsigned need) {
  if (__builtin_expect(rfl(have) < need, 0)) {
    for (;;) { if (rfl(vld<unsigned>(addr)) >= need || spent(c)) break; }
  }
  CFENCE();
}
DI void wait_slot(CW& c, unsigned have, unsigned t) {
  if (__builtin_expect(have < t, 0)) {
    for (;;) { const v2u v = vld<v2u>(L_PL); if (rfl(min(v.x, v.y)) >= t || spent(c)) break; }
  }
  CFENCE();
}
DI void post(CW& c, int word, unsigned v) { vst<unsigned>(c.fa + (word - L_FCNT), v); }
DI void permit(CW& c, unsigned v) { c.perm = v; post(c, L_FCNT, min(c.s + NSLOT, v)); }
DI void set_flag(CW& c, int off) {
  const char* base = uni(c.ws + off);
  asm volatile("s_mov_b64 exec, 1\n\ts_nop 4\n\tglobal_atomic_swap %0, %1, %2\n\ts_mov_b64 exec, -1" ::"v"(0u), "v"(c.tag), "s"(base) : "memory");
}
DI void wait_flags(CW& c, int off, unsigned long long mask) {
  unsigned f, bud = rfl(c.budget);
  const unsigned voff = c.lane * 4;
  const char* base = uni(c.ws + off);
  asm volatile(
      "s_mov_b64 exec, %5\n\ts_nop 4\n\t"
      "1:\n\t"
      "global_atomic_or %0, %2, %6, %3 sc0\n\t"
      "s_waitcnt vmcnt(0)\n\t"
      "v_cmp_ne_u32 vcc, %4, %0\n\t"
      "s_cbranch_vccz 2f\n\t"
      "s_sub_i32 %1, %1, 8\n\t"
      "s_cmp_ge_i32 %1, 0\n\t"
      "s_cbranch_scc1 1b\n\t"
      "2:\n\t"
      "s_mov_b64 exec, -1\n\t"
      : "=&v"(f), "+s"(bud) : "v"(voff), "s"(base), "s"(c.tag), "s"(mask), "v"(0u) : "memory", "vcc", "scc");
  c.budget = rfl(bud);
}
DI unsigned poll_issue(CW& c, int off, unsigned long long mask) {
  unsigned f = c.tag;
  const unsigned voff = c.lane * 4;
  const char* base = uni(c.ws + off);
  asm volatile("s_mov_b64 exec, %3\n\tv_not_b32 %0, %0\n\ts_nop 4\n\tglobal_atomic_or %0, %1, %4, %2 sc0\n\ts_mov_b64 exec, -1"
               : "+v"(f) : "v"(voff), "s"(base), "s"(mask), "v"(0u) : "memory");
  return f;
}
template <int N> DI void poll_done(CW& c, unsigned f, int off, unsigned long long mask) {
  asm volatile("s_waitcnt vmcnt(%c1)" : "+v"(f) : "i"(N) : "memory");
  if (__builtin_expect(__builtin_amdgcn_ballot_w64(f != c.tag) != 0ull, 0)) wait_flags(c, off, mask);
}

template <int V> struct IC { static constexpr int value = V; DI constexpr operator int() const { return V; } };
template <int I, int E, class F> DI void static_for(F&& f) { if constexpr (I < E) { f(IC<I>{}); static_for<I + 1, E>(f); } }
struct Pipe { bf16x8 wa[PD + 1], wb[PD + 1]; v2u pl; unsigned ad, ad2, adn, have, rel; };
#define SPIN(x) asm volatile("" : "+s"(x))
template <bool Q0> DI void slot_first(CW& c, Pipe& q) {
  if (Q0) { while (rfl(vld<unsigned>(L_RDY)) < 4u && !spent(c)) {} CFENCE(); } else { const v2u v = vld<v2u>(L_PL); wait_slot(c, rfl(min(v.x, v.y)), c.s + 1); }
  q.adn = c.lane_tr + c.pos * 16384u;
}
template <int K, bool NX, int CK, bool Q0 = false> DI void book(CW& c, Pipe& q) {
  if constexpr (CK && K == 0) { if (Q0) q.pl.x = vld<unsigned>(L_RDY + (c.s + 1) * 4); else q.pl = vld<v2u>(L_PL); }
  if constexpr (NX && K == 2) { q.adn = c.lane_tr + ((c.pos + 1) & (NSLOT - 1)) * 16384u; PIN(q.adn); }
  if constexpr (CK && K == 3) { q.have = rfl(Q0 ? q.pl.x : min(q.pl.x, q.pl.y)); SPIN(q.have); }
  if constexpr (CK && K == 4) { if (Q0) wait_count(c, q.have, L_RDY + (c.s + 1) * 4, 4u); else wait_slot(c, q.have, c.s + CK); }
  if constexpr (K == 5) { c.s += 1; c.pos = (c.pos + 1) & (NSLOT - 1); q.rel = min(c.s + NSLOT, c.perm); PIN(q.rel); }
  if constexpr (K == 6) post(c, L_FCNT, q.rel);
}
template <bool SWAP, int I0, int I1, int NEND, bool MORE, bool FR = true, class HOOK>
DI void pair_steps(CW& c, Pipe& q, v4u (&B)[32], f32x16& acc0, f32x16& acc1, HOOK hook) {
  const unsigned keep = c.lane_tr;
  if (FR) c.lane_tr = c.lane * 16;
  static_for<I0, I1>([&](auto ic) __attribute__((always_inline)) {
    constexpr int i = ic.value, n = i + PD;
    constexpr bool F = n < NEND, NX = F && (MORE || n + 8 - n % 8 < NEND);
    constexpr int CK = !NX ? 0 : !FR ? 1 : (n / 8) % 2 ? 2 : 0;
    if constexpr (n == 0) slot_first<!FR>(c, q);
    if constexpr (i >= 0) acc0 = SWAP ? mfma(asb(B[i % 32]), q.wa[i % (PD + 1)], acc0) : mfma(q.wa[i % (PD + 1)], asb(B[i % 32]), acc0);
    SCHED();
    if constexpr (F && n % 8 == 0) q.ad = q.adn;
    if constexpr (F) q.wa[n % (PD + 1)] = FR ? asb(vld<v4u>(q.ad + (n % 8) * 1024)) : ldw(q.ad + (n % 8) * 1024, 512);
    if constexpr (F && n % 8 >= 2) book<n % 8 - 2, NX, CK, !FR>(c, q);
    hook(IC<(i < 0 ? i : i % 32)>{}, IC<0>{});
    SCHED();
    if constexpr (i >= 0) acc1 = SWAP ? mfma(asb(B[i % 32]), q.wb[i % (PD + 1)], acc1) : mfma(q.wb[i % (PD + 1)], asb(B[i % 32]), acc1);
    SCHED();
    if constexpr (F) q.wb[n % (PD + 1)] = FR ? asb(vld<v4u>(q.ad + 8192 + (n % 8) * 1024)) : ldw(q.ad + 8192 + (n % 8) * 1024, 512);
    if constexpr (F && n % 8 == 7) book<6, NX, CK>(c, q);
    hook(IC<(i < 0 ? i : i % 32)>{}, IC<1>{});
    SCHED();
  });
  c.lane_tr = keep;
}
template <bool SWAP, class HOOK>
DI void gemm_pair(CW& c, v4u (&B)[32], f32x16& acc0, f32x16& acc1, HOOK hook) {
  Pipe q;
  pair_steps<SWAP, -PD, 32, 32, false, false>(c, q, B, acc0, acc1, hook);
}
template <class HOOK>
DI void gemm_single(CW& c, v4u (&B)[32], f32x16& acc, HOOK hook) {
  bf16x8 wa[5];
  Pipe q;
  static_for<-4, 32>([&](auto ic) __attribute__((always_inline)) {
    constexpr int i = ic.value, n = i + 4;
    if constexpr (n == 0) { const v2u v = vld<v2u>(L_PL); wait_slot(c, rfl(min(v.x, v.y)), c.s); q.adn = c.lane_tr + c.pos * 16384u; }
    if constexpr (i >= 0) acc = mfma(wa[i % 5], asb(B[i]), acc);
    SCHED();
    if constexpr (n < 32) {
      if constexpr (n % 16 == 0) q.ad = q.adn;
      wa[n % 5] = ldw(q.ad + (n % 16) * 1024, 512);
      if constexpr (n % 16 == 6) book<0, (n < 16), (n < 16)>(c, q);
      if constexpr (n % 16 >= 10) book<n % 16 - 9, (n < 16), (n < 16)>(c, q);
    }
    hook(ic);
    SCHED();
  });
}
constexpr int TL = 6;
template <int PW> struct Wide { bf16x8 wa[PW + 1 + TL], wb[PW + 1]; Pipe q; unsigned dO, lt; };
template <int NK, int I0, int I1, int PW, class HOOK>
DI void wide_steps(CW& c, Wide<PW>& d, v4u (&B)[32], f32x16& accM, f32x16& accO, int kh, HOOK hook) {
  Pipe& q = d.q;
  const unsigned keep = c.lane_tr;
  if constexpr (I0 == -PW) { d.lt = c.lane_tr + kh * (8192u + 512u); d.dO = kh ? -512u : 512u; }
  c.lane_tr = d.lt;
  static_for<I0, I1>([&](auto ic) __attribute__((always_inline)) {
    constexpr int i = ic.value, n = i + PW;
    constexpr bool F = n < NK, NX = F && n + 4 - n % 4 < NK;
    constexpr int CK = !NX || (n / 4) % 2 == 0 ? 0 : n + 8 - n % 4 < NK ? 2 : 1;
    if constexpr (n == 0) slot_first<false>(c, q);
    if constexpr (i >= 0) accO = mfma(d.wb[i % (PW + 1)], asb(B[i % 16]), accO);
    SCHED();
    if constexpr (F && n % 4 == 0) { q.ad = q.adn; if constexpr (n + 3 == NK) q.ad -= kh * 2048u; q.ad2 = q.ad + d.dO; }
    if constexpr (F) d.wb[n % (PW + 1)] = ldw(q.ad2 + (n % 4) * 2048, 1024);
    if constexpr (F && n % 4 == 1) { book<2, NX, CK>(c, q); }
    if constexpr (F && n % 4 == 2) { book<3, NX, CK>(c, q); }
    if constexpr (F && (n % 4 == 3 || n == NK - 1)) { book<5, NX, CK>(c, q); }
    hook(ic, IC<0>{});
    SCHED();
    if constexpr (i >= 0 && i < NK - TL) accM = mfma(d.wa[i % (PW + 1 + TL)], asb(B[i % 16]), accM);
    SCHED();
    if constexpr (F) d.wa[n % (PW + 1 + TL)] = ldw(q.ad + (n % 4) * 2048, 1024);
    if constexpr (F && n % 4 == 0) book<0, NX, CK>(c, q);
    if constexpr (F && n % 4 == 2) book<4, NX, CK>(c, q);
    if constexpr (F && (n % 4 == 3 || n == NK - 1)) book<6, NX, CK>(c, q);
    hook(ic, IC<1>{});
    SCHED();
  });
  c.lane_tr = keep;
}
struct NoHook { template <class A, class B> DI void operator()(A, B) const {} };

DI void ln_factors(float sum, float sq, float& a, unsigned& mf, bool up) {
  float mean = sum * (1.0f / 512.0f);
  PIN(mean);
  const float var = fmaxf(sq * (1.0f / 512.0f) - mean * mean, 0.f);
  a = __builtin_amdgcn_rsqf(var + 1e-5f);
  const unsigned hi = pk2(-mean, -mean);
  const float lo = -mean - blo(hi);
  mf = up ? pk2(lo, lo) : hi;
}
struct Stats { v4u sv[4]; f32x4 t; f32x2 u[4]; float mean, var; };
constexpr int K0 = 34;
DI void stats_hook(CW& c, Stats& t, int k, rsrc_t rs, int soff, float& a, unsigned& mf) {
  if (k == 3 || k == 7 || k == 11 || k == 15) t.sv[k >> 2] = ld16(rs, c.lane * 16 + (k >> 2) * 1024, soff);
  const unsigned sc = L_SCR + (c.w ^ 2) * 4096;
  if (k >= K0 && k < K0 + 4) { const int e = k - K0; t.t[e] = fadd1(fadd1(u2f(t.sv[0][e]), u2f(t.sv[1][e])), fadd1(u2f(t.sv[2][e]), u2f(t.sv[3][e]))); PIN(t.t[e]); }
  if (k == K0 + 4) vst<f32x4>(sc + c.lane * 16, t.t);
  if (k == K0 + 5) { t.u[0] = vld<f32x2>(sc + (c.lane & 31) * 8); t.u[1] = vld<f32x2>(sc + 256 + (c.lane & 31) * 8); }
  if (k == K0 + 6) { t.u[2] = vld<f32x2>(sc + 512 + (c.lane & 31) * 8); t.u[3] = vld<f32x2>(sc + 768 + (c.lane & 31) * 8); }
  if (k == K0 + 10) { t.mean = fadd1(fadd1(t.u[0].x, t.u[1].x), fadd1(t.u[2].x, t.u[3].x)); PIN(t.mean); }
  if (k == K0 + 11) { t.var = fadd1(fadd1(t.u[0].y, t.u[1].y), fadd1(t.u[2].y, t.u[3].y)); PIN(t.var); }
  if (k == K0 + 12) { t.mean *= -1.0f / 512.0f; PIN(t.mean); t.var = t.var * (1.0f / 512.0f) + 1e-5f; PIN(t.var); }
  if (k == K0 + 13) { t.var = fmaxf(t.var - t.mean * t.mean, 1e-5f); PIN(t.var); a = __builtin_amdgcn_rsqf(t.var); PIN(a); }
  if (k == K0 + 14) { mf = pk2(t.mean, t.mean); t.mean -= blo(mf); PIN(mf); PIN(t.mean); }
  if (k == K0 + 15) { mf = c.lane >= 32 ? pk2(t.mean, t.mean) : mf; PIN(mf); }
}
DI f32x16 unpack(const unsigned (&R)[8]) {
  f32x16 a;
#pragma unroll
  for (int j = 0; j < 8; ++j) { a[2 * j] = blo(R[j]); a[2 * j + 1] = bhi(R[j]); }
  return a;
}
template <int NK, int PW>
DI void finish_wide(CW& c, Wide<PW>& d, v4u (&B)[32], f32x16& accM, const f32x16& accO, unsigned (&R)[8], unsigned seq) {
  const unsigned mine = L_SCR + c.w * 4096 + c.lane * 16, other = L_SCR + (c.w ^ 2) * 4096 + c.lane * 16;
  auto step = [&](int k) __attribute__((always_inline)) { accM = mfma(d.wa[(NK - TL + k) % (PW + 1 + TL)], asb(B[(NK - TL + k) % 16]), accM); SCHED(); };
  auto put = [&](int g) __attribute__((always_inline)) { vst<f32x4>(mine + g * 1024, f32x4{accO[4 * g], accO[4 * g + 1], accO[4 * g + 2], accO[4 * g + 3]}); };
  step(0); step(1);
  put(0); put(1); SCHED();
  step(2);
  put(2); put(3); post(c, L_F3, seq); SCHED();
  step(3); step(4);
  unsigned fl = vld<unsigned>(L_F3 + (c.w ^ 2) * 4);
  f32x4 v[4];
#pragma unroll
  for (int g = 0; g < 4; ++g) v[g] = vld<f32x4>(other + g * 1024);
  SCHED();
  step(5);
  if (rfl(fl) < seq) {
    for (;;) {
      fl = vld<unsigned>(L_F3 + (c.w ^ 2) * 4);
#pragma unroll
      for (int g = 0; g < 4; ++g) v[g] = vld<f32x4>(other + g * 1024);
      if (rfl(fl) >= seq || spent(c)) break;
    }
  }
#pragma unroll
  for (int g = 0; g < 4; ++g) {
    R[2 * g] = pk2(pair(accM, 2 * g) + f32x2{v[g].x, v[g].y});
    R[2 * g + 1] = pk2(pair(accM, 2 * g + 1) + f32x2{v[g].z, v[g].w});
  }
}
DI void publish_x(CW& c, rsrc_t rs, const unsigned (&R)[8], int xoff, int stoff) {
  st16(rs, v4u{R[0], R[1], R[2], R[3]}, c.lane * 16, xoff);
  st16(rs, v4u{R[4], R[5], R[6], R[7]}, c.lane * 16 + 1024, xoff);
  SCHED();
  float s0 = 0.f, q0 = 0.f, s1 = 0.f, q1 = 0.f;
#pragma unroll
  for (int i = 0; i < 4; ++i) { s0 = dot2(R[i], ONES, s0); q0 = dot2(R[i], R[i], q0); s1 = dot2(R[4 + i], ONES, s1); q1 = dot2(R[4 + i], R[4 + i], q1); }
  const float s = sum_halves(s0 + s1), q = sum_halves(q0 + q1);
  st8(rs, v2u{f2u(s), f2u(q)}, (c.lane & 31) * 8, stoff);
}

DI void dma4(const char* base, unsigned v0, unsigned v1, unsigned v2, unsigned v3, unsigned dst) {
  asm volatile("s_mov_b32 m0, %5\n\ts_nop 3\n\t"
               "global_load_lds_dwordx4 %0, %4\n\tglobal_load_lds_dwordx4 %1, %4 offset:1024\n\t"
               "global_load_lds_dwordx4 %2, %4 offset:2048\n\tglobal_load_lds_dwordx4 %3, %4 offset:3072"
               ::"v"(v0), "v"(v1), "v"(v2), "v"(v3), "s"(uni(base)), "s"(rfl(dst)) : "memory");
}
#define PACE "s_sleep 1\n\t"
DI void dma4_sc1(const char* base, unsigned v, unsigned dst) {
  asm volatile("s_mov_b32 m0, %2\n\ts_nop 3\n\t"
               "global_load_lds_dwordx4 %0, %1 sc1\n\t" PACE "global_load_lds_dwordx4 %0, %1 offset:1024 sc1\n\t" PACE
               "global_load_lds_dwordx4 %0, %1 offset:2048 sc1\n\t" PACE "global_load_lds_dwordx4 %0, %1 offset:3072 sc1\n\t" PACE
               ::"v"(v), "s"(uni(base)), "s"(rfl(dst)) : "memory");
}
DI void dma4_sc1f(const char* base, unsigned v, unsigned dst) {
  asm volatile("s_mov_b32 m0, %2\n\ts_nop 3\n\t"
               "global_load_lds_dwordx4 %0, %1 sc1\n\tglobal_load_lds_dwordx4 %0, %1 offset:1024 sc1\n\t"
               "global_load_lds_dwordx4 %0, %1 offset:2048 sc1\n\tglobal_load_lds_dwordx4 %0, %1 offset:3072 sc1"
               ::"v"(v), "s"(uni(base)), "s"(rfl(dst)) : "memory");
}
template <int N> DI void wait_vm() { asm volatile("s_waitcnt vmcnt(%c0)" ::"i"(N) : "memory"); }
DI unsigned tr_lane(int lane) { return (4 * (lane >> 5) + ((lane & 15) >> 2)) * 64 + ((lane >> 4) & 1) * 32 + (lane & 3) * 8; }
DI int fresh_lane() { int l; asm volatile("v_mbcnt_lo_u32_b32 %0, -1, 0\n\tv_mbcnt_hi_u32_b32 %0, -1, %0" : "=v"(l)); return l; }
DI void lds_add(unsigned pa, unsigned off, unsigned n) { CFENCE(); (void)__hip_atomic_fetch_add(lp<unsigned>(pa + off), n, __ATOMIC_RELAXED, __HIP_MEMORY_SCOPE_WORKGROUP); CFENCE(); }
DI void loader(const Params& P, int p, int m, int lw) {
  const int lane = fresh_lane();
  const unsigned smem_base = (unsigned)(uintptr_t)(LAS char*)smem;
  const unsigned nr = GM(m) < 11 ? 3 : 2, NS = 21 + 4 * nr, NT = 2 * NS;
  const unsigned v_lin = lane * 16, v_wide = ((lane & 31) >> 2) * 1024 + (lane >> 5) * 64 + (lane & 3) * 16, v_raw = (lane >> 2) * 3072 + (lane & 3) * 16;
  const unsigned pa = lane == 0 ? L_RDY : L_DUMMY + lane * 4;
  const unsigned pp = pa + (L_PL - L_RDY) + lw * 4;
  if (blockIdx.x < 16) {
    const unsigned vi = blockIdx.x * 2 + lw, first = lw * 8192u, poff = WS_PROBE + vi * 128u;
    unsigned long long t0, t1, t2; unsigned j0, j1, epoch;
    asm volatile("s_load_dword %5, %8, 0x0\n\ts_load_dword %3, %6, 0x1000\n\ts_waitcnt lgkmcnt(0)\n\t"
                 "s_memtime %0\n\ts_waitcnt lgkmcnt(0)\n\ts_load_dword %3, %6, 0x0\n\ts_waitcnt lgkmcnt(0)\n\ts_memtime %1\n\ts_waitcnt lgkmcnt(0)\n\t"
                 "s_load_dword %4, %7, 0x0\n\ts_waitcnt lgkmcnt(0)\n\ts_memtime %2\n\ts_waitcnt lgkmcnt(0)"
                 : "=&s"(t0), "=&s"(t1), "=&s"(t2), "=&s"(j0), "=&s"(j1), "=&s"(epoch)
                 : "s"(uni(P.ws + poff + first)), "s"(uni(P.ws + poff + (first ^ 8192u))), "s"(uni(P.ws)) : "memory");
    int d = min(max((int)((unsigned)(t1 - t0) - (unsigned)(t2 - t1)), -400), 400);
    if ((lw ^ (p >> 3) ^ (poff >> 21)) & 1) d = -d;
    st8(__builtin_amdgcn_make_buffer_rsrc((void*)P.ws, 0, WS_END, 0x00020000), v2u{(unsigned)d, epoch + 1u}, 0, WS_VOTE + vi * 8);
  }
  __builtin_amdgcn_s_setprio(0);
  unsigned limit = 0;
  int budget = sopaque(BUDGET);
  bool pend = false;
  auto entry = [&](unsigned s) __attribute__((always_inline)) -> unsigned long long {
    const bool l2 = s >= NS;
    const unsigned q = lane & 3, i = l2 ? s - NS : s, g = i - 10u, d = g - 4u * nr, r4 = 4u * ROT(m);
    const unsigned blk = i < 4u ? q >> 1 : 2u, ks0 = ((i < 4u ? 8u * i + (q & 1) * 4u : 16u * (i - 4u) + q * 4u) + r4) & 31u;
    if (i < 6u && l2) return (unsigned long long)P.ws + WS_RQ + ((m * 3 + blk) * 32u + ks0) * 1024u + 1u;
    if (i < 6u) return (unsigned long long)P.wqkv + m * 64 + ks0 * (16u * 3072u);
    if (i < 10u) return (unsigned long long)P.wo + ((i - 6u) * 128u + q * 32u) * 1024u + (m & 7) * 128 + 2u;
    if (g < 4u * nr) return (unsigned long long)P.ws + ((q >> 1) ? WS_RU : WS_RG) + ((16u * (g >> 2) + GM(m)) * 32u + ((8u * (g & 3u) + (q & 1) * 4u + r4) & 31u)) * 1024u + 1u;
    return (unsigned long long)P.wd + min((d < 8u ? (d & 4u) | ((d + ROT(m)) & 3u) : d) * 128u + q * 32u, 1344u) * 1024u + (m & 7) * 128 + 2u;
  };
  unsigned long long tab = entry(4u + lw + 2u * (lane >> 2));
  const unsigned long long tab2 = entry(36u + lw + 2u * (lane >> 2));
  for (unsigned s = 4 + lw, t = 0; s < NT; s += 2, t = (t + 4) & 63u) {
    const unsigned i = s >= NS ? s - NS : s;
    if (s >= 36u) tab = tab2;
    const char* src[4];
#pragma unroll
    for (int q = 0; q < 4; ++q)
      src[q] = (const char*)(((unsigned long long)__builtin_amdgcn_readlane((unsigned)(tab >> 32), t + q) << 32) | (__builtin_amdgcn_readlane((unsigned)tab, t + q) & ~3u));
    const unsigned kind = __builtin_amdgcn_readlane((unsigned)tab, t) & 3u;
    if (pend && i < 18u) {
      wait_vm<0>(); vst<unsigned>(pp, s - 1); pend = false; }
    while (s >= limit) {
      limit = min4(vld<v4u>(L_FCNT));
      if (s < limit) break;
      if (pend) { wait_vm<0>(); vst<unsigned>(pp, s - 1); pend = false; continue; }
      __builtin_amdgcn_s_sleep(1);
      if (--budget < 0) break;
    }
    const unsigned dst = smem_base + L_RING + (s & (NSLOT - 1)) * 16384u;
    const unsigned v0 = kind == 2u ? v_wide : v_raw, st = kind == 2u ? 7168u : 48128u;
    if (i - 10u < 8u) {
      dma4_sc1(src[0], v_lin, dst); dma4_sc1(src[1], v_lin, dst + 4096); dma4_sc1(src[2], v_lin, dst + 8192); dma4_sc1(src[3], v_lin, dst + 12288);
    } else if (kind == 1u) {
      dma4_sc1f(src[0], v_lin, dst); dma4_sc1f(src[1], v_lin, dst + 4096); dma4_sc1f(src[2], v_lin, dst + 8192); dma4_sc1f(src[3], v_lin, dst + 12288);
    } else {
#pragma unroll
      for (int q = 0; q < 4; ++q) dma4(src[q], v0, v0 + st, v0 + 2 * st, v0 + 3 * st, dst + q * 4096);
    }
    if (pend) { wait_vm<16>(); vst<unsigned>(pp, s - 1); }
    pend = true;
    if (s < 6u) {
      wait_vm<0>(); vst<unsigned>(pp, s + 1); pend = false;
      __builtin_amdgcn_s_setprio(3);
      const unsigned a = tr_lane(lane) + s * 16384u;
      float c0 = 0.f, c1 = 0.f;
#pragma unroll
      for (int n = 0; n < 16; ++n) {
        const v4u f = __builtin_bit_cast(v4u, ldw(a + n * 1024, 512));
        c0 = dot2(f.x, ONES, c0); c1 = dot2(f.y, ONES, c1); c0 = dot2(f.z, ONES, c0); c1 = dot2(f.w, ONES, c1);
      }
      const float t = sum_halves(c0 + c1);
      vst<v2u>(L_CSF + 2 * 512 + (lane & 31) * 16 + lw * 8, split3(t));
      lds_add(pa, L_CSC - L_RDY + 4, 1);
      __builtin_amdgcn_s_setprio(0);
    }
    if (s + 2 >= NT) { wait_vm<0>(); vst<unsigned>(pp, s + 1); }
  }
}
DI void server(const Params& P, int p, int m, int sw) {
  const int lane = fresh_lane();
  unsigned epoch;
  asm volatile("s_load_dword %0, %1, 0x0" : "=s"(epoch) : "s"(P.ws) : "memory");
  const unsigned pa = lane == 0 ? L_RDY : L_DUMMY + lane * 4;
  const rsrc_t rs = __builtin_amdgcn_make_buffer_rsrc((void*)P.ws, 0, WS_END, 0x00020000);
  CW c;
  c.lane = lane; c.ws = P.ws; c.budget = sopaque(BUDGET);
  asm volatile("s_waitcnt lgkmcnt(0)" : "+s"(epoch) :: "memory");
  c.tag = epoch + 1u;
  const int co = WS_CSP + (m * 2 + sw) * 16 * 768;
  DRAIN();
  lds_add(pa, L_RPD - L_RDY, 1);
  {
    const unsigned idx = (blockIdx.x * 2 + sw) * 4 + (lane & 3), line = (idx / 3u) % 576u;
    asm volatile("s_mov_b64 exec, 0xf\n\ts_nop 4\n\tglobal_atomic_or %0, %1, %2\n\ts_mov_b64 exec, -1" ::"v"(WS_FKV + line * 256u + (idx % 3u) * 64u), "v"(0u), "s"(uni(P.ws)) : "memory");
  }
  {
    {
      const unsigned b = blockIdx.x;
      const char* tb = sw ? P.wo : P.wd;
      const unsigned to = sw ? (b * 16u + (lane & 15)) * 128u : (b * 43u + min(lane, 42)) * 128u;
      asm volatile("s_mov_b32 m0, %2\n\ts_nop 3\n\tglobal_load_lds_dword %0, %1" :: "v"(to), "s"(uni(tb)), "s"((unsigned)(uintptr_t)(LAS char*)smem + L_DUMMY) : "memory");
    }
    while (rfl(vld<unsigned>(L_RPD)) < 2u && !spent(c)) __builtin_amdgcn_s_sleep(1);
  }
  for (unsigned layer = 0; layer < 2; ++layer) {
    __builtin_amdgcn_s_setprio(3);
    f32x4 tk[4], tz[4];
    const int g0 = WS_KVP + ((layer * 16 + m) * 2 + sw) * 65536, f0 = WS_FKV + layer * 16384 + (m * 2 + sw) * 256, po = g0 + fr(2 * p);
    while (rfl(vld<unsigned>(L_FCNT)) < layer * (GM(m) < 11 ? 42u : 38u) && !spent(c)) __builtin_amdgcn_s_sleep(8);
    v4u vt = ld16(rs, (lane & 15) * 16, WS_VOTE);
    for (;;) {
      const v4u fl = vld<v4u>(L_F1);
      if (min4(fl) > layer || spent(c)) break;
      __builtin_amdgcn_s_sleep(1);
    }
#pragma unroll
    for (int u = 0; u < 4; ++u) { tk[u] = vld<f32x4>(L_KVS + u * 2048 + sw * 1024 + lane * 16); tz[u] = vld<f32x4>(L_ZS + u * 128 + (lane & 1) * 64 + sw * 32 + (lane & 2) * 8); }
    v4u sk, sz;
#pragma unroll
    for (int e = 0; e < 4; ++e) { sk[e] = f2u(fadd1(fadd1(tk[0][e], tk[1][e]), fadd1(tk[2][e], tk[3][e]))); sz[e] = f2u(fadd1(fadd1(tz[0][e], tz[1][e]), fadd1(tz[2][e], tz[3][e]))); }
    st16(rs, sk, lane * 16, po); st16(rs, sk, lane * 16, po + 8192);
    st16(rs, sz, 1024 + (lane & 7) * 16 + (lane & 8) * 1024, po);
    DRAIN();
    asm volatile("s_mov_b64 exec, 3\n\ts_nop 4\n\tglobal_atomic_swap %0, %1, %2\n\ts_mov_b64 exec, -1" ::"v"(lane * 8192), "v"(c.tag), "s"(uni(P.ws + f0 + p * 4)) : "memory");
    while (__builtin_amdgcn_ballot_w64(vt.y != c.tag || vt.w != c.tag) != 0ull && !spent(c)) { CFENCE(); vt = ld16(rs, (lane & 15) * 16, WS_VOTE); }
    const int cbit = rfl(row_sum((float)((int)vt.x + (int)vt.z)) > 0.f ? 1u : 0u);
    vst<v2u>(L_CB, v2u{(unsigned)cbit, c.tag});
    const int go = near(g0, cbit ^ (p >> 3)), fo = near(f0, cbit ^ (p >> 3));
    wait_flags(c, fo, 0xffffull);
    const v4u zl = ld16(rs, 1024 + fr(2 * (lane & 15)) + (lane >> 4) * 16, go);
    v4u g[16];
#pragma unroll
    for (int i = 0; i < 16; ++i) { SCHED(); g[i] = ld16(rs, lane * 16 + (i & 1) * 2048, go + fr(4 * (i >> 1))); }
    SCHED();
    const float z0 = row_sum(u2f(zl.x)), z1 = row_sum(u2f(zl.y)), z2 = row_sum(u2f(zl.z)), z3 = row_sum(u2f(zl.w));
    if ((lane & 15) == 0) vst<v2u>(L_ZR + (lane >> 4) * 16 + sw * 8, v2u{pk2(z0, z1), pk2(z2, z3)});
    f32x4 r = __builtin_bit_cast(f32x4, g[0]);
#pragma unroll
    for (int i = 1; i < 16; ++i) r += __builtin_bit_cast(f32x4, g[i]);
    vst<v2u>(L_KVR + lane * 16 + sw * 8, v2u{pk2(r.x, r.y), pk2(r.z, r.w)});
    lds_add(pa, L_F2 - L_RDY, 2);
    __builtin_amdgcn_s_setprio(0);
    if (layer == 0) {
      v4u t[16];
#pragma unroll
      for (int i = 0; i < 16; ++i) t[i] = ld16(rs, min(lane, 47) * 16, co + i * 768);
      SCHED();
      f32x4 v = __builtin_bit_cast(f32x4, t[0]);
#pragma unroll
      for (int i = 1; i < 16; ++i) v += __builtin_bit_cast(f32x4, t[i]);
      if (lane < 48) {
#pragma unroll
        for (int e = 0; e < 4; ++e) vst<v2u>(L_CSF + 2048 + (lane * 4 + e) * 16 + sw * 8, split3(v[e]));
      }
      lds_add(pa, L_CSC - L_RDY + 8, 2); lds_add(pa, L_CSC - L_RDY + 12, 2); lds_add(pa, L_CSC - L_RDY + 16, 2);
    }
  }
}
template <int AT, bool TWO> DI void cs_hook(CW& c, int i, unsigned k, v4u& a, v4u& b) {
  if (i == AT) a.x = vld<unsigned>(L_CSC + k * 4);
  if (i == AT + 3) {
    wait_count(c, a.x, L_CSC + k * 4, TWO ? 4u : 2u);
    a = vld<v4u>(L_CSF + k * 1024 + (c.lane & 31) * 16);
    if (TWO) b = vld<v4u>(L_CSF + k * 1024 + 512 + (c.lane & 31) * 16);
  }
}

__global__ void __launch_bounds__(512, 2) fused_fwd(Params P) {
  const int tid = threadIdx.x; int lane = tid & 63; const int wv = __builtin_amdgcn_readfirstlane(tid >> 6);
  const int b = blockIdx.x;
  P.ws += (0u - (unsigned)(uintptr_t)P.ws) & 0x1fffffu;
  {
    const unsigned co = CODE_BYTES - 256u - min(((b >> 3) * 8 + wv) * 256u, CODE_BYTES - 256u) + lane * 4;
    unsigned long long pc;
    asm volatile("s_mov_b32 m0, %2\n\ts_getpc_b64 %0\n\tglobal_load_lds_dword %1, %0" : "=&s"(pc) : "v"(co), "s"((unsigned)(uintptr_t)(LAS char*)smem + L_DUMMY) : "memory");
  }
  int p = ((b & 7) >> 1) * 4 + (b >> 6), m = (b & 1) * 8 + ((b >> 3) & 7);
  if (tid < (L_END - L_RDY) / 4) vst<unsigned>(L_RDY + tid * 4, tid == 4 || tid == 5 ? tid - 1u : 0u);
  asm volatile("s_waitcnt lgkmcnt(0)\n\ts_barrier" ::: "memory");
  if (wv >= 4) __builtin_amdgcn_s_setprio(3);
  if (b < 16 && (wv & 6) == 4) {
    const unsigned po = WS_PROBE + (b * 2 + (wv & 1)) * 128u + (lane & 1) * 64 + (lane >> 1) * 4096;
    asm volatile("s_mov_b64 exec, 0xff\n\ts_nop 4\n\tglobal_atomic_or %0, %1, %2\n\ts_mov_b64 exec, -1" ::"v"(po), "v"(0u), "s"(uni(P.ws)) : "memory");
  }
  const unsigned smem_base = (unsigned)(uintptr_t)(LAS char*)smem;
  int w = wv & 3;
  unsigned xv[4];
  const char* xbase = (wv < 4 ? P.x : P.wqkv) + (unsigned)sopaque(wv < 4 ? (4 * p + w) * 32768 : ((w >> 1) + 1) * 1024 + m * 64);
  auto xbuf = [&](int q) -> unsigned { const int j = q % 5; return (j == 0 ? L_SCR : j == 1 ? 7 * 16384 : j == 2 ? 6 * 16384 : j == 3 ? 4 * 16384 : 5 * 16384) + w * 4096; };
  {
#pragma unroll
    for (int k = 0; k < 4; ++k) {
      const unsigned a = (8 * k + (lane >> 3)) * 1024 + (((lane & 7) ^ ((4 * k + (lane >> 4)) & 7)) * 16) - k * 1024;
      const unsigned c = (lane >> 2) * 3072 + (lane & 3) * 16 + k * 48128;
      xv[k] = wv < 4 ? a : c;
    }
    const unsigned cw = wv < 4, stride = sopaque(cw ? 128 : 64 * 3072), r0 = ROT(m) + (cw ? 0 : (w & 1));
    dma4(xbase + (r0 & 7) * stride, xv[0], xv[1], xv[2], xv[3], smem_base + (cw ? xbuf(0) : w * 4096));
    if (!cw) {
      const unsigned pa = smem_base + (lane == 0 ? L_RDY : L_DUMMY + lane * 4), a = tr_lane(lane) + w * 4096u;
      float c0 = 0.f, c1 = 0.f;
      auto cs = [&](int j) __attribute__((always_inline)) {
#pragma unroll
        for (int n = 0; n < 4; ++n) {
          const v4u f = __builtin_bit_cast(v4u, ldw(a + j * 16384 + n * 1024, 512));
          c0 = dot2(f.x, ONES, c0); c1 = dot2(f.y, ONES, c1); c0 = dot2(f.z, ONES, c0); c1 = dot2(f.w, ONES, c1);
        }
        PIN(c0); PIN(c1);
      };
      { unsigned e; asm volatile("s_load_dword %0, %1, 0x0\n\ts_waitcnt lgkmcnt(0)" : "=s"(e) : "s"(uni(P.ws)) : "memory"); }
      asm volatile("s_waitcnt vmcnt(0)\n\tds_add_u32 %0, %1" ::"v"(pa), "v"(1u) : "memory");
      dma4(xbase + ((2 + r0) & 7) * stride, xv[0], xv[1], xv[2], xv[3], smem_base + 1 * 16384 + w * 4096);
      cs(0);
      asm volatile("s_waitcnt vmcnt(0)\n\tds_add_u32 %0, %1 offset:4" ::"v"(pa), "v"(1u) : "memory");
      dma4(xbase + ((4 + r0) & 7) * stride, xv[0], xv[1], xv[2], xv[3], smem_base + 2 * 16384 + w * 4096);
      cs(1);
      asm volatile("s_waitcnt vmcnt(0)\n\tds_add_u32 %0, %1 offset:8" ::"v"(pa), "v"(1u) : "memory");
      dma4(xbase + ((6 + r0) & 7) * stride, xv[0], xv[1], xv[2], xv[3], smem_base + 3 * 16384 + w * 4096);
      cs(2);
      asm volatile("s_waitcnt vmcnt(0)\n\tds_add_u32 %0, %1 offset:12" ::"v"(pa), "v"(1u) : "memory");
      const int ks = 2 * p + (w & 1), r2 = GM(m) < 11 ? 2 : 1;
      v4u rp[9];
      if (w >= 2) {
        __builtin_amdgcn_s_setprio(0);
        const GAS char* sq = (const GAS char*)P.wqkv + m * 64 + ks * (16 * 3072) + xv[0];
        const unsigned vg = GM(m) * 64 + ks * (16 * 2752) + (lane >> 2) * 2752 + (lane & 3) * 16;
        const GAS char* sg = (const GAS char*)P.wg + vg; const GAS char* su = (const GAS char*)P.wu + vg;
        rp[0] = *(const GAS v4u*)(sg); rp[1] = *(const GAS v4u*)(su); rp[2] = *(const GAS v4u*)(sg + 1024); rp[3] = *(const GAS v4u*)(su + 1024);
        rp[4] = *(const GAS v4u*)(sg + r2 * 1024); rp[5] = *(const GAS v4u*)(su + r2 * 1024);
        rp[6] = *(const GAS v4u*)(sq + 1024); rp[7] = *(const GAS v4u*)(sq + 2048); rp[8] = *(const GAS v4u*)(sq);
        SCHED();
        __builtin_amdgcn_s_setprio(3);
      }
      cs(3);
      const float t = sum_halves(c0 + c1);
      vst<v2u>(L_CSF + (w >> 1) * 512 + (lane & 31) * 16 + (w & 1) * 8, split3(t));
      asm volatile("ds_add_u32 %0, %1 offset:%c2" ::"v"(pa), "v"(1u), "i"(L_CSC - L_RDY) : "memory");
      if (sopaque(w) >= 2) {
        const rsrc_t rs = __builtin_amdgcn_make_buffer_rsrc((void*)P.ws, 0, WS_END, 0x00020000);
        const int dg = (GM(m) * 32 + ks) * 1024, dq = WS_RQ + (m * 96 + ks) * 1024;
        const unsigned SCX = L_CSF + 3072 + (w & 1) * 1024;
        v4u f[8];
#pragma unroll
        for (int u = 0; u < 8; ++u) {
          vst<v4u>(SCX + lane * 16, rp[u]);
          CFENCE();
          f[u] = __builtin_bit_cast(v4u, ldw(SCX + tr_lane(lane), 512));
          CFENCE();
        }
        st16(rs, f[0], lane * 16, WS_RG + dg); st16(rs, f[1], lane * 16, WS_RU + dg);
        st16(rs, f[2], lane * 16, WS_RG + dg + 16 * 32768); st16(rs, f[3], lane * 16, WS_RU + dg + 16 * 32768);
        st16(rs, f[4], lane * 16, WS_RG + dg + r2 * (16 * 32768)); st16(rs, f[5], lane * 16, WS_RU + dg + r2 * (16 * 32768));
        st16(rs, f[6], lane * 16, dq); st16(rs, f[7], lane * 16, dq + 32768); st16(rs, rp[8], lane * 16, dq + 65536);
        float cs6[6];
#pragma unroll
        for (int u = 0; u < 6; ++u) cs6[u] = dot2(f[u].x, ONES, dot2(f[u].y, ONES, dot2(f[u].z, ONES, dot2(f[u].w, ONES, 0.f))));
#pragma unroll
        for (int u = 0; u < 3; ++u) {
          const auto r = __builtin_amdgcn_permlane32_swap(f2u(cs6[2 * u]), f2u(cs6[2 * u + 1]), false, false);
          __builtin_amdgcn_raw_buffer_store_b32(f2u(u2f(r[0]) + u2f(r[1])), rs, lane * 4 + u * 256, WS_CSP + ((m * 2 + (w & 1)) * 16 + p) * 768, 16);
        }
      }
      __builtin_amdgcn_s_waitcnt(0x0F70);
    }
  }
  if (sopaque(wv) >= 6) { server(P, p, m, wv - 6); return; }
  if (sopaque(wv) >= 4) { loader(P, p, m, wv - 4); return; }
  __builtin_amdgcn_s_setprio(2);

  v4u B[32];
  auto xdma = [&](int q) __attribute__((always_inline)) { dma4(xbase + ((q + ROT(m)) & 7) * 128, xv[0], xv[1], xv[2], xv[3], smem_base + xbuf(q)); };
  xdma(1);
  const rsrc_t rs = __builtin_amdgcn_make_buffer_rsrc((void*)P.ws, 0, WS_END, 0x00020000);
  CW c;
  c.s = 0; c.pos = 0; c.perm = 4; c.lane_tr = tr_lane(lane); c.fa = lane == 0 ? L_FCNT + w * 4 : L_DUMMY + lane * 4;
  c.budget = sopaque(BUDGET); c.lane = lane; c.w = w; c.ws = P.ws; c.tag = 0;
  int h, t32, tbq, tbw, kh, cb;
  int sdn = 0;
#define FRESH() do { lane = opaque(lane); h = lane >> 5; t32 = lane & 31; c.lane = lane; p = sopaque(p); m = sopaque(m); w = sopaque(w); c.w = w; \
    tbq = layer * 64 + 4 * p + w; kh = w >> 1; tbw = layer * 64 + 4 * p + 2 * (m >> 3) + (w & 1); cb = 2 * (m & 7) + kh; } while (0)
  unsigned R[8];

  int layer = 0;
    f32x16 aK = {}, aV = {};
    float la; unsigned mf;
    f32x4 ar[4];
    v4u ck, cv;
    FRESH();
    auto rows_ab = [&]() __attribute__((always_inline)) {
      vst<float>(L_SA + w * 128 + t32 * 4, la);
#pragma unroll
      for (int g = 0; g < 4; ++g) ar[g] = vld<f32x4>(L_SA + w * 128 + (8 * g + 4 * h) * 4);
    };
    {
      float s1 = 0.f, s2 = 0.f, s3 = 0.f, s4 = 0.f;
      const unsigned xl = t32 * 128 + 8 * h, xf = (t32 >> 1) & 7;
      auto hook = [&](int i, int hf) __attribute__((always_inline)) {
        const int n = i + 2;
        if (n >= 0 && n < 32 && hf == 0) {
          const int q = n / 4, j = n % 4;
          if (j == 0) {
            if (q == 0) wait_vm<4>();
            if (q >= 1 && q <= 4) wait_vm<12>();
            if (q == 5) wait_vm<8>();
            if (q == 6) wait_vm<4>();
            if (q == 7) wait_vm<0>();
          }
          const v2u lo = vld<v2u>(xbuf(q) + xl + (((2 * j) ^ xf) * 16)), hi = vld<v2u>(xbuf(q) + xl + (((2 * j + 1) ^ xf) * 16));
          B[n] = v4u{lo.x, lo.y, hi.x, hi.y};
        }
        if (hf == 1) {
          if (i == -1) xdma(2);
          if (i == 0) xdma(3);
          if (i == 1) xdma(4);
          if (i == 3 || i == 7 || i == 11) xdma(5 + i / 4);
          if (i >= 0 && i % 4 == 3) {
#pragma unroll
            for (int u = i - 3; u <= i; ++u) {
              const v4u f = B[u];
              s1 = dot2(f.x, ONES, s1); s2 = dot2(f.x, f.x, s2); s3 = dot2(f.y, ONES, s3); s4 = dot2(f.y, f.y, s4);
              s1 = dot2(f.z, ONES, s1); s2 = dot2(f.z, f.z, s2); s3 = dot2(f.w, ONES, s3); s4 = dot2(f.w, f.w, s4);
            }
            PIN(s1); PIN(s2); PIN(s3); PIN(s4);
          }
          cs_hook<26, true>(c, i, 0, ck, cv);
          if (n == 15 || n == 19) permit(c, n / 4 + 2);
          if (i == 30) permit(c, 10);
        }
      };
      gemm_pair<true>(c, B, aK, aV, hook);
      ln_factors(sum_halves(s1 + s3), sum_halves(s2 + s4), la, mf, h != 0);
      rows_ab();
    }
  for (; layer < 2; ++layer) {
    const bf16x8 mfr = asb(v4u{mf, mf, mf, mf});
    aK = mfma(mfr, asb(ck), aK);
    aV = mfma(mfr, asb(cv), aV);
    FRESH();
    {
      unsigned pkk[8], pkv[8];
#pragma unroll
      for (int j = 0; j < 8; ++j) {
        const f32x2 a = (j & 1) ? f32x2{ar[j >> 1].z, ar[j >> 1].w} : f32x2{ar[j >> 1].x, ar[j >> 1].y};
        pkk[j] = phi2(pair(aK, j), a);
        pkv[j] = pk2(pair(aV, j) * a);
      }
      const bf16x8 k0 = asb(v4u{pkk[0], pkk[1], pkk[2], pkk[3]}), k1 = asb(v4u{pkk[4], pkk[5], pkk[6], pkk[7]});
      const bf16x8 v0 = asb(v4u{pkv[0], pkv[1], pkv[2], pkv[3]}), v1 = asb(v4u{pkv[4], pkv[5], pkv[6], pkv[7]});
      const bf16x8 one = asb(v4u{ONES, ONES, ONES, ONES});
      f32x16 kv = {}, zz = {};
      unsigned ka = L_KVS + w * 2048 + lane * 16, za = L_ZS + w * 128 + t32 * 4;
      PIN(ka); PIN(za);
      SCHED();
      kv = mfma(k0, v0, kv); kv = mfma(k1, v1, kv); zz = mfma(one, k0, zz); zz = mfma(one, k1, zz);
      SCHED();
      const bool hd = (lane & 16) != 0;
      f32x4 q0, q1;
#pragma unroll
      for (int j = 0; j < 4; ++j) { q0[j] = sel(hd, kv[8 + j], kv[j]); q1[j] = sel(hd, kv[12 + j], kv[4 + j]); }
      vst<f32x4>(ka, q0);
      vst<f32x4>(ka + 1024, q1);
      vst<float>(za, zz[0]);
      post(c, L_F1, layer + 1);
    }
    unsigned pq[8];
    {
      f32x16 aQ = {};
      v4u c0;
      auto hook = [&](int i) __attribute__((always_inline)) {
        if (i == 20) {
          if (layer == 0) {
            const GAS char* xr = (const GAS char*)P.x + (size_t)(tbw * 32 + t32) * 1024 + cb * 64 + h * 8;
#pragma unroll
            for (int g = 0; g < 4; ++g) { const v2u v = *(const GAS v2u*)(xr + g * 16); R[2 * g] = v.x; R[2 * g + 1] = v.y; }
          }
        }
        cs_hook<24, false>(c, i, 1, c0, c0);
      };
      gemm_single(c, B, aQ, hook);
      aQ = mfma(asb(c0), mfr, aQ);
#pragma unroll
      for (int j = 0; j < 8; ++j) { pq[j] = phi2(pair(aQ, j), f32x2{la, la}); PIN(pq[j]); }
    }
    {
      FRESH();
      f32x16 aM = {}, aO = {};
      Wide<16> d;
      const unsigned s0 = c.s;
      wide_steps<16, -16, 0>(c, d, B, aM, aO, kh, NoHook());
      v4u fk, fz; v2u cbv;
      for (;;) {
        const unsigned fl = vld<unsigned>(L_F2);
        fk = vld<v4u>(L_KVR + lane * 16); fz = vld<v4u>(L_ZR + (lane >> 4) * 16); cbv = vld<v2u>(L_CB);
        if (rfl(fl) >= 4u * layer + 4u || spent(c)) break;
      }
      sdn = (p >> 3) ^ rfl(cbv.x); c.tag = rfl(cbv.y);
      permit(c, s0 + 8);
      const bool hd = (lane & 16) != 0;
      const v4u zero = {0u, 0u, 0u, 0u};
      const bf16x8 p0 = asb(v4u{pq[0], pq[1], pq[2], pq[3]}), p1 = asb(v4u{pq[4], pq[5], pq[6], pq[7]});
      f32x16 num = {}, den = {};
      den = mfma(asb(hd ? zero : fz), p0, den); den = mfma(asb(hd ? fz : zero), p1, den);
      num = mfma(asb(hd ? zero : fk), p0, num); num = mfma(asb(hd ? fk : zero), p1, num);
      const float i0 = __builtin_amdgcn_rcpf(den[0] + 1e-6f), i1 = __builtin_amdgcn_rcpf(den[8] + 1e-6f);
      const int os = a_act(WS_O, tbq, sdn) + fr(2 * m);
      st16(rs, v4u{pk2(pair(num, 0) * i0), pk2(pair(num, 1) * i0), pk2(pair(num, 2) * i0), pk2(pair(num, 3) * i0)}, lane * 16, os);
      st16(rs, v4u{pk2(pair(num, 4) * i1), pk2(pair(num, 5) * i1), pk2(pair(num, 6) * i1), pk2(pair(num, 7) * i1)}, lane * 16 + 1024, os);
      aM = unpack(R); PIN(aM);
      int oo = a_act(WS_O, tbw, sdn) + kh * 4096, xs = a_act(WS_X1, tbw, sdn) + fr(2 * cb), ss = a_stat(WS_ST1, tbw, sdn) + cb * 256;
      SPIN(oo); SPIN(xs); SPIN(ss);
      DRAIN();
      set_flag(c, a_flag(WS_FO, tbq, sdn) + m * 4);
      wait_flags(c, a_flag(WS_FO, tbw, sdn), kh ? 0xccccull : 0x3333ull);
#pragma unroll
      for (int i = 0; i < AW; ++i) B[i] = ld16(rs, lane * 16 + (i & 3) * 1024, oo + (i >> 2) * 16384);
      SCHED();
      auto hook = [&](int i, int hf) __attribute__((always_inline)) {
        const int n = i + AW;
        if (hf == 0) return;
        if (n < 16) B[n] = ld16(rs, lane * 16 + (n & 3) * 1024, oo + (n >> 2) * 16384);
        if (n == 16) permit(c, s0 + 12);
      };
      wide_steps<16, 0, 16>(c, d, B, aM, aO, kh, hook);
      finish_wide<16>(c, d, B, aM, aO, R, 2 * layer + 1);
      publish_x(c, rs, R, xs, ss);
      vst<v4u>(L_KVS + w * 2048 + lane * 16, v4u{R[0], R[1], R[2], R[3]});
      vst<v4u>(L_KVS + w * 2048 + 1024 + lane * 16, v4u{R[4], R[5], R[6], R[7]});
    }
    unsigned pf;
    f32x16 aM = {}, aO = {};
    Wide<4> d;
    unsigned sd;
    int fd, hd0, fl2, xs2, ss2; unsigned long long mk;
    auto ldh = [&](int n) __attribute__((always_inline)) {
      if (n < 32) B[n % 16] = ld16(rs, lane * 16 + (n & 3) * 1024, hd0 + (((n >> 2) & 4) | (((n >> 2) + ROT(m)) & 3)) * 16384);
      else if (n < 40) B[n % 16] = ld16(rs, lane * 16 + (n & 3) * 1024, hd0 + (n >> 2) * 16384);
      else B[n % 16] = ld16(rs, lane * 16 + (n & 3) * 1024, hd0 + 10 * 16384 - kh * 1024);
    };
    {
      FRESH();
      f32x16 G0 = {}, U0 = {}, G1 = {}, U1 = {};
      Pipe q;
      const unsigned s0 = c.s;
      pair_steps<false, -PD, 0, 64, false>(c, q, B, G0, U0, NoHook());
      const int xo = a_act(WS_X1, tbq, sdn);
      int xa = xo + fr(4 * ROT(m)), xb = xo + fr(4 * ((ROT(m) + 1) & 7));
      SPIN(xa); SPIN(xb);
      DRAIN();
      set_flag(c, a_flag(WS_FX1, tbw, sdn) + cb * 4);
      wait_flags(c, a_flag(WS_FX1, tbq, sdn), 0xffffull);
      Stats sv;
#pragma unroll
      for (int i = 0; i < 8; ++i) B[i] = ld16(rs, lane * 16 + (i & 3) * 1024, i < 4 ? xa : xb);
      SCHED();
      float a2; unsigned gm;
      v4u cg, cu;
      unsigned hp[8];
      const int ho = a_hid(tbq, sdn) + fr(2 * GM(m)), fo = a_flag(WS_FH, tbq, sdn) + GM(m) * 4;
      float hv;
      fd = a_flag(WS_FH, tbw, sdn); mk = kh ? 0xccccull : 0x3333ull; hd0 = a_hid(tbw, sdn) + kh * 4096;
      fl2 = fo + 128;
      auto piece = [&](int e, const f32x16& G, const f32x16& U) __attribute__((always_inline)) {
        float g;
        if (e & 1) asm volatile("v_max_f32 %1, 0, %2\n\tv_mul_f32 %0, %3, %4\n\tv_mul_f32 %1, %1, %0\n\tv_cvt_pk_bf16_f32 %0, %5, %1" : "=&v"(hp[e >> 1]), "=&v"(g) : "v"(G[e]), "v"(U[e]), "v"(a2), "v"(hv));
        else asm volatile("v_max_f32 %1, 0, %2\n\tv_mul_f32 %0, %3, %4\n\tv_mul_f32 %0, %1, %0" : "=&v"(hv), "=&v"(g) : "v"(G[e]), "v"(U[e]), "v"(a2));
      };
      auto tail = [&](const f32x16& G, const f32x16& U) __attribute__((always_inline)) {
#pragma unroll
        for (int j = 0; j < 8; ++j) {
          const f32x2 g = {fmaxf(G[2 * j], 0.f), fmaxf(G[2 * j + 1], 0.f)};
          hp[j] = pk2(g * (pair(U, j) * a2));
        }
      };
      auto store_h = [&](int r, int half) __attribute__((always_inline)) { st16(rs, v4u{hp[4 * half], hp[4 * half + 1], hp[4 * half + 2], hp[4 * half + 3]}, lane * 16 + half * 1024, ho + r * 65536); };
      auto correct = [&](f32x16& G, f32x16& U) __attribute__((always_inline)) {
        const bf16x8 f = asb(v4u{gm, gm, gm, gm});
        G = mfma(asb(cg), f, G); U = mfma(asb(cu), f, U);
      };
      auto shadow = [&](int i, int hf, int r, const f32x16& G, const f32x16& U) __attribute__((always_inline)) {
        constexpr int at[16] = {3, 5, 7, 9, 12, 13, 14, 15, 17, 19, 21, 23, 25, 28, 29, 30};
#pragma unroll
        for (int e = 0; e < 16; ++e) if (2 * i + hf == at[e]) piece(e, G, U);
        if (2 * i + hf == 31) store_h(r, 0);
        if (2 * i + hf == 33) store_h(r, 1);
        if (hf == 0 && i == 25) { if (r == 0) DRAIN(); else wait_vm<8>(); set_flag(c, fo + r * 64); }
        if (hf == 1) cs_hook<25, true>(c, i, 3 + r, cg, cu);
        if (r == 1) {
          if (hf == 0 && i == 8) pf = poll_issue(c, fd, mk);
          if (hf == 0 && i == 17) poll_done<2>(c, pf, fd, mk);
          if (hf == 1 && i >= 17) ldh(i - 17);
          if (hf == 0 && i == 30) pf = poll_issue(c, fd, mk << 16);
        }
      };
      pair_steps<false, 0, 32, 64, false>(c, q, B, G0, U0, [&](int i, int hf) __attribute__((always_inline)) {
        const int n = i + AH;
        stats_hook(c, sv, 2 * i + hf, rs, a_stat(WS_ST1, tbq, sdn), a2, gm);
        if (hf == 0) return;
        if (n < 32) B[n] = ld16(rs, lane * 16 + (n & 3) * 1024, xo + fr(4 * (((n >> 2) + ROT(m)) & 7)));
        if (n == 32) permit(c, s0 + (GM(m) < 11 ? 23 : 19));
        if (i == 26) { a2 = fmul1(a2, a2); PIN(a2); }
        cs_hook<25, true>(c, i, 2, cg, cu);
      });
      correct(G0, U0);
      pair_steps<false, 32, 64, 96, false>(c, q, B, G1, U1, [&](int i, int hf) __attribute__((always_inline)) { shadow(i, hf, 0, G0, U0); });
      correct(G1, U1);
      pf = ~c.tag;
      if (sopaque(GM(m)) >= 11) {
        tail(G1, U1);
        store_h(1, 0); store_h(1, 1);
        DRAIN();
        set_flag(c, fo + 64);
        wait_flags(c, fd, mk);
      }
      asm volatile("" : "=v"(G0), "=v"(U0));
      if (sopaque(GM(m)) < 11) {
        G0 = f32x16{}; U0 = f32x16{};
        pair_steps<false, 64, 96, 96, false>(c, q, B, G0, U0, [&](int i, int hf) __attribute__((always_inline)) { shadow(i, hf, 1, G1, U1); });
        correct(G0, U0);
      }
      if (sopaque(GM(m)) >= 11) {
#pragma unroll
        for (int i = 0; i < 15; ++i) ldh(i);
      }
      sd = c.s;
      const v4u r0 = vld<v4u>(L_KVS + w * 2048 + lane * 16), r1 = vld<v4u>(L_KVS + w * 2048 + 1024 + lane * 16);
      wide_steps<43, -4, 0>(c, d, B, aM, aO, kh, NoHook());
      if (sopaque(GM(m)) < 11) {
        tail(G0, U0);
        store_h(2, 0); store_h(2, 1);
      }
      R[0] = r0.x; R[1] = r0.y; R[2] = r0.z; R[3] = r0.w; R[4] = r1.x; R[5] = r1.y; R[6] = r1.z; R[7] = r1.w;
      aM = unpack(R);
    }
    {
      unsigned pf2;
      auto hook = [&](int i, int hf) __attribute__((always_inline)) {
        if (hf == 0) {
          if (i == 7) { wait_vm<7>(); set_flag(c, fl2); }
          if (i == 13) pf2 = poll_issue(c, fd, 0x7ffull << 32);
          return;
        }
        if (i == 1) poll_done<5>(c, pf, fd, mk << 16);
        if (i + 15 < 32) ldh(i + 15);
        if (i == 20) poll_done<4>(c, pf2, fd, 0x7ffull << 32);
        if (i >= 20 && i <= 23) { ldh(2 * i - 8); ldh(2 * i - 7); }
        if (i >= 24 && i <= 26) ldh(i + 16);
        if (i == 16) permit(c, sd + 11 + 8);
        if (i == 34) { xs2 = a_act(WS_X2, tbw, sdn) + fr(2 * cb); ss2 = a_stat(WS_ST2, tbw, sdn) + cb * 256; SPIN(xs2); SPIN(ss2); }
      };
      wide_steps<43, 0, 43>(c, d, B, aM, aO, kh, hook);
      finish_wide<43>(c, d, B, aM, aO, R, 2 * layer + 2);
    }
#pragma unroll
    for (int i = 0; i < 32; ++i) asm volatile("" : "=v"(B[i]));
    asm volatile("" : "=v"(aK), "=v"(aV), "=v"(la), "=v"(mf), "=v"(ck), "=v"(cv));
    asm volatile("" : "=v"(ar[0]), "=v"(ar[1]), "=v"(ar[2]), "=v"(ar[3]));
    if (layer == 0) {
      publish_x(c, rs, R, xs2, ss2);
      FRESH();
      aK = f32x16{}; aV = f32x16{};
      Pipe q;
      const unsigned s0 = c.s;
      pair_steps<true, -PD, 0, 32, false>(c, q, B, aK, aV, NoHook());
      const int xo = a_act(WS_X2, tbq, sdn);
      int xa = xo + fr(4 * ROT(m)), xb = xo + fr(4 * ((ROT(m) + 1) & 7));
      SPIN(xa); SPIN(xb);
      DRAIN();
      set_flag(c, a_flag(WS_FX2, tbw, sdn) + cb * 4);
      wait_flags(c, a_flag(WS_FX2, tbq, sdn), 0xffffull);
      Stats sv;
#pragma unroll
      for (int i = 0; i < 8; ++i) B[i] = ld16(rs, lane * 16 + (i & 3) * 1024, i < 4 ? xa : xb);
      SCHED();
      auto hook = [&](int i, int hf) __attribute__((always_inline)) {
        const int n = i + AH;
        stats_hook(c, sv, 2 * i + hf, rs, a_stat(WS_ST2, tbq, sdn), la, mf);
        if (hf == 0) {
          if (i == 25) vst<float>(L_SA + w * 128 + t32 * 4, la);
          if (i == 26) { ar[0] = vld<f32x4>(L_SA + w * 128 + 4 * h * 4); ar[1] = vld<f32x4>(L_SA + w * 128 + (8 + 4 * h) * 4); }
          if (i == 27) { ar[2] = vld<f32x4>(L_SA + w * 128 + (16 + 4 * h) * 4); ar[3] = vld<f32x4>(L_SA + w * 128 + (24 + 4 * h) * 4); }
          return;
        }
        if (n < 32) B[n] = ld16(rs, lane * 16 + (n & 3) * 1024, xo + fr(4 * (((n >> 2) + ROT(m)) & 7)));
        if (n == 32) permit(c, s0 + 14);
        cs_hook<26, true>(c, i, 0, ck, cv);
      };
      pair_steps<true, 0, 32, 32, false>(c, q, B, aK, aV, hook);
    }
  }
  {
    layer = 0;
    FRESH();
    const rsrc_t ro = __builtin_amdgcn_make_buffer_rsrc((void*)P.out, 0, 2048 * 1024, 0x00020000);
#pragma unroll
    for (int g = 0; g < 2; ++g) {
      const auto r0 = __builtin_amdgcn_permlane32_swap(R[4 * g], R[4 * g + 2], false, false), r1 = __builtin_amdgcn_permlane32_swap(R[4 * g + 1], R[4 * g + 3], false, false);
      __builtin_amdgcn_raw_buffer_store_b128(v4u{r0[0], r1[0], r0[1], r1[1]}, ro, (tbw * 32 + t32) * 1024 + cb * 64 + g * 32 + h * 16, 0, 0);
    }
  }
  if (b == 0 && w == 0 && lane == 0) *(GAS unsigned*)(P.ws + WS_EPOCH) = c.tag;
}
}

extern "C" void kernel_launch(void* const* d_in, const int* in_sizes, int n_in, void* d_out, int out_size, void* d_ws,
                              size_t ws_size, hipStream_t stream) {
  Params P;
  P.x = (const char*)d_in[0]; P.wqkv = (const char*)d_in[1]; P.wo = (const char*)d_in[2];
  P.wg = (const char*)d_in[4]; P.wu = (const char*)d_in[5]; P.wd = (const char*)d_in[6];
  P.out = (char*)d_out; P.ws = (char*)d_ws;
  (void)hipFuncSetAttribute((const void*)fused_fwd, hipFuncAttributeMaxDynamicSharedMemorySize, LDS_BYTES);
  fused_fwd<<<256, 512, LDS_BYTES, stream>>>(P);
}
```
